```python
import torch

def _softmax(x, axis=-1):
    return torch.softmax(x, dim=axis)
def _gelu(x):
    return torch.nn.functional.gelu(x, approximate="tanh")
def _silu(x):
    return torch.nn.functional.silu(x)
def _sigmoid(x):
    return torch.sigmoid(x)
def _layernorm(x, axis=-1, eps=1e-5):
    m = x.mean(dim=axis, keepdim=True)
    v = x.var(dim=axis, keepdim=True, unbiased=False)
    return (x - m) / torch.sqrt(v + eps)
def _scatter_add(tgt, idx, src, axis=0):
    return tgt.clone().index_add_(axis, idx, src)
def _segment_sum(src, seg, n):
    out = torch.zeros((n,) + src.shape[1:], dtype=src.dtype)
    return out.index_add_(0, seg, src)
def _pad(x, pw):
    flat = tuple(v for pair in reversed(pw) for v in pair)
    return torch.nn.functional.pad(x, flat)

kernel_name = 'gated_mix__sliding_local_dilated__moe_arch_mod__prompt'

def setup_inputs():

    _w = (lambda a: (lambda t: t.to(torch.bfloat16) if t.is_floating_point() else t)(torch.as_tensor(a)))
    def _A_setup():
        B, S, D = 1, 2048, 640
        return {
            "x": _w((lambda *s: torch.randn(*s, dtype=torch.bfloat16) * 0.05)(B, S, D)),
            "wqkv": _w((lambda *s: torch.randn(*s, dtype=torch.bfloat16) * 0.8)(D, 3 * D)),
            "wo": _w((lambda *s: torch.randn(*s, dtype=torch.bfloat16) * 0.05)(D, D)),
        }
    _A_in = _A_setup()

    def _B_setup():
        B, S, D, F, C = 1, 2048, 640, 1728, 2
        return {
            "x": _w((lambda *s: torch.randn(*s, dtype=torch.bfloat16) * 0.05)(B, S, D)),
            "w_r": _w((lambda *s: torch.randn(*s, dtype=torch.bfloat16) * 1.6)(D)),
            "w_gate": _w((lambda *s: torch.randn(*s, dtype=torch.bfloat16) * 0.4)(D, F)),
            "w_up": _w((lambda *s: torch.randn(*s, dtype=torch.bfloat16) * 0.4)(D, F)),
            "w_down": _w((lambda *s: torch.randn(*s, dtype=torch.bfloat16) * 0.4)(F, D)),
        }
    _B_in = _B_setup()

    return {
        "A_x": _A_in["x"],
        "A_wqkv": _A_in["wqkv"],
        "A_wo": _A_in["wo"],
        "B_x": _B_in["x"],
        "B_w_r": _B_in["w_r"],
        "B_w_gate": _B_in["w_gate"],
        "B_w_up": _B_in["w_up"],
        "B_w_down": _B_in["w_down"],
        "gate_w": _w((lambda *s: torch.randn(*s, dtype=torch.bfloat16) * 0.05)(640) * 16.0),
    }

def reference(A_x, A_wqkv, A_wo, B_x, B_w_r, B_w_gate, B_w_up, B_w_down, gate_w):
    def _A_ref(x, wqkv, wo):
        B, S, D = x.shape
        H = 32; Dh = D // H
        W, dil = 8, 2
        qkv = (lambda x, *d: x.permute(*d))((x @ wqkv).reshape(B, S, 3, H, Dh), 2, 0, 3, 1, 4)
        q, k, v = qkv[0], qkv[1], qkv[2]
        scale = Dh ** -0.5
        pos = torch.arange(S)
        off = torch.arange(W) * dil
        idx = pos[:, None] - off[None, :]
        mask = idx >= 0
        idx_c = torch.where(mask, idx, 0).reshape(-1)
        kg = k[:, :, idx_c, :].reshape(B, H, S, W, Dh)
        vg = v[:, :, idx_c, :].reshape(B, H, S, W, Dh)
        logits = torch.einsum("bhsd,bhswd->bhsw", q, kg) * scale
        logits = torch.where(mask[None, None, :, :], logits, -1e9)
        attn = _softmax(logits, axis=-1)
        o = torch.einsum("bhsw,bhswd->bhsd", attn, vg)
        return (lambda x, *d: x.permute(*d))(o, 0, 2, 1, 3).reshape(B, S, D) @ wo

    def _B_ref(x, w_r, w_gate, w_up, w_down):
        B, S, D = x.shape
        C = 2
        r = x @ w_r
        idx = torch.argsort(-r, axis=-1)[:, :C]
        idx_e = (lambda x, n, axis: torch.repeat_interleave(x, n, dim=axis))(idx.reshape(B, C, 1), D, axis=2)
        x_sel = (lambda x, i, axis: torch.take_along_dim(x, i, dim=axis))(x, idx_e, axis=1)
        g = _silu(x_sel @ w_gate)
        u = x_sel @ w_up
        y = (g * u) @ w_down
        r_sel = _sigmoid((lambda x, i, axis: torch.take_along_dim(x, i, dim=axis))(r, idx, axis=1))
        y = y * r_sel.reshape(B, C, 1)
        b_off = (torch.arange(B) * S).reshape(B, 1)
        flat_idx = (idx + b_off).reshape(B * C)
        zero = (x * 0.0).reshape(B * S, D)
        upd = _scatter_add(zero, flat_idx, y.reshape(B * C, D), axis=0)
        return x + upd.reshape(B, S, D)

    x = A_x
    g = _sigmoid((x * gate_w).sum(-1, keepdims=True))
    a = _A_ref(x, A_wqkv, A_wo)
    b = _B_ref(x, B_w_r, B_w_gate, B_w_up, B_w_down)
    return g * a + (1 - g) * b
```

```cpp
#include <hip/hip_runtime.h>
#include <stdint.h>

typedef unsigned short u16;
typedef unsigned long long u64;
typedef __attribute__((ext_vector_type(8))) __bf16 bf16x8;
typedef __attribute__((ext_vector_type(2))) __bf16 bf16x2;
typedef __attribute__((ext_vector_type(4))) short s16x4;
typedef __attribute__((ext_vector_type(4))) float f32x4;
typedef __attribute__((ext_vector_type(2))) float f32x2;
typedef __attribute__((ext_vector_type(4))) unsigned u32x4;
typedef __attribute__((ext_vector_type(2))) unsigned u32x2;
typedef __attribute__((ext_vector_type(8))) unsigned u32x8;
typedef __attribute__((ext_vector_type(16))) unsigned u32x16;
#define GAS __attribute__((address_space(1)))
#define LAS __attribute__((address_space(3)))
typedef GAS unsigned gu32;

#define S_LEN 2048
#define DM 640
#define NQKV 1920
#define FF 1728

#define WS_O 0
#define WS_O_B 2703360
#define WS_H WS_O_B
#define INB 10240
#define WS_H_B (220 * INB)
#define WS_C (WS_H + WS_H_B)
#define C_K 0
#define C_H 56320
#define C_R 84480
#define C_F (C_R + 256)
#define C_A (C_F + 23552)
#define C_E (C_A + 32768)
#define C_B (C_E + 256)
static_assert(C_E == 141056, "(used in the assembly)");
#define WS_YP (WS_C + C_B)
#define TAG_STEP 0x9E3779B9u
#define SENT 0xFFFFFFFFu
#define SPIN_CAP (1u << 18)
#define OOB 0x7FFF0000
#define RLX_AGENT __ATOMIC_RELAXED, __HIP_MEMORY_SCOPE_AGENT

__device__ __forceinline__ float bflo(unsigned u) { return __uint_as_float(u << 16); }
__device__ __forceinline__ float bfhi(unsigned u) { return __uint_as_float(u & 0xFFFF0000u); }
__device__ __forceinline__ unsigned pk2(float a, float b) {
  f32x2 v = {a, b};
  return __builtin_bit_cast(unsigned, __builtin_convertvector(v, bf16x2));
}
__device__ __forceinline__ float rbf(float f) { return bflo(pk2(f, f)); }
__device__ __forceinline__ unsigned perm_lo(unsigned hi_src, unsigned lo_src) {
  return __builtin_amdgcn_perm(hi_src, lo_src, 0x05040100u);
}
__device__ __forceinline__ unsigned perm_hi(unsigned hi_src, unsigned lo_src) {
  return __builtin_amdgcn_perm(hi_src, lo_src, 0x07060302u);
}
__device__ __forceinline__ f32x4 mfma16(u32x4 a, u32x4 b, f32x4 c) {
  return __builtin_amdgcn_mfma_f32_16x16x32_bf16(__builtin_bit_cast(bf16x8, a), __builtin_bit_cast(bf16x8, b), c, 0, 0, 0);
}
__device__ __forceinline__ u32x2 tr_read(const char* p) {
  return __builtin_bit_cast(u32x2, __builtin_amdgcn_ds_read_tr16_b64_v4i16((LAS s16x4*)(p)));
}
#define BARRIER() asm volatile("s_barrier" ::: "memory")
#define WAITV(n) asm volatile("s_waitcnt vmcnt(" #n ")" ::: "memory")
#define WAITV_(n) asm volatile("s_waitcnt vmcnt(%0)" ::"i"(n) : "memory")
#define WAITL0() do { asm volatile("" ::: "memory"); __builtin_amdgcn_s_waitcnt(0xC07F); asm volatile("" ::: "memory"); } while (0)

__device__ __forceinline__ void top2_mrg(unsigned& k1, unsigned& k2, unsigned b1, unsigned b2) {
  k2 = max(max(k2, b2), min(k1, b1));
  k1 = max(k1, b1);
}
template <int CTRL>
__device__ __forceinline__ void top2_dpp(unsigned& k1, unsigned& k2) {
  const unsigned b1 = (unsigned)__builtin_amdgcn_update_dpp(0, (int)k1, CTRL, 0xF, 0xF, true);
  const unsigned b2 = (unsigned)__builtin_amdgcn_update_dpp(0, (int)k2, CTRL, 0xF, 0xF, true);
  top2_mrg(k1, k2, b1, b2);
}
__device__ __forceinline__ void top2_all(unsigned& k1, unsigned& k2) {
  top2_dpp<0xB1>(k1, k2);
  top2_dpp<0x4E>(k1, k2);
  top2_dpp<0x141>(k1, k2);
  top2_dpp<0x140>(k1, k2);
  {
    auto a = __builtin_amdgcn_permlane16_swap(k1, k1, false, false);
    auto c = __builtin_amdgcn_permlane16_swap(k2, k2, false, false);
    k1 = a[0];
    k2 = c[0];
    top2_mrg(k1, k2, a[1], c[1]);
  }
  {
    auto a = __builtin_amdgcn_permlane32_swap(k1, k1, false, false);
    auto c = __builtin_amdgcn_permlane32_swap(k2, k2, false, false);
    k1 = a[0];
    k2 = c[0];
    top2_mrg(k1, k2, a[1], c[1]);
  }
}
__device__ __forceinline__ void touch_line(const char* p, unsigned lds_off) {
  asm volatile("s_mov_b32 m0, %1\n\ts_nop 0\n\tglobal_load_lds_dword %0, off" ::"v"(p), "s"(lds_off) : "memory");
}
__device__ __forceinline__ void dma16(unsigned voff, const void* sbase, unsigned lds_off) {
  asm volatile("s_mov_b32 m0, %2\n\ts_nop 0\n\tglobal_load_lds_dwordx4 %0, %1" ::"v"(voff), "s"(sbase), "s"(lds_off) : "memory");
}
__device__ __forceinline__ void dma16_sc1(unsigned voff, const void* sbase, unsigned lds_off) {
  asm volatile("s_mov_b32 m0, %2\n\ts_nop 0\n\tglobal_load_lds_dwordx4 %0, %1 sc1" ::"v"(voff), "s"(sbase), "s"(lds_off) : "memory");
}
__device__ __forceinline__ void bdma16(unsigned voff, u32x4 srd, unsigned soff, unsigned lds_off) {
  asm volatile("s_mov_b32 m0, %3\n\ts_nop 0\n\tbuffer_load_dwordx4 %0, %1, %2 offen lds" ::"v"(voff), "s"(srd), "s"(soff), "s"(lds_off) : "memory");
}
__device__ __forceinline__ u32x4 make_srd(const void* p, unsigned bytes) {
  return (u32x4){(unsigned)(uintptr_t)p, (unsigned)((uintptr_t)p >> 32) & 0xFFFFu, bytes, 0x00020000u};
}
template <class T> __device__ __forceinline__ T* uni(T* p) {
  const unsigned lo = __builtin_amdgcn_readfirstlane((unsigned)(uintptr_t)p), hi = __builtin_amdgcn_readfirstlane((unsigned)((uintptr_t)p >> 32));
  return (T*)(((u64)hi << 32) | lo);
}
#define RSRC(p, bytes) __builtin_amdgcn_make_buffer_rsrc((void*)(p), 0, bytes, 0x00020000)
__device__ __forceinline__ float key_val(unsigned key) {
  unsigned u = key >> 16;
  u ^= (u & 0x8000u) ? 0x8000u : 0xFFFFu;
  return bflo(u);
}
__device__ __forceinline__ float fmx(float a, float b) { return __builtin_elementwise_maximum(a, b); }
#define SB() __builtin_amdgcn_sched_barrier(0)
__device__ __forceinline__ float dot2(unsigned a, unsigned b, float c) {
  return __builtin_amdgcn_fdot2_f32_bf16(__builtin_bit_cast(bf16x2, a), __builtin_bit_cast(bf16x2, b), c, false);
}
template <int CTRL>
__device__ __forceinline__ float dpp_add(float v) {
  return v + __int_as_float(__builtin_amdgcn_update_dpp(0, __float_as_int(v), CTRL, 0xF, 0xF, true));
}
__device__ __forceinline__ float row_sum(float v) {
  v = dpp_add<0xB1>(v);
  v = dpp_add<0x4E>(v);
  v = dpp_add<0x141>(v);
  return dpp_add<0x140>(v);
}
__device__ __forceinline__ unsigned max4(u32x4 v) { return max(max(v[0], v[1]), max(v[2], v[3])); }
template <int N> struct IC { static constexpr int value = N; };
#define PIN(v) asm volatile("" : "+v"(v))
#define PINS(v) do { v = __builtin_amdgcn_readfirstlane(v); asm volatile("" : "+s"(v)); } while (0)
#define AI __attribute__((always_inline))

#define SLOT_B 36864
#define BUF(q) ((((q) + 1) % 3) * SLOT_B)
#define L_WO 73728
#define RS 168
#define TS (16 * RS)
#define SO (104 * RS)
#define L_TAB (L_WO + SLOT_B)
#define L_EX 53248
#define L_XT 61440
#define L_MISC 155648
#define L_SMP L_MISC
#define L_Z (L_MISC + 1024)
#define L_FL (L_Z + 8)
#define L_Y (L_Z + 64)
#define L_SM2 (L_MISC + 2048)
#define L_TOUCH (L_MISC + 3072)
#define L_TOTAL (L_MISC + 3584)
#define CODE_LINES 247
#define POS_OF(hl) ((int)((0xBA9F73E62D51C408ull >> ((hl) * 4)) & 15))
#define HL_OF(pos) ((int)((0xC963FED0B852A741ull >> ((pos) * 4)) & 15))

__global__ __launch_bounds__(512, 2) void kf(const u16*, const u16*, const u16*, const u16*, const u16*, const u16*, const u16*, const u16*, char*, u16*) {
  __shared__ __attribute__((aligned(16))) char smem[L_TOTAL];
  typedef const __attribute__((address_space(4))) u64* karg_t;
  const karg_t kp0 = (karg_t)__builtin_amdgcn_kernarg_segment_ptr();
  u64 pc;
  asm volatile("s_load_dword s100, %1, 0x0\n\ts_load_dword s101, %1, 0x40\n\ts_getpc_b64 %0" : "=s"(pc) : "s"(kp0) : "s100", "s101");
  const u16 *x, *wqkv, *wo, *w_r, *gate_w, *w_gate, *w_up, *w_down;
  char* ws;
  u16* out;
  auto args = [&]() AI {
    SB();
    karg_t kp = kp0;
    asm volatile("" : "+s"(kp));
    x = (const u16*)kp[0]; wqkv = (const u16*)kp[1]; wo = (const u16*)kp[2]; w_r = (const u16*)kp[3]; gate_w = (const u16*)kp[4];
    w_gate = (const u16*)kp[5]; w_up = (const u16*)kp[6]; w_down = (const u16*)kp[7]; ws = (char*)kp[8]; out = (u16*)kp[9];
  };
  const int tid = threadIdx.x, b = blockIdx.x;
  {
    const int ln = ((b >> 3) & 3) + 4 * tid;
    if (ln < CODE_LINES)
      touch_line((const char*)((pc & ~255ull) + ln * 128), (unsigned)(uintptr_t)(LAS char*)smem + L_TOUCH);
  }
  unsigned tag;
  auto get_tag = [&]() AI {
    asm volatile("s_load_dword %0, %1, 0x0\n\ts_waitcnt lgkmcnt(0)" : "=s"(tag) : "s"(ws + WS_C + C_E) : "memory");
    tag += TAG_STEP;
  };
  const int lane = tid & 63;
  const int wave = __builtin_amdgcn_readfirstlane(tid >> 6);
  const int xcd = b & 7, jj = b >> 3;
  const int c15 = lane & 15, g4 = lane >> 4, q4 = c15 >> 2, p4 = lane & 3;
  const int nb = 28 - (xcd & 1);
  const int q_ = (xcd & 1) * 28 + jj;
  const int par = (xcd >> 1) & 1, hf = xcd >> 2;
  const int gi = (q_ * 205) >> 10, c = q_ - gi * 5;
  const int rg = par * 11 + gi, cg = hf * 5 + c, gb = rg * 10 + cg;
  const int tb = 96 * gi;
  const unsigned lds0 = (unsigned)(uintptr_t)(LAS char*)smem;
#define rc_ RSRC(ws + WS_C, C_B)

  if (__builtin_expect(jj >= nb, 0)) {
    const int f = (jj - nb) * 8 + xcd;
    args();
    if (f > 27) {
      const int mi = f < 32 ? f - 28 : 4 + ((f - 32) >> 1);
      const __amdgpu_buffer_rsrc_t r = RSRC(ws + WS_O, WS_O_B);
      const u32x4 sent = {SENT, SENT, SENT, SENT};
      const int l3 = lane < 8 ? lane * 16 + 3072 : OOB, l4 = lane < 48 ? lane * 16 + 3072 : OOB, l0 = lane < 40 ? lane * 16 : OOB;
#pragma unroll 1
      for (int k = mi * 66 + wave; k < mi * 66 + 66; k += 8) {
        const int k12 = (k * 1366) >> 14, so = (k + k12 * 4) * 3840 + 640;
#pragma unroll
        for (int j = 0; j < 3; ++j) __builtin_amdgcn_raw_buffer_store_b128(sent, r, lane * 16 + j * 1024, so, 16);
        __builtin_amdgcn_raw_buffer_store_b128(sent, r, l3, so, 16);
      }
      get_tag();
      const int m2 = tid < 176 ? C_F + 1024 + (tid >> 3) * 1024 + mi * 128 + (tid & 7) * 16 : OOB;
      if (wave < 3) __builtin_amdgcn_raw_buffer_store_b128((u32x4){~tag, ~tag, ~tag, ~tag}, rc_, m2, 0, 16);
#pragma unroll 1
      for (int k = mi * 22 + 7 - wave; k < mi * 22 + 22; k += 8) {
        const int so = ((k >> 2) * 16 + 12 + (k & 3)) * 3840;
#pragma unroll
        for (int j = 0; j < 3; ++j) {
          __builtin_amdgcn_raw_buffer_store_b128(sent, r, lane * 16 + j * 1024, so, 16);
          __builtin_amdgcn_raw_buffer_store_b128(sent, r, l0, so - (j + 1) * 4 * 3840, 16);
        }
        __builtin_amdgcn_raw_buffer_store_b128(sent, r, l4, so, 16);
        if (k < mi * 22 + 8) {
          WAITV(7);
          BARRIER();
          if (wave == 0) __builtin_amdgcn_raw_buffer_store_b128((u32x4){tag, tag, tag, tag}, rc_, lane < 8 ? C_F + mi * 128 + lane * 16 : lane == 8 ? C_A + b * 128 : OOB, 0, 16);
        }
      }
      WAITV(0);
      BARRIER();
      if (wave < 3) __builtin_amdgcn_raw_buffer_store_b128((u32x4){tag, tag, tag, tag}, rc_, m2, 0, 16);
      return;
    }
    if (f == 27) {
      get_tag();
      u32x2 kv[3];
      int ko[3];
      ko[0] = (tid >> 2) * 256 + (tid & 2) * 32 + (tid & 1) * 16;
      ko[1] = tid < 368 ? ko[0] + 128 * 256 : OOB;
      ko[2] = tid < 176 ? ((tid >> 3) * 10 + ((tid >> 1) & 3)) * 256 + 128 + (tid & 1) * 16 : OOB;
      kv[0] = kv[1] = kv[2] = (u32x2){0u, 0u};
#pragma unroll 1
      for (unsigned spins = 0; spins < SPIN_CAP; ++spins) {
        asm volatile("" ::: "memory");
        u32x4 t_[3];
#pragma unroll
        for (int q = 0; q < 3; ++q) t_[q] = __builtin_amdgcn_raw_buffer_load_b128(rc_, ko[q], C_K, 16);
        bool ok = true;
#pragma unroll
        for (int q = 0; q < 3; ++q)
          if (ko[q] != OOB) {
            if ((t_[q][1] == tag) & (t_[q][3] == tag)) {
              kv[q] = (u32x2){t_[q][0], t_[q][2]};
              ko[q] = OOB;
            } else {
              ok = false;
            }
          }
        if (__all(ok)) break;
        __builtin_amdgcn_s_sleep(4);
      }
      unsigned k1 = max(kv[0][0], kv[0][1]), k2 = min(kv[0][0], kv[0][1]);
      top2_mrg(k1, k2, max(kv[1][0], kv[1][1]), min(kv[1][0], kv[1][1]));
      top2_mrg(k1, k2, max(kv[2][0], kv[2][1]), min(kv[2][0], kv[2][1]));
      top2_all(k1, k2);
      unsigned* lk = (unsigned*)smem;
      if (lane == 0) *(u32x2*)(lk + wave * 2) = (u32x2){k1, k2};
      WAITL0();
      BARRIER();
      k1 = 0u;
      k2 = 0u;
#pragma unroll
      for (int q = 0; q < 4; ++q) {
        const u32x4 a_ = *(const u32x4*)(lk + q * 4);
        top2_mrg(k1, k2, a_[0], a_[1]);
        top2_mrg(k1, k2, a_[2], a_[3]);
      }
      if (tid < 8) __builtin_amdgcn_raw_buffer_store_b128((u32x4){k1, tag, k2, tag}, rc_, C_R + tid * 16, 0, 16);
      int ao = (tid < 256 && tid != b) ? C_A + tid * 128 : OOB;
#pragma unroll 1
      for (unsigned spins = 0; spins < SPIN_CAP; ++spins) {
        asm volatile("" ::: "memory");
        if (__builtin_amdgcn_raw_buffer_load_b32(rc_, ao, 0, 16) == tag) ao = OOB;
        if (__all(ao == OOB)) break;
        __builtin_amdgcn_s_sleep(16);
      }
      BARRIER();
      if (tid == 0) __hip_atomic_store((gu32*)(ws + WS_C + C_E), tag, RLX_AGENT);
      return;
    }
    float* red = (float*)(smem + 81920);
    u16* hs = (u16*)(smem + 90112);
    volatile LAS unsigned* cnt = (volatile LAS unsigned*)(LAS char*)(smem + 90624);
    if (tid < 3) cnt[tid] = 0u;
    WAITL0();
    BARRIER();
    auto await = [&](int i, unsigned n) AI {
#pragma unroll 1
      for (unsigned spins = 0; spins < (SPIN_CAP << 8); ++spins) {
        if (__builtin_amdgcn_readfirstlane(cnt[i]) >= n) break;
        __builtin_amdgcn_s_sleep(1);
      }
      asm volatile("" ::: "memory");
    };
    auto arrive = [&](int i) AI {
      if (lane == 0) __hip_atomic_fetch_add((LAS unsigned*)cnt + i, 1u, __ATOMIC_RELAXED, __HIP_MEMORY_SCOPE_WORKGROUP);
    };
    if (wave == 7) {
      get_tag();
      await(2, 7);
      const u32x4 rsd = make_srd(w_down, DM * FF * 2);
      const int kr = lane >> 3;
      int vd = kr * (DM * 2) + (((lane & 7) ^ (kr & 2)) * 16);
#pragma unroll 1
      for (int o = 0; o < 8; ++o) {
#pragma unroll
        for (int j = 0; j < 10; ++j)
          asm volatile("s_mov_b32 m0, %3\n\ts_nop 0\n\tbuffer_load_dwordx4 %0, %1, %2 offen nt lds" ::"v"(vd), "s"(rsd), "s"((f * 64 + o * 8) * (DM * 2) + j * 128),
                       "s"(lds0 + (o * 10 + j) * 1024)
                       : "memory");
        vd ^= 64;
      }
      WAITV(0);
    } else {
      u32x4 r1[3][8];
      {
        const char* pa = (const char*)(c15 < 8 ? w_gate : w_up) + f * 128 + (c15 & 7) * 16 + (wave * 96 + g4 * 8) * (FF * 2);
        const GAS char* pp[12];
#pragma unroll
        for (int j = 0; j < 12; ++j) {
          pp[j] = (const GAS char*)pa + ((j >> 2) * 32 + (j & 3) * 2) * (FF * 2) + FF;
          PIN(pp[j]);
        }
        SB();
#pragma unroll
        for (int j = 0; j < 24; ++j)
          if (j < 16 || wave < 6) r1[j >> 3][j & 7] = __builtin_nontemporal_load((const GAS u32x4*)(pp[j >> 1] + ((j & 1) ? FF : -FF)));
      }
      SB();
      arrive(2);
      get_tag();
      auto transp = [&](u32x4(&w)[8]) AI {
        u32x4 t[8];
#pragma unroll
        for (int d = 0; d < 4; ++d)
#pragma unroll
          for (int p = 0; p < 4; ++p) {
            t[2 * d][p] = perm_lo(w[2 * p + 1][d], w[2 * p][d]);
            t[2 * d + 1][p] = perm_hi(w[2 * p + 1][d], w[2 * p][d]);
          }
#pragma unroll
        for (int j = 0; j < 8; ++j) {
          w[j] = t[j];
          PIN(w[j]);
        }
      };
      transp(r1[0]);
      transp(r1[1]);
      if (wave < 6) transp(r1[2]);
      if (wave == 0) {
        u32x4 v;
#pragma unroll 1
        for (unsigned spins = 0; spins < SPIN_CAP; ++spins) {
          asm volatile("" ::: "memory");
          v = __builtin_amdgcn_raw_buffer_load_b128(rc_, C_R, 0, 16);
          if (__all((v[1] == tag) & (v[3] == tag))) break;
          __builtin_amdgcn_s_sleep(2);
        }
#pragma unroll
        for (int t = 0; t < 2; ++t) {
          const u16* xr = x + (2047 - (int)(__builtin_amdgcn_readfirstlane(v[2 * t]) & 2047u)) * DM;
          dma16(lane * 16, xr, lds0 + 92160 + t * 1296);
          if (lane < 16) dma16(lane * 16, xr + 512, lds0 + 92160 + t * 1296 + 1024);
        }
        WAITV(0);
        cnt[0] = 1u;
      }
      await(0, 1);
      u32x4 xf[3];
#pragma unroll
      for (int k = 0; k < 3; ++k) xf[k] = *(const u32x4*)(smem + 92160 + (c15 & 1) * 1296 + ((wave * 3 + (k < 2 || wave < 6 ? k : 0)) * 32 + g4 * 8) * 2);
      f32x4 acc[8];
#pragma unroll
      for (int ff = 0; ff < 8; ++ff) acc[ff] = (f32x4){0.f, 0.f, 0.f, 0.f};
#pragma unroll
      for (int k = 0; k < 3; ++k)
        if (k < 2 || wave < 6) {
#pragma unroll
          for (int j = 0; j < 8; ++j) acc[j] = mfma16(xf[k], r1[k][j], acc[j]);
        }
      f32x4 pv;
#pragma unroll
      for (int q = 0; q < 4; ++q) {
        const float lo = (g4 & 1) ? acc[2 + (q >> 1)][q & 1] : acc[q >> 1][q & 1], hi = (g4 & 1) ? acc[6 + (q >> 1)][q & 1] : acc[4 + (q >> 1)][q & 1];
        pv[q] = (g4 & 2) ? hi : lo;
      }
      *(f32x4*)(red + wave * 256 + c15 * 16 + g4 * 4) = pv;
      WAITL0();
      arrive(1);
      if (wave < 2) {
        await(1, 7);
        float gs = 0.f, us = 0.f;
#pragma unroll
        for (int w = 0; w < 7; ++w) {
          gs += red[w * 256 + tid];
          us += red[w * 256 + 128 + tid];
        }
        const float gv = rbf(gs), uv = rbf(us);
        const float hv = rbf(gv * __builtin_amdgcn_rcpf(1.0f + __expf(-gv))) * uv;
        hs[(tid & 1) * 64 + (tid >> 1)] = (u16)pk2(hv, hv);
      }
    }
    int da[4];
#pragma unroll
    for (int t = 0; t < 4; ++t) {
      da[t] = (g4 * 10 + wave * 2) * 1024 + q4 * 128 + ((t ^ (q4 >> 1) ^ ((g4 & 1) << 1)) * 32) + p4 * 8;
      PIN(da[t]);
    }
    WAITL0();
    BARRIER();
    if (wave < 5) {
      const u32x4 h0 = *(const u32x4*)(hs + (c15 >> 3) * 64 + g4 * 8), h1 = *(const u32x4*)(hs + (c15 >> 3) * 64 + 32 + g4 * 8);
      const __amdgpu_buffer_rsrc_t ryp = RSRC(ws + WS_YP, 27 * 10240);
#pragma unroll
      for (int e = 0; e < 2; ++e) {
        f32x4 a[4];
#pragma unroll
        for (int t = 0; t < 4; ++t) {
          const u32x2 w0 = tr_read(smem + da[t] + e * 1024), w1 = tr_read(smem + da[t] + e * 1024 + 512);
          const u32x2 w2 = tr_read(smem + da[t] + e * 1024 + 40960), w3 = tr_read(smem + da[t] + e * 1024 + 41472);
          a[t] = mfma16((u32x4){w0[0], w0[1], w1[0], w1[1]}, h0, (f32x4){0.f, 0.f, 0.f, 0.f});
          a[t] = mfma16((u32x4){w2[0], w2[1], w3[0], w3[1]}, h1, a[t]);
        }
        float y[2];
#pragma unroll
        for (int q = 0; q < 2; ++q) {
          float s_[4];
#pragma unroll
          for (int t = 0; t < 4; ++t) s_[t] = (lane & 1) ? a[t][2 + q] : a[t][q];
          y[q] = (lane & 4) ? ((lane & 2) ? s_[3] : s_[2]) : ((lane & 2) ? s_[1] : s_[0]);
        }
        __builtin_amdgcn_raw_buffer_store_b128((u32x4){__float_as_uint(y[0]), tag, __float_as_uint(y[1]), tag}, ryp, lane * 16 + (f * 10 + wave * 2 + e) * 1024, 0, 16);
        asm volatile("s_nop 1");
        SB();
      }
    } else if (tid == 320) {
      __hip_atomic_store((gu32*)(ws + WS_C + C_A + b * 128), tag, RLX_AGENT);
    }
    return;
  }

  u32x16 p0, p1, p2, p3, r0;
  unsigned sc[16];
#define UNI(x) __builtin_amdgcn_readfirstlane(x)
  const int kr = lane >> 3, hw = wave & 3;
  const int mt = wave < 4 ? wave + 1 : wave < 6 ? 0 : 5;

  if (__builtin_expect(wave >= 4, 1)) {
    asm volatile("" : "=v"(p0), "=v"(p1), "=v"(p2), "=v"(p3), "=v"(r0));
    const int sw16 = cg * 128 + (((lane & 7) ^ ((kr & 2) | ((hw & 1) << 2))) * 16);
#pragma unroll
    for (int v = 0; v < 3; ++v) {
      r0[v] = ((tb + (hw >> 1) * 48 + v * 16 + (hw & 1) * 8 + kr) * 2 + par) * (DM * 2) + (((lane & 7) ^ (((hw & 1) * 4 + g4) & 7)) * 16) + (v == 2 ? 3072 : 0);
      r0[3 + v] = (hw * 8 + kr) * (NQKV * 2) + sw16 + v * 256;
      r0[6 + v] = r0[3 + v] + 32 * (NQKV * 2);
    }
    int kc = rg * 3 - ((rg * 3 * 205) >> 11) * 10;
    args();
    asm volatile("s_mov_b32 m0, %9\n\ts_nop 0\n\tbuffer_load_dwordx4 %0, %5, %7 offen lds\n\tbuffer_load_dwordx4 %1, %5, %7 offen offset:1024 lds\n\t"
                 "buffer_load_dwordx4 %2, %5, %7 offen offset:2048 lds\n\tbuffer_load_dwordx4 %3, %6, %8 offen offset:3072 lds\n\t"
                 "s_add_u32 m0, m0, 8192\n\ts_nop 0\n\tbuffer_load_dwordx4 %4, %6, %8 offen lds" ::"v"(r0[3]),
                 "v"(r0[4]), "v"(r0[5]), "v"(r0[0]), "v"(r0[2]), "s"(make_srd(wqkv, DM * NQKV * 2)), "s"(make_srd((const char*)x - 3072, S_LEN * DM * 2 + 3072)),
                 "s"(kc * (64 * NQKV * 2)), "s"(kc * 128), "s"(BUF(0) + hw * 9216)
                 : "memory", "scc");
    SB();
    r0[9] = lane < 16 ? (gb * 4 + hw) * 1024 + lane * 64 : OOB;
    tag = 0;
    sc[2] = kc;
    sc[4] = sc[5] = 0;
  } else {
    const bool wjob = wave < 2 || (wave == 2 && cg < 4);
    const int tj = tb + (wave * 10 + cg) * 4, tj0 = min(tj, 1020);
    u32x4 xs[5], wv[4];
    int jo0 = lane * 16, jo1 = g4 * 2560 + c15 * 16, jo2 = c15 * 16;
    PIN(jo0); PIN(jo1); PIN(jo2);
    args();
    if (wjob) {
      const u16* xr = x + (2 * tj0 + par) * DM;
      asm volatile("global_load_dwordx4 %0, %9, %12\n\tglobal_load_dwordx4 %1, %9, %12 offset:2560\n\t"
                   "global_load_dwordx4 %2, %9, %13\n\tglobal_load_dwordx4 %3, %9, %13 offset:2560\n\t"
                   "global_load_dwordx4 %4, %10, %12 offset:1024\n\tglobal_load_dwordx4 %5, %9, %14\n\t"
                   "global_load_dwordx4 %6, %11, %14 offset:1024\n\tglobal_load_dwordx4 %7, %9, %15\n\tglobal_load_dwordx4 %8, %11, %15 offset:1024"
                   : "=&v"(xs[0]), "=&v"(xs[1]), "=&v"(xs[2]), "=&v"(xs[3]), "=&v"(xs[4]), "=&v"(wv[0]), "=&v"(wv[1]), "=&v"(wv[2]), "=&v"(wv[3])
                   : "v"(jo0), "v"(jo1), "v"(jo2), "s"(xr), "s"(xr + 4 * DM), "s"(w_r), "s"(gate_w)
                   : "memory");
    }
    const int rh = wave >> 1, ch = wave & 1;
    {
      const int e0 = c15 - 4 * g4;
#pragma unroll
      for (int i = 0; i < 4; ++i) {
        p0[i] = e0 - i <= -9 ? 0u : 0xFF800000u;
        p0[4 + i] = (e0 - i >= 0 && e0 - i <= 7) ? 0u : 0xFF800000u;
      }
      p0[8] = (e0 <= -9 ? 0xFFFFu : 0u) | (e0 - 1 <= -9 ? 0xFFFF0000u : 0u);
      p0[9] = (e0 - 2 <= -9 ? 0xFFFFu : 0u) | (e0 - 3 <= -9 ? 0xFFFF0000u : 0u);
      p0[10] = ~p0[8];
      p0[11] = ~p0[9];
      p0[12] = (g4 >> 1) * 61440 + (lane & 31) * 16;
      p0[13] = g4 * 3840 + c15 * 8 + 512;
      p0[14] = lane < 32 ? lane * 16 : OOB;
      p0[15] = g4 == 2 ? c15 * 8 + 512 : OOB;
      const int sw = (q4 >> 1) | ((g4 & 1) << 1), ho = (rg * 2 + hf) * 16;
      const unsigned aq = c15 * RS + g4 * 8, akh = c15 * RS + 32, av = 2 * SO - TS + (4 * g4 + q4) * RS + (p4 < 2 ? p4 * 8 : 32);
      unsigned xa[4];
#pragma unroll
      for (int f = 0; f < 4; ++f) {
        p2[f] = L_WO + (8 * g4 + q4) * 128 + ((f ^ sw) * 32) + p4 * 8;
        xa[f] = L_XT + c15 * 128 + ((((f * 2 + (g4 >> 1)) ^ (c15 >> 1)) & 7) * 16) + (g4 & 1) * 8;
      }
      if (wave == 3) {
        u32x4* tp = (u32x4*)(smem + L_TAB + lane * 16);
#pragma unroll
        for (int k = 0; k < 4; ++k) tp[k * 64] = (u32x4){p0[4 * k], p0[4 * k + 1], p0[4 * k + 2], p0[4 * k + 3]};
        const int s = ((c ? 0x2FDFCFC9u : 0x20D0C0C9u) >> (c15 * 2)) & 3, m = ((c ? 0x3064205315420455ull : 0x0064205312420155ull) >> (c15 * 4)) & 7;
        const bool nv = ((c == 0 ? 0x0040u : c == 4 ? 0x0800u : 0x0330u) >> c15) & 1;
        tp[4 * 64] = (u32x4){aq, akh, g4 == 0 ? akh : L_Z, av};
        tp[5 * 64] = (u32x4){g4 == 0 ? 1u : 0u, (unsigned)(m * TS + s * 40), nv ? ~0u : (unsigned)((ho + POS_OF(3 * c + s)) * 3840 + m * 640), 0u};
        tp[6 * 64] = (u32x4){p2[0], p2[1], p2[2], p2[3]};
        tp[7 * 64] = (u32x4){xa[0], xa[1], xa[2], xa[3]};
        const int row = lane >> 2, sw0 = cg * 128 + (((lane & 7) ^ (kr & 2)) * 16), j = lane >> 1, w = j / 10;
        tp[8 * 64] = (u32x4){(unsigned)(L_XT + row * 128 + ((p4 ^ (row >> 1)) * 16)), (unsigned)(((tb + row) * 2 + par) * (DM * 2) + cg * 128 + p4 * 16),
                             (unsigned)(L_SMP + c15 * 8), (unsigned)(L_EX + lane * 16)};
        tp[9 * 64] = (u32x4){(unsigned)((kr + (kr & 4)) * (DM * 2) + sw0), (unsigned)(((kr & 3) + (kr >> 2) * 320 + 16) * (DM * 2) + sw0), 0u,
                             (unsigned)(lane < 48 ? C_K + (rg * 10 + j - w * 10) * 256 + w * 64 + 32 + (lane & 1) * 16 : lane < 56 ? C_F + (lane - 48) * 128 : C_R)};
        const int hi = lane >> 5, g = kr & 3, r8 = lane & 7;
        const unsigned g2 = gi ? gb * INB + 6144 + hi * 1024 + lane * 16 : OOB;
        const unsigned g3 = hi ? OOB : g >= 4 - c ? gb * INB + 9216 + lane * 16 : gi ? (gb + 1) * INB + 6144 + lane * 16 : OOB;
        tp[10 * 64] = (u32x4){(unsigned)(C_H + (rg * 10 + min(c15, 9) - (gi && (g4 & 1) ? 10 : 0)) * 128), 0u, (unsigned)(C_F + 1024 + rg * 1024 + r8 * 128), (unsigned)(gb * INB + lane * 16)};
        tp[11 * 64] = (u32x4){(gi && hi && c < 4 && g >= 3 - c) ? (gb - 9) * INB + 4096 + (g * 16 + 8 + r8) * 16 : g2, g2, g3,
                              (unsigned)(g4 < 4 - c ? c15 * RS + 8 * c + 128 + 8 * g4 : (48 + c15) * RS + 8 * (g4 - 4 + c))};
        tp[12 * 64] = (u32x4){(unsigned)(SO + (r8 - 8) * RS + 8 * c + 8 * (hi * 12 + g)), (unsigned)(SO + (r8 - 8) * RS + 8 * c + 8 * (hi * 4 + 4 + g)),
                              (unsigned)(hi ? 160 : g >= 4 - c ? SO + (40 + r8) * RS + 8 * (g - 4 + c) : SO + (r8 - 8) * RS + 8 * c + 128 + 8 * g), g3 == OOB ? 0u : ~0u};
        if (lane < 16) *(unsigned*)(smem + L_Z + lane * 4) = 0u;
      }
      SB();
      const int swz = (c15 >> 1) & 7;
      asm volatile("" : "=v"(r0), "=v"(p3), "=v"(p1));
#pragma unroll
      for (int h = 0; h < 2; ++h) {
        const int tl = ch ? 3 - h : h, tba = mt * TS + 40 + 40 * h;
        p1[4 * h] = aq + tba;
        p1[4 * h + 1] = akh + tba;
        p1[4 * h + 2] = g4 == 0 ? akh + tba : L_Z;
        p1[4 * h + 3] = av + tba;
        sc[4 + h] = (ho + (c < 4 ? c + 4 * h : 9 + h)) * 3840 + mt * 640;
#pragma unroll
        for (int q = 0; q < 3; ++q) {
          r0[8 + 2 * q + h] = BUF(q) + (rh * 2 + (c15 >> 3)) * 9216 + (c15 & 7) * 128 + (((4 * h + g4) ^ swz) * 16);
          const unsigned wa = BUF(q) + g4 * 9216 + q4 * 128 + ((tl ^ sw) * 32) + p4 * 8;
          if (q < 2) p2[12 + 2 * q + h] = wa;
          else p3[h] = wa;
        }
        p1[8 + h] = (rh * 48 + c15) * RS + 8 * c + 8 * (tl * 4 + g4);
      }
#pragma unroll
      for (int f = 0; f < 4; ++f) p2[4 + f] = xa[f] + mt * 2048;
      p2[8] = L_XT + (mt * 16 + kr) * 128 + (((lane & 7) ^ (kr >> 1)) * 16);
      p2[9] = ((tb + mt * 16 + kr) * 2 + par) * (DM * 2) + cg * 128 + (lane & 7) * 16;
      p2[10] = L_SMP + (mt * 16 + c15) * 8;
      p2[11] = (p2[8] + 1024) ^ 64;
      const bool tor = c < 4 && g4 >= 3 - c, dn = rh == 1 && c15 >= 8 && gi < 10;
      const int hidx = (g4 * 8 + c15 - 8) * 16;
      p3[2] = (wave == 0 && g4 < 5 - c && c > 0) ? (gb - 1) * INB + lane * 16 : (wave == 3 && tor) ? (gb + 1) * INB + lane * 16 : OOB;
      p3[5] = dn ? (gb + 10) * INB + 6144 + ch * 1536 + hidx : OOB;
      p3[4] = (wave == 1 && tor && c15 >= 8) ? (gb + 1) * INB + 9216 + hidx : p3[5];
      p3[3] = p3[2] != OOB ? p3[2] + 4096 : p3[4];
      p3[12] = p3[13] = p3[14] = p3[15] = 0x3F803F80u;
      PIN(p0); PIN(p1); PIN(p2); PIN(p3); PIN(r0);
      sc[2] = 0;
    }
    int ko = wave == 3 ? C_H + gb * 128 + c15 * 8 : c15 > 1 ? OOB : C_K + gb * 256 + wave * 64 + c15 * 32 + g4 * 8;
    PIN(ko);
    auto rows4 = [&](float (&p)[5]) AI {
      const auto a = __builtin_amdgcn_permlane32_swap(__float_as_uint(p[0]), __float_as_uint(p[2]), false, false);
      const auto c_ = __builtin_amdgcn_permlane32_swap(__float_as_uint(p[1]), __float_as_uint(p[3]), false, false);
      const auto e = __builtin_amdgcn_permlane16_swap(__float_as_uint(__uint_as_float(a[0]) + __uint_as_float(a[1])),
                                                      __float_as_uint(__uint_as_float(c_[0]) + __uint_as_float(c_[1])), false, false);
      return row_sum(__uint_as_float(e[0]) + __uint_as_float(e[1]) + p[4]);
    };
    SB();
    get_tag();
    if (wave == 3) __builtin_amdgcn_raw_buffer_store_b64((u32x2){0u, ~tag}, rc_, ko, 0, 16);
    int rowi = tj < 1024 ? 2047 - (2 * (tj0 + g4) + par) : -1;
    PIN(rowi);
    SB();
    if (wjob) {
      WAITV(0);
#pragma unroll
      for (int i = 0; i < 5; ++i) PIN(xs[i]);
#pragma unroll
      for (int i = 0; i < 4; ++i) PIN(wv[i]);
      float p[5], pg[5];
#pragma unroll
      for (int i = 0; i < 5; ++i) {
        p[i] = pg[i] = 0.f;
#pragma unroll
        for (int q = 0; q < 4; ++q) {
          p[i] = dot2(xs[i][q], wv[i == 4][q], p[i]);
          pg[i] = dot2(xs[i][q], wv[2 + (i == 4)][q], pg[i]);
        }
      }
      const float r_ = rows4(p), g_ = rbf(rows4(pg));
      unsigned u = pk2(r_, r_) & 0xFFFFu;
      u ^= (u & 0x8000u) ? 0xFFFFu : 0x8000u;
      const unsigned key = rowi >= 0 ? (u << 16) | (unsigned)rowi : 0u;
      __builtin_amdgcn_raw_buffer_store_b64((u32x2){c15 ? __float_as_uint(rbf(__builtin_amdgcn_rcpf(1.0f + __expf(-g_)))) : key, tag}, rc_, ko, 0, 16);
      SB();
    }
    WAITL0();
  }
  sc[0] = tag;
  sc[1] = wave;
  sc[3] = hw * 9216;
  sc[6] = 0;
  sc[7] = rg * 122880 + mt * 640;
  sc[8] = sc[7] + ((wave < 4 || (wave & 1)) ? 30720 : 0);
  sc[9] = (gi > 0 ? 8 : 0) | ((0x7801Eu >> (hw * 5 + c)) & 1);
  sc[10] = cg * 128;
  sc[11] = hw * 1024;
  sc[12] = ((hw >> 1) * 6 + (hw & 1)) * 1024;
  sc[13] = par;
  sc[14] = tb;
  sc[15] = mt;
  u32x16 uc;
#pragma unroll
  for (int i = 0; i < 16; ++i) {
    PINS(sc[i]);
    uc[i] = sc[i];
  }
  asm volatile(
    "s_nop 4\n"
    "s_load_dwordx4 s[92:95], s[90:91], 0x0\n"
    "s_load_dwordx2 s[44:45], s[90:91], 0x10\n"
    "s_load_dwordx4 s[96:99], s[90:91], 0x40\n"
    "s_waitcnt lgkmcnt(0)\n"
    "s_cmp_ge_u32 s69, 4\n"
    "s_cbranch_scc1 LOADER%=\n"
    "s_barrier\n"
    "ds_read_b64_tr_b16 v[72:73], v236 offset:1024\n"
    "ds_read_b64_tr_b16 v[74:75], v236 offset:1536\n"
    "ds_read_b128 v[96:99], v184 offset:3072\n"
    "ds_read_b64_tr_b16 v[76:77], v236 offset:2048\n"
    "ds_read_b64_tr_b16 v[78:79], v236 offset:2560\n"
    "ds_read_b64_tr_b16 v[80:81], v236\n"
    "ds_read_b64_tr_b16 v[82:83], v236 offset:512\n"
    "ds_read_b64_tr_b16 v[84:85], v237 offset:1024\n"
    "ds_read_b64_tr_b16 v[86:87], v237 offset:1536\n"
    "ds_read_b64_tr_b16 v[88:89], v237 offset:2048\n"
    "ds_read_b64_tr_b16 v[90:91], v237 offset:2560\n"
    "ds_read_b64_tr_b16 v[92:93], v237\n"
    "ds_read_b64_tr_b16 v[94:95], v237 offset:512\n"
    "ds_read_b128 v[100:103], v184 offset:7168\n"
    "ds_read_b128 v[104:107], v184 offset:8192\n"
    "s_waitcnt lgkmcnt(12)\n"
    "v_mfma_f32_16x16x32_bf16 v[0:3], v[72:75], v[96:99], 0\n"
    "ds_read_b64_tr_b16 v[108:109], v236 offset:5120\n"
    "s_waitcnt lgkmcnt(11)\n"
    "v_mfma_f32_16x16x32_bf16 v[4:7], v[76:79], v[96:99], 0\n"
    "ds_read_b64_tr_b16 v[110:111], v236 offset:5632\n"
    "s_waitcnt lgkmcnt(10)\n"
    "v_mfma_f32_16x16x32_bf16 v[8:11], v[80:83], v[96:99], 0\n"
    "ds_read_b128 v[132:135], v185 offset:3072\n"
    "s_waitcnt lgkmcnt(9)\n"
    "v_mfma_f32_16x16x32_bf16 v[12:15], v[84:87], v[96:99], 0\n"
    "ds_read_b64_tr_b16 v[112:113], v236 offset:6144\n"
    "s_waitcnt lgkmcnt(8)\n"
    "v_mfma_f32_16x16x32_bf16 v[16:19], v[88:91], v[96:99], 0\n"
    "ds_read_b64_tr_b16 v[114:115], v236 offset:6656\n"
    "s_waitcnt lgkmcnt(7)\n"
    "v_mfma_f32_16x16x32_bf16 v[20:23], v[92:95], v[96:99], 0\n"
    "ds_read_b64_tr_b16 v[116:117], v236 offset:4096\n"
    "s_waitcnt lgkmcnt(7)\n"
    "v_mfma_f32_16x16x32_bf16 v[24:27], v[72:75], v[100:103], 0\n"
    "ds_read_b64_tr_b16 v[118:119], v236 offset:4608\n"
    "v_mfma_f32_16x16x32_bf16 v[28:31], v[76:79], v[100:103], 0\n"
    "ds_read_b64_tr_b16 v[120:121], v237 offset:5120\n"
    "v_mfma_f32_16x16x32_bf16 v[32:35], v[80:83], v[100:103], 0\n"
    "ds_read_b64_tr_b16 v[122:123], v237 offset:5632\n"
    "v_mfma_f32_16x16x32_bf16 v[36:39], v[84:87], v[100:103], 0\n"
    "ds_read_b64_tr_b16 v[124:125], v237 offset:6144\n"
    "v_mfma_f32_16x16x32_bf16 v[40:43], v[88:91], v[100:103], 0\n"
    "ds_read_b64_tr_b16 v[126:127], v237 offset:6656\n"
    "v_mfma_f32_16x16x32_bf16 v[44:47], v[92:95], v[100:103], 0\n"
    "ds_read_b64_tr_b16 v[128:129], v237 offset:4096\n"
    "s_waitcnt lgkmcnt(12)\n"
    "v_mfma_f32_16x16x32_bf16 v[48:51], v[72:75], v[104:107], 0\n"
    "ds_read_b64_tr_b16 v[130:131], v237 offset:4608\n"
    "v_mfma_f32_16x16x32_bf16 v[52:55], v[76:79], v[104:107], 0\n"
    "ds_read_b128 v[136:139], v185 offset:7168\n"
    "v_mfma_f32_16x16x32_bf16 v[56:59], v[80:83], v[104:107], 0\n"
    "ds_read_b128 v[140:143], v185 offset:8192\n"
    "v_mfma_f32_16x16x32_bf16 v[60:63], v[84:87], v[104:107], 0\n"
    "s_mov_b32 s48, s96\n"
    "s_and_b32 s49, s97, 65535\n"
    "v_mfma_f32_16x16x32_bf16 v[64:67], v[88:91], v[104:107], 0\n"
    "s_mov_b32 s50, 2703360\n"
    "s_mov_b32 s51, 131072\n"
    "v_mfma_f32_16x16x32_bf16 v[68:71], v[92:95], v[104:107], 0\n"
    "s_add_u32 s52, s96, 2703360\n"
    "s_addc_u32 s53, s97, 0\n"
    "s_waitcnt lgkmcnt(0)\n"
    "s_barrier\n"
    "v_mfma_f32_16x16x32_bf16 v[0:3], v[108:111], v[132:135], v[0:3]\n"
    "ds_read_b64_tr_b16 v[72:73], v238 offset:1024\n"
    "v_mfma_f32_16x16x32_bf16 v[4:7], v[112:115], v[132:135], v[4:7]\n"
    "ds_read_b64_tr_b16 v[74:75], v238 offset:1536\n"
    "v_mfma_f32_16x16x32_bf16 v[8:11], v[116:119], v[132:135], v[8:11]\n"
    "ds_read_b128 v[96:99], v186 offset:3072\n"
    "v_mfma_f32_16x16x32_bf16 v[12:15], v[120:123], v[132:135], v[12:15]\n"
    "ds_read_b64_tr_b16 v[76:77], v238 offset:2048\n"
    "v_mfma_f32_16x16x32_bf16 v[16:19], v[124:127], v[132:135], v[16:19]\n"
    "ds_read_b64_tr_b16 v[78:79], v238 offset:2560\n"
    "v_mfma_f32_16x16x32_bf16 v[20:23], v[128:131], v[132:135], v[20:23]\n"
    "ds_read_b64_tr_b16 v[80:81], v238\n"
    "v_mfma_f32_16x16x32_bf16 v[24:27], v[108:111], v[136:139], v[24:27]\n"
    "ds_read_b64_tr_b16 v[82:83], v238 offset:512\n"
    "v_mfma_f32_16x16x32_bf16 v[28:31], v[112:115], v[136:139], v[28:31]\n"
    "ds_read_b64_tr_b16 v[84:85], v239 offset:1024\n"
    "v_mfma_f32_16x16x32_bf16 v[32:35], v[116:119], v[136:139], v[32:35]\n"
    "ds_read_b64_tr_b16 v[86:87], v239 offset:1536\n"
    "v_mfma_f32_16x16x32_bf16 v[36:39], v[120:123], v[136:139], v[36:39]\n"
    "ds_read_b64_tr_b16 v[88:89], v239 offset:2048\n"
    "v_mfma_f32_16x16x32_bf16 v[40:43], v[124:127], v[136:139], v[40:43]\n"
    "ds_read_b64_tr_b16 v[90:91], v239 offset:2560\n"
    "v_mfma_f32_16x16x32_bf16 v[44:47], v[128:131], v[136:139], v[44:47]\n"
    "ds_read_b64_tr_b16 v[92:93], v239\n"
    "v_mfma_f32_16x16x32_bf16 v[48:51], v[108:111], v[140:143], v[48:51]\n"
    "ds_read_b64_tr_b16 v[94:95], v239 offset:512\n"
    "v_mfma_f32_16x16x32_bf16 v[52:55], v[112:115], v[140:143], v[52:55]\n"
    "ds_read_b128 v[100:103], v186 offset:7168\n"
    "v_mfma_f32_16x16x32_bf16 v[56:59], v[116:119], v[140:143], v[56:59]\n"
    "ds_read_b128 v[104:107], v186 offset:8192\n"
    "v_mfma_f32_16x16x32_bf16 v[60:63], v[120:123], v[140:143], v[60:63]\n"
    "s_and_b32 s53, s53, 65535\n"
    "s_mov_b32 s54, 2252800\n"
    "v_mfma_f32_16x16x32_bf16 v[64:67], v[124:127], v[140:143], v[64:67]\n"
    "s_mov_b32 s55, 131072\n"
    "s_mov_b32 s56, s98\n"
    "v_mfma_f32_16x16x32_bf16 v[68:71], v[128:131], v[140:143], v[68:71]\n"
    "s_and_b32 s57, s99, 65535\n"
    "s_mov_b32 s58, 2621440\n"
    "s_waitcnt lgkmcnt(6)\n"
    "v_mfma_f32_16x16x32_bf16 v[0:3], v[72:75], v[96:99], v[0:3]\n"
    "ds_read_b64_tr_b16 v[108:109], v238 offset:5120\n"
    "v_mfma_f32_16x16x32_bf16 v[4:7], v[76:79], v[96:99], v[4:7]\n"
    "ds_read_b64_tr_b16 v[110:111], v238 offset:5632\n"
    "v_mfma_f32_16x16x32_bf16 v[8:11], v[80:83], v[96:99], v[8:11]\n"
    "ds_read_b128 v[132:135], v187 offset:3072\n"
    "v_mfma_f32_16x16x32_bf16 v[12:15], v[84:87], v[96:99], v[12:15]\n"
    "ds_read_b64_tr_b16 v[112:113], v238 offset:6144\n"
    "s_waitcnt lgkmcnt(4)\n"
    "v_mfma_f32_16x16x32_bf16 v[16:19], v[88:91], v[96:99], v[16:19]\n"
    "ds_read_b64_tr_b16 v[114:115], v238 offset:6656\n"
    "v_mfma_f32_16x16x32_bf16 v[20:23], v[92:95], v[96:99], v[20:23]\n"
    "ds_read_b64_tr_b16 v[116:117], v238 offset:4096\n"
    "v_mfma_f32_16x16x32_bf16 v[24:27], v[72:75], v[100:103], v[24:27]\n"
    "ds_read_b64_tr_b16 v[118:119], v238 offset:4608\n"
    "v_mfma_f32_16x16x32_bf16 v[28:31], v[76:79], v[100:103], v[28:31]\n"
    "ds_read_b64_tr_b16 v[120:121], v239 offset:5120\n"
    "v_mfma_f32_16x16x32_bf16 v[32:35], v[80:83], v[100:103], v[32:35]\n"
    "ds_read_b64_tr_b16 v[122:123], v239 offset:5632\n"
    "v_mfma_f32_16x16x32_bf16 v[36:39], v[84:87], v[100:103], v[36:39]\n"
    "ds_read_b64_tr_b16 v[124:125], v239 offset:6144\n"
    "v_mfma_f32_16x16x32_bf16 v[40:43], v[88:91], v[100:103], v[40:43]\n"
    "ds_read_b64_tr_b16 v[126:127], v239 offset:6656\n"
    "v_mfma_f32_16x16x32_bf16 v[44:47], v[92:95], v[100:103], v[44:47]\n"
    "ds_read_b64_tr_b16 v[128:129], v239 offset:4096\n"
    "v_mfma_f32_16x16x32_bf16 v[48:51], v[72:75], v[104:107], v[48:51]\n"
    "ds_read_b64_tr_b16 v[130:131], v239 offset:4608\n"
    "v_mfma_f32_16x16x32_bf16 v[52:55], v[76:79], v[104:107], v[52:55]\n"
    "ds_read_b128 v[136:139], v187 offset:7168\n"
    "v_mfma_f32_16x16x32_bf16 v[56:59], v[80:83], v[104:107], v[56:59]\n"
    "ds_read_b128 v[140:143], v187 offset:8192\n"
    "v_mfma_f32_16x16x32_bf16 v[60:63], v[84:87], v[104:107], v[60:63]\n"
    "s_mov_b32 s59, 131072\n"
    "s_add_u32 s64, s96, 5097472\n"
    "v_mfma_f32_16x16x32_bf16 v[64:67], v[88:91], v[104:107], v[64:67]\n"
    "s_addc_u32 s65, s97, 0\n"
    "s_and_b32 s65, s65, 65535\n"
    "v_mfma_f32_16x16x32_bf16 v[68:71], v[92:95], v[104:107], v[68:71]\n"
    "s_mov_b32 s66, 276480\n"
    "s_mov_b32 s67, 131072\n"
    "s_waitcnt lgkmcnt(0)\n"
    "s_barrier\n"
    "v_mfma_f32_16x16x32_bf16 v[0:3], v[108:111], v[132:135], v[0:3]\n"
    "ds_read_b64_tr_b16 v[72:73], v240 offset:1024\n"
    "v_mfma_f32_16x16x32_bf16 v[4:7], v[112:115], v[132:135], v[4:7]\n"
    "ds_read_b64_tr_b16 v[74:75], v240 offset:1536\n"
    "v_mfma_f32_16x16x32_bf16 v[8:11], v[116:119], v[132:135], v[8:11]\n"
    "ds_read_b128 v[96:99], v188 offset:3072\n"
    "v_mfma_f32_16x16x32_bf16 v[12:15], v[120:123], v[132:135], v[12:15]\n"
    "ds_read_b64_tr_b16 v[76:77], v240 offset:2048\n"
    "v_mfma_f32_16x16x32_bf16 v[16:19], v[124:127], v[132:135], v[16:19]\n"
    "ds_read_b64_tr_b16 v[78:79], v240 offset:2560\n"
    "v_mfma_f32_16x16x32_bf16 v[20:23], v[128:131], v[132:135], v[20:23]\n"
    "ds_read_b64_tr_b16 v[80:81], v240\n"
    "v_mfma_f32_16x16x32_bf16 v[24:27], v[108:111], v[136:139], v[24:27]\n"
    "ds_read_b64_tr_b16 v[82:83], v240 offset:512\n"
    "v_mfma_f32_16x16x32_bf16 v[28:31], v[112:115], v[136:139], v[28:31]\n"
    "ds_read_b64_tr_b16 v[84:85], v241 offset:1024\n"
    "v_mfma_f32_16x16x32_bf16 v[32:35], v[116:119], v[136:139], v[32:35]\n"
    "ds_read_b64_tr_b16 v[86:87], v241 offset:1536\n"
    "v_mfma_f32_16x16x32_bf16 v[36:39], v[120:123], v[136:139], v[36:39]\n"
    "ds_read_b64_tr_b16 v[88:89], v241 offset:2048\n"
    "v_mfma_f32_16x16x32_bf16 v[40:43], v[124:127], v[136:139], v[40:43]\n"
    "ds_read_b64_tr_b16 v[90:91], v241 offset:2560\n"
    "v_mfma_f32_16x16x32_bf16 v[44:47], v[128:131], v[136:139], v[44:47]\n"
    "ds_read_b64_tr_b16 v[92:93], v241\n"
    "v_mfma_f32_16x16x32_bf16 v[48:51], v[108:111], v[140:143], v[48:51]\n"
    "ds_read_b64_tr_b16 v[94:95], v241 offset:512\n"
    "v_mfma_f32_16x16x32_bf16 v[52:55], v[112:115], v[140:143], v[52:55]\n"
    "ds_read_b128 v[100:103], v188 offset:7168\n"
    "v_mfma_f32_16x16x32_bf16 v[56:59], v[116:119], v[140:143], v[56:59]\n"
    "ds_read_b128 v[104:107], v188 offset:8192\n"
    "v_mfma_f32_16x16x32_bf16 v[60:63], v[120:123], v[140:143], v[60:63]\n"
    "s_lshl_b32 s85, s85, 7\n"
    "s_add_u32 s85, s85, 108288\n"
    "v_mfma_f32_16x16x32_bf16 v[64:67], v[124:127], v[140:143], v[64:67]\n"
    "v_mfma_f32_16x16x32_bf16 v[68:71], v[128:131], v[140:143], v[68:71]\n"
    "s_waitcnt lgkmcnt(6)\n"
    "v_mfma_f32_16x16x32_bf16 v[0:3], v[72:75], v[96:99], v[0:3]\n"
    "ds_read_b64_tr_b16 v[108:109], v240 offset:5120\n"
    "v_mfma_f32_16x16x32_bf16 v[4:7], v[76:79], v[96:99], v[4:7]\n"
    "ds_read_b64_tr_b16 v[110:111], v240 offset:5632\n"
    "v_mfma_f32_16x16x32_bf16 v[8:11], v[80:83], v[96:99], v[8:11]\n"
    "ds_read_b128 v[132:135], v189 offset:3072\n"
    "v_mfma_f32_16x16x32_bf16 v[12:15], v[84:87], v[96:99], v[12:15]\n"
    "ds_read_b64_tr_b16 v[112:113], v240 offset:6144\n"
    "s_waitcnt lgkmcnt(4)\n"
    "v_mfma_f32_16x16x32_bf16 v[16:19], v[88:91], v[96:99], v[16:19]\n"
    "ds_read_b64_tr_b16 v[114:115], v240 offset:6656\n"
    "v_mfma_f32_16x16x32_bf16 v[20:23], v[92:95], v[96:99], v[20:23]\n"
    "ds_read_b64_tr_b16 v[116:117], v240 offset:4096\n"
    "v_mfma_f32_16x16x32_bf16 v[24:27], v[72:75], v[100:103], v[24:27]\n"
    "ds_read_b64_tr_b16 v[118:119], v240 offset:4608\n"
    "v_mfma_f32_16x16x32_bf16 v[28:31], v[76:79], v[100:103], v[28:31]\n"
    "ds_read_b64_tr_b16 v[120:121], v241 offset:5120\n"
    "v_mfma_f32_16x16x32_bf16 v[32:35], v[80:83], v[100:103], v[32:35]\n"
    "ds_read_b64_tr_b16 v[122:123], v241 offset:5632\n"
    "v_mfma_f32_16x16x32_bf16 v[36:39], v[84:87], v[100:103], v[36:39]\n"
    "ds_read_b64_tr_b16 v[124:125], v241 offset:6144\n"
    "v_mfma_f32_16x16x32_bf16 v[40:43], v[88:91], v[100:103], v[40:43]\n"
    "ds_read_b64_tr_b16 v[126:127], v241 offset:6656\n"
    "v_mfma_f32_16x16x32_bf16 v[44:47], v[92:95], v[100:103], v[44:47]\n"
    "ds_read_b64_tr_b16 v[128:129], v241 offset:4096\n"
    "v_mfma_f32_16x16x32_bf16 v[48:51], v[72:75], v[104:107], v[48:51]\n"
    "ds_read_b64_tr_b16 v[130:131], v241 offset:4608\n"
    "v_mfma_f32_16x16x32_bf16 v[52:55], v[76:79], v[104:107], v[52:55]\n"
    "ds_read_b128 v[136:139], v189 offset:7168\n"
    "v_mfma_f32_16x16x32_bf16 v[56:59], v[80:83], v[104:107], v[56:59]\n"
    "ds_read_b128 v[140:143], v189 offset:8192\n"
    "v_mfma_f32_16x16x32_bf16 v[60:63], v[84:87], v[104:107], v[60:63]\n"
    "v_mfma_f32_16x16x32_bf16 v[64:67], v[88:91], v[104:107], v[64:67]\n"
    "v_mfma_f32_16x16x32_bf16 v[68:71], v[92:95], v[104:107], v[68:71]\n"
    "s_waitcnt lgkmcnt(0)\n"
    "s_barrier\n"
    "v_mfma_f32_16x16x32_bf16 v[0:3], v[108:111], v[132:135], v[0:3]\n"
    "ds_read_b64_tr_b16 v[72:73], v236 offset:1024\n"
    "v_mfma_f32_16x16x32_bf16 v[4:7], v[112:115], v[132:135], v[4:7]\n"
    "ds_read_b64_tr_b16 v[74:75], v236 offset:1536\n"
    "v_mfma_f32_16x16x32_bf16 v[8:11], v[116:119], v[132:135], v[8:11]\n"
    "ds_read_b128 v[96:99], v184 offset:3072\n"
    "v_mfma_f32_16x16x32_bf16 v[12:15], v[120:123], v[132:135], v[12:15]\n"
    "ds_read_b64_tr_b16 v[76:77], v236 offset:2048\n"
    "v_mfma_f32_16x16x32_bf16 v[16:19], v[124:127], v[132:135], v[16:19]\n"
    "ds_read_b64_tr_b16 v[78:79], v236 offset:2560\n"
    "v_mfma_f32_16x16x32_bf16 v[20:23], v[128:131], v[132:135], v[20:23]\n"
    "ds_read_b64_tr_b16 v[80:81], v236\n"
    "v_mfma_f32_16x16x32_bf16 v[24:27], v[108:111], v[136:139], v[24:27]\n"
    "ds_read_b64_tr_b16 v[82:83], v236 offset:512\n"
    "v_mfma_f32_16x16x32_bf16 v[28:31], v[112:115], v[136:139], v[28:31]\n"
    "ds_read_b64_tr_b16 v[84:85], v237 offset:1024\n"
    "v_mfma_f32_16x16x32_bf16 v[32:35], v[116:119], v[136:139], v[32:35]\n"
    "ds_read_b64_tr_b16 v[86:87], v237 offset:1536\n"
    "v_mfma_f32_16x16x32_bf16 v[36:39], v[120:123], v[136:139], v[36:39]\n"
    "ds_read_b64_tr_b16 v[88:89], v237 offset:2048\n"
    "v_mfma_f32_16x16x32_bf16 v[40:43], v[124:127], v[136:139], v[40:43]\n"
    "ds_read_b64_tr_b16 v[90:91], v237 offset:2560\n"
    "v_mfma_f32_16x16x32_bf16 v[44:47], v[128:131], v[136:139], v[44:47]\n"
    "ds_read_b64_tr_b16 v[92:93], v237\n"
    "v_mfma_f32_16x16x32_bf16 v[48:51], v[108:111], v[140:143], v[48:51]\n"
    "ds_read_b64_tr_b16 v[94:95], v237 offset:512\n"
    "v_mfma_f32_16x16x32_bf16 v[52:55], v[112:115], v[140:143], v[52:55]\n"
    "ds_read_b128 v[100:103], v184 offset:7168\n"
    "v_mfma_f32_16x16x32_bf16 v[56:59], v[116:119], v[140:143], v[56:59]\n"
    "ds_read_b128 v[104:107], v184 offset:8192\n"
    "v_mfma_f32_16x16x32_bf16 v[60:63], v[120:123], v[140:143], v[60:63]\n"
    "v_mfma_f32_16x16x32_bf16 v[64:67], v[124:127], v[140:143], v[64:67]\n"
    "v_mfma_f32_16x16x32_bf16 v[68:71], v[128:131], v[140:143], v[68:71]\n"
    "s_waitcnt lgkmcnt(6)\n"
    "v_mfma_f32_16x16x32_bf16 v[0:3], v[72:75], v[96:99], v[0:3]\n"
    "ds_read_b64_tr_b16 v[108:109], v236 offset:5120\n"
    "v_mfma_f32_16x16x32_bf16 v[4:7], v[76:79], v[96:99], v[4:7]\n"
    "ds_read_b64_tr_b16 v[110:111], v236 offset:5632\n"
    "v_mfma_f32_16x16x32_bf16 v[8:11], v[80:83], v[96:99], v[8:11]\n"
    "ds_read_b128 v[132:135], v185 offset:3072\n"
    "v_mfma_f32_16x16x32_bf16 v[12:15], v[84:87], v[96:99], v[12:15]\n"
    "ds_read_b64_tr_b16 v[112:113], v236 offset:6144\n"
    "s_waitcnt lgkmcnt(4)\n"
    "v_mfma_f32_16x16x32_bf16 v[16:19], v[88:91], v[96:99], v[16:19]\n"
    "ds_read_b64_tr_b16 v[114:115], v236 offset:6656\n"
    "v_mfma_f32_16x16x32_bf16 v[20:23], v[92:95], v[96:99], v[20:23]\n"
    "ds_read_b64_tr_b16 v[116:117], v236 offset:4096\n"
    "v_mfma_f32_16x16x32_bf16 v[24:27], v[72:75], v[100:103], v[24:27]\n"
    "ds_read_b64_tr_b16 v[118:119], v236 offset:4608\n"
    "v_mfma_f32_16x16x32_bf16 v[28:31], v[76:79], v[100:103], v[28:31]\n"
    "ds_read_b64_tr_b16 v[120:121], v237 offset:5120\n"
    "v_mfma_f32_16x16x32_bf16 v[32:35], v[80:83], v[100:103], v[32:35]\n"
    "ds_read_b64_tr_b16 v[122:123], v237 offset:5632\n"
    "v_mfma_f32_16x16x32_bf16 v[36:39], v[84:87], v[100:103], v[36:39]\n"
    "ds_read_b64_tr_b16 v[124:125], v237 offset:6144\n"
    "v_mfma_f32_16x16x32_bf16 v[40:43], v[88:91], v[100:103], v[40:43]\n"
    "ds_read_b64_tr_b16 v[126:127], v237 offset:6656\n"
    "v_mfma_f32_16x16x32_bf16 v[44:47], v[92:95], v[100:103], v[44:47]\n"
    "ds_read_b64_tr_b16 v[128:129], v237 offset:4096\n"
    "v_mfma_f32_16x16x32_bf16 v[48:51], v[72:75], v[104:107], v[48:51]\n"
    "ds_read_b64_tr_b16 v[130:131], v237 offset:4608\n"
    "v_mfma_f32_16x16x32_bf16 v[52:55], v[76:79], v[104:107], v[52:55]\n"
    "ds_read_b128 v[136:139], v185 offset:7168\n"
    "v_mfma_f32_16x16x32_bf16 v[56:59], v[80:83], v[104:107], v[56:59]\n"
    "ds_read_b128 v[140:143], v185 offset:8192\n"
    "v_mfma_f32_16x16x32_bf16 v[60:63], v[84:87], v[104:107], v[60:63]\n"
    "v_mfma_f32_16x16x32_bf16 v[64:67], v[88:91], v[104:107], v[64:67]\n"
    "v_mfma_f32_16x16x32_bf16 v[68:71], v[92:95], v[104:107], v[68:71]\n"
    "s_waitcnt lgkmcnt(0)\n"
    "s_barrier\n"
    "v_mfma_f32_16x16x32_bf16 v[0:3], v[108:111], v[132:135], v[0:3]\n"
    "ds_read_b64_tr_b16 v[72:73], v238 offset:1024\n"
    "v_mfma_f32_16x16x32_bf16 v[4:7], v[112:115], v[132:135], v[4:7]\n"
    "ds_read_b64_tr_b16 v[74:75], v238 offset:1536\n"
    "v_mfma_f32_16x16x32_bf16 v[8:11], v[116:119], v[132:135], v[8:11]\n"
    "ds_read_b128 v[96:99], v186 offset:3072\n"
    "v_mfma_f32_16x16x32_bf16 v[12:15], v[120:123], v[132:135], v[12:15]\n"
    "ds_read_b64_tr_b16 v[76:77], v238 offset:2048\n"
    "v_mfma_f32_16x16x32_bf16 v[16:19], v[124:127], v[132:135], v[16:19]\n"
    "ds_read_b64_tr_b16 v[78:79], v238 offset:2560\n"
    "v_mfma_f32_16x16x32_bf16 v[20:23], v[128:131], v[132:135], v[20:23]\n"
    "ds_read_b64_tr_b16 v[80:81], v238\n"
    "v_mfma_f32_16x16x32_bf16 v[24:27], v[108:111], v[136:139], v[24:27]\n"
    "ds_read_b64_tr_b16 v[82:83], v238 offset:512\n"
    "v_mfma_f32_16x16x32_bf16 v[28:31], v[112:115], v[136:139], v[28:31]\n"
    "ds_read_b64_tr_b16 v[84:85], v239 offset:1024\n"
    "v_mfma_f32_16x16x32_bf16 v[32:35], v[116:119], v[136:139], v[32:35]\n"
    "ds_read_b64_tr_b16 v[86:87], v239 offset:1536\n"
    "v_mfma_f32_16x16x32_bf16 v[36:39], v[120:123], v[136:139], v[36:39]\n"
    "ds_read_b64_tr_b16 v[88:89], v239 offset:2048\n"
    "v_mfma_f32_16x16x32_bf16 v[40:43], v[124:127], v[136:139], v[40:43]\n"
    "ds_read_b64_tr_b16 v[90:91], v239 offset:2560\n"
    "v_mfma_f32_16x16x32_bf16 v[44:47], v[128:131], v[136:139], v[44:47]\n"
    "ds_read_b64_tr_b16 v[92:93], v239\n"
    "v_mfma_f32_16x16x32_bf16 v[48:51], v[108:111], v[140:143], v[48:51]\n"
    "ds_read_b64_tr_b16 v[94:95], v239 offset:512\n"
    "v_mfma_f32_16x16x32_bf16 v[52:55], v[112:115], v[140:143], v[52:55]\n"
    "ds_read_b128 v[100:103], v186 offset:7168\n"
    "v_mfma_f32_16x16x32_bf16 v[56:59], v[116:119], v[140:143], v[56:59]\n"
    "ds_read_b128 v[104:107], v186 offset:8192\n"
    "v_mfma_f32_16x16x32_bf16 v[60:63], v[120:123], v[140:143], v[60:63]\n"
    "v_mfma_f32_16x16x32_bf16 v[64:67], v[124:127], v[140:143], v[64:67]\n"
    "v_mfma_f32_16x16x32_bf16 v[68:71], v[128:131], v[140:143], v[68:71]\n"
    "s_waitcnt lgkmcnt(6)\n"
    "v_mfma_f32_16x16x32_bf16 v[0:3], v[72:75], v[96:99], v[0:3]\n"
    "ds_read_b64_tr_b16 v[108:109], v238 offset:5120\n"
    "v_mfma_f32_16x16x32_bf16 v[4:7], v[76:79], v[96:99], v[4:7]\n"
    "ds_read_b64_tr_b16 v[110:111], v238 offset:5632\n"
    "v_mfma_f32_16x16x32_bf16 v[8:11], v[80:83], v[96:99], v[8:11]\n"
    "ds_read_b128 v[132:135], v187 offset:3072\n"
    "v_mfma_f32_16x16x32_bf16 v[12:15], v[84:87], v[96:99], v[12:15]\n"
    "ds_read_b64_tr_b16 v[112:113], v238 offset:6144\n"
    "s_waitcnt lgkmcnt(4)\n"
    "v_mfma_f32_16x16x32_bf16 v[16:19], v[88:91], v[96:99], v[16:19]\n"
    "ds_read_b64_tr_b16 v[114:115], v238 offset:6656\n"
    "v_mfma_f32_16x16x32_bf16 v[20:23], v[92:95], v[96:99], v[20:23]\n"
    "ds_read_b64_tr_b16 v[116:117], v238 offset:4096\n"
    "v_mfma_f32_16x16x32_bf16 v[24:27], v[72:75], v[100:103], v[24:27]\n"
    "ds_read_b64_tr_b16 v[118:119], v238 offset:4608\n"
    "v_mfma_f32_16x16x32_bf16 v[28:31], v[76:79], v[100:103], v[28:31]\n"
    "ds_read_b64_tr_b16 v[120:121], v239 offset:5120\n"
    "v_mfma_f32_16x16x32_bf16 v[32:35], v[80:83], v[100:103], v[32:35]\n"
    "ds_read_b64_tr_b16 v[122:123], v239 offset:5632\n"
    "v_mfma_f32_16x16x32_bf16 v[36:39], v[84:87], v[100:103], v[36:39]\n"
    "ds_read_b64_tr_b16 v[124:125], v239 offset:6144\n"
    "v_mfma_f32_16x16x32_bf16 v[40:43], v[88:91], v[100:103], v[40:43]\n"
    "ds_read_b64_tr_b16 v[126:127], v239 offset:6656\n"
    "v_mfma_f32_16x16x32_bf16 v[44:47], v[92:95], v[100:103], v[44:47]\n"
    "ds_read_b64_tr_b16 v[128:129], v239 offset:4096\n"
    "v_mfma_f32_16x16x32_bf16 v[48:51], v[72:75], v[104:107], v[48:51]\n"
    "ds_read_b64_tr_b16 v[130:131], v239 offset:4608\n"
    "v_mfma_f32_16x16x32_bf16 v[52:55], v[76:79], v[104:107], v[52:55]\n"
    "ds_read_b128 v[136:139], v187 offset:7168\n"
    "v_mfma_f32_16x16x32_bf16 v[56:59], v[80:83], v[104:107], v[56:59]\n"
    "ds_read_b128 v[140:143], v187 offset:8192\n"
    "v_mfma_f32_16x16x32_bf16 v[60:63], v[84:87], v[104:107], v[60:63]\n"
    "v_mfma_f32_16x16x32_bf16 v[64:67], v[88:91], v[104:107], v[64:67]\n"
    "v_mfma_f32_16x16x32_bf16 v[68:71], v[92:95], v[104:107], v[68:71]\n"
    "s_waitcnt lgkmcnt(0)\n"
    "s_barrier\n"
    "v_mfma_f32_16x16x32_bf16 v[0:3], v[108:111], v[132:135], v[0:3]\n"
    "ds_read_b64_tr_b16 v[72:73], v240 offset:1024\n"
    "v_mfma_f32_16x16x32_bf16 v[4:7], v[112:115], v[132:135], v[4:7]\n"
    "ds_read_b64_tr_b16 v[74:75], v240 offset:1536\n"
    "v_mfma_f32_16x16x32_bf16 v[8:11], v[116:119], v[132:135], v[8:11]\n"
    "ds_read_b128 v[96:99], v188 offset:3072\n"
    "v_mfma_f32_16x16x32_bf16 v[12:15], v[120:123], v[132:135], v[12:15]\n"
    "ds_read_b64_tr_b16 v[76:77], v240 offset:2048\n"
    "v_mfma_f32_16x16x32_bf16 v[16:19], v[124:127], v[132:135], v[16:19]\n"
    "ds_read_b64_tr_b16 v[78:79], v240 offset:2560\n"
    "v_mfma_f32_16x16x32_bf16 v[20:23], v[128:131], v[132:135], v[20:23]\n"
    "ds_read_b64_tr_b16 v[80:81], v240\n"
    "v_mfma_f32_16x16x32_bf16 v[24:27], v[108:111], v[136:139], v[24:27]\n"
    "ds_read_b64_tr_b16 v[82:83], v240 offset:512\n"
    "v_mfma_f32_16x16x32_bf16 v[28:31], v[112:115], v[136:139], v[28:31]\n"
    "ds_read_b64_tr_b16 v[84:85], v241 offset:1024\n"
    "v_mfma_f32_16x16x32_bf16 v[32:35], v[116:119], v[136:139], v[32:35]\n"
    "ds_read_b64_tr_b16 v[86:87], v241 offset:1536\n"
    "v_mfma_f32_16x16x32_bf16 v[36:39], v[120:123], v[136:139], v[36:39]\n"
    "ds_read_b64_tr_b16 v[88:89], v241 offset:2048\n"
    "v_mfma_f32_16x16x32_bf16 v[40:43], v[124:127], v[136:139], v[40:43]\n"
    "ds_read_b64_tr_b16 v[90:91], v241 offset:2560\n"
    "v_mfma_f32_16x16x32_bf16 v[44:47], v[128:131], v[136:139], v[44:47]\n"
    "ds_read_b64_tr_b16 v[92:93], v241\n"
    "v_mfma_f32_16x16x32_bf16 v[48:51], v[108:111], v[140:143], v[48:51]\n"
    "ds_read_b64_tr_b16 v[94:95], v241 offset:512\n"
    "v_mfma_f32_16x16x32_bf16 v[52:55], v[112:115], v[140:143], v[52:55]\n"
    "ds_read_b128 v[100:103], v188 offset:7168\n"
    "v_mfma_f32_16x16x32_bf16 v[56:59], v[116:119], v[140:143], v[56:59]\n"
    "ds_read_b128 v[104:107], v188 offset:8192\n"
    "v_mfma_f32_16x16x32_bf16 v[60:63], v[120:123], v[140:143], v[60:63]\n"
    "v_mfma_f32_16x16x32_bf16 v[64:67], v[124:127], v[140:143], v[64:67]\n"
    "v_mfma_f32_16x16x32_bf16 v[68:71], v[128:131], v[140:143], v[68:71]\n"
    "s_waitcnt lgkmcnt(6)\n"
    "v_mfma_f32_16x16x32_bf16 v[0:3], v[72:75], v[96:99], v[0:3]\n"
    "ds_read_b64_tr_b16 v[108:109], v240 offset:5120\n"
    "v_mfma_f32_16x16x32_bf16 v[4:7], v[76:79], v[96:99], v[4:7]\n"
    "ds_read_b64_tr_b16 v[110:111], v240 offset:5632\n"
    "v_mfma_f32_16x16x32_bf16 v[8:11], v[80:83], v[96:99], v[8:11]\n"
    "ds_read_b128 v[132:135], v189 offset:3072\n"
    "v_mfma_f32_16x16x32_bf16 v[12:15], v[84:87], v[96:99], v[12:15]\n"
    "ds_read_b64_tr_b16 v[112:113], v240 offset:6144\n"
    "s_waitcnt lgkmcnt(4)\n"
    "v_mfma_f32_16x16x32_bf16 v[16:19], v[88:91], v[96:99], v[16:19]\n"
    "ds_read_b64_tr_b16 v[114:115], v240 offset:6656\n"
    "v_mfma_f32_16x16x32_bf16 v[20:23], v[92:95], v[96:99], v[20:23]\n"
    "ds_read_b64_tr_b16 v[116:117], v240 offset:4096\n"
    "v_mfma_f32_16x16x32_bf16 v[24:27], v[72:75], v[100:103], v[24:27]\n"
    "ds_read_b64_tr_b16 v[118:119], v240 offset:4608\n"
    "v_mfma_f32_16x16x32_bf16 v[28:31], v[76:79], v[100:103], v[28:31]\n"
    "ds_read_b64_tr_b16 v[120:121], v241 offset:5120\n"
    "v_mfma_f32_16x16x32_bf16 v[32:35], v[80:83], v[100:103], v[32:35]\n"
    "ds_read_b64_tr_b16 v[122:123], v241 offset:5632\n"
    "v_mfma_f32_16x16x32_bf16 v[36:39], v[84:87], v[100:103], v[36:39]\n"
    "ds_read_b64_tr_b16 v[124:125], v241 offset:6144\n"
    "v_mfma_f32_16x16x32_bf16 v[40:43], v[88:91], v[100:103], v[40:43]\n"
    "ds_read_b64_tr_b16 v[126:127], v241 offset:6656\n"
    "v_mfma_f32_16x16x32_bf16 v[44:47], v[92:95], v[100:103], v[44:47]\n"
    "ds_read_b64_tr_b16 v[128:129], v241 offset:4096\n"
    "v_mfma_f32_16x16x32_bf16 v[48:51], v[72:75], v[104:107], v[48:51]\n"
    "ds_read_b64_tr_b16 v[130:131], v241 offset:4608\n"
    "v_mfma_f32_16x16x32_bf16 v[52:55], v[76:79], v[104:107], v[52:55]\n"
    "ds_read_b128 v[136:139], v189 offset:7168\n"
    "v_mfma_f32_16x16x32_bf16 v[56:59], v[80:83], v[104:107], v[56:59]\n"
    "ds_read_b128 v[140:143], v189 offset:8192\n"
    "v_mfma_f32_16x16x32_bf16 v[60:63], v[84:87], v[104:107], v[60:63]\n"
    "v_mfma_f32_16x16x32_bf16 v[64:67], v[88:91], v[104:107], v[64:67]\n"
    "v_mfma_f32_16x16x32_bf16 v[68:71], v[92:95], v[104:107], v[68:71]\n"
    "s_waitcnt lgkmcnt(0)\n"
    "s_barrier\n"
    "v_mfma_f32_16x16x32_bf16 v[0:3], v[108:111], v[132:135], v[0:3]\n"
    "ds_read_b64_tr_b16 v[72:73], v236 offset:1024\n"
    "v_mfma_f32_16x16x32_bf16 v[4:7], v[112:115], v[132:135], v[4:7]\n"
    "ds_read_b64_tr_b16 v[74:75], v236 offset:1536\n"
    "v_mfma_f32_16x16x32_bf16 v[8:11], v[116:119], v[132:135], v[8:11]\n"
    "ds_read_b128 v[96:99], v184 offset:3072\n"
    "v_mfma_f32_16x16x32_bf16 v[12:15], v[120:123], v[132:135], v[12:15]\n"
    "ds_read_b64_tr_b16 v[76:77], v236 offset:2048\n"
    "v_mfma_f32_16x16x32_bf16 v[16:19], v[124:127], v[132:135], v[16:19]\n"
    "ds_read_b64_tr_b16 v[78:79], v236 offset:2560\n"
    "v_mfma_f32_16x16x32_bf16 v[20:23], v[128:131], v[132:135], v[20:23]\n"
    "ds_read_b64_tr_b16 v[80:81], v236\n"
    "v_mfma_f32_16x16x32_bf16 v[24:27], v[108:111], v[136:139], v[24:27]\n"
    "ds_read_b64_tr_b16 v[82:83], v236 offset:512\n"
    "v_mfma_f32_16x16x32_bf16 v[28:31], v[112:115], v[136:139], v[28:31]\n"
    "ds_read_b64_tr_b16 v[84:85], v237 offset:1024\n"
    "v_mfma_f32_16x16x32_bf16 v[32:35], v[116:119], v[136:139], v[32:35]\n"
    "ds_read_b64_tr_b16 v[86:87], v237 offset:1536\n"
    "v_mfma_f32_16x16x32_bf16 v[36:39], v[120:123], v[136:139], v[36:39]\n"
    "ds_read_b64_tr_b16 v[88:89], v237 offset:2048\n"
    "v_mfma_f32_16x16x32_bf16 v[40:43], v[124:127], v[136:139], v[40:43]\n"
    "ds_read_b64_tr_b16 v[90:91], v237 offset:2560\n"
    "v_mfma_f32_16x16x32_bf16 v[44:47], v[128:131], v[136:139], v[44:47]\n"
    "ds_read_b64_tr_b16 v[92:93], v237\n"
    "v_mfma_f32_16x16x32_bf16 v[48:51], v[108:111], v[140:143], v[48:51]\n"
    "ds_read_b64_tr_b16 v[94:95], v237 offset:512\n"
    "v_mfma_f32_16x16x32_bf16 v[52:55], v[112:115], v[140:143], v[52:55]\n"
    "ds_read_b128 v[100:103], v184 offset:7168\n"
    "v_mfma_f32_16x16x32_bf16 v[56:59], v[116:119], v[140:143], v[56:59]\n"
    "ds_read_b128 v[104:107], v184 offset:8192\n"
    "v_mfma_f32_16x16x32_bf16 v[60:63], v[120:123], v[140:143], v[60:63]\n"
    "v_mfma_f32_16x16x32_bf16 v[64:67], v[124:127], v[140:143], v[64:67]\n"
    "v_mfma_f32_16x16x32_bf16 v[68:71], v[128:131], v[140:143], v[68:71]\n"
    "s_waitcnt lgkmcnt(6)\n"
    "v_mfma_f32_16x16x32_bf16 v[0:3], v[72:75], v[96:99], v[0:3]\n"
    "ds_read_b64_tr_b16 v[108:109], v236 offset:5120\n"
    "v_mfma_f32_16x16x32_bf16 v[4:7], v[76:79], v[96:99], v[4:7]\n"
    "ds_read_b64_tr_b16 v[110:111], v236 offset:5632\n"
    "v_mfma_f32_16x16x32_bf16 v[8:11], v[80:83], v[96:99], v[8:11]\n"
    "ds_read_b128 v[132:135], v185 offset:3072\n"
    "v_mfma_f32_16x16x32_bf16 v[12:15], v[84:87], v[96:99], v[12:15]\n"
    "ds_read_b64_tr_b16 v[112:113], v236 offset:6144\n"
    "s_waitcnt lgkmcnt(4)\n"
    "v_mfma_f32_16x16x32_bf16 v[16:19], v[88:91], v[96:99], v[16:19]\n"
    "ds_read_b64_tr_b16 v[114:115], v236 offset:6656\n"
    "v_mfma_f32_16x16x32_bf16 v[20:23], v[92:95], v[96:99], v[20:23]\n"
    "ds_read_b64_tr_b16 v[116:117], v236 offset:4096\n"
    "v_mfma_f32_16x16x32_bf16 v[24:27], v[72:75], v[100:103], v[24:27]\n"
    "ds_read_b64_tr_b16 v[118:119], v236 offset:4608\n"
    "v_mfma_f32_16x16x32_bf16 v[28:31], v[76:79], v[100:103], v[28:31]\n"
    "ds_read_b64_tr_b16 v[120:121], v237 offset:5120\n"
    "v_mfma_f32_16x16x32_bf16 v[32:35], v[80:83], v[100:103], v[32:35]\n"
    "ds_read_b64_tr_b16 v[122:123], v237 offset:5632\n"
    "v_mfma_f32_16x16x32_bf16 v[36:39], v[84:87], v[100:103], v[36:39]\n"
    "ds_read_b64_tr_b16 v[124:125], v237 offset:6144\n"
    "v_mfma_f32_16x16x32_bf16 v[40:43], v[88:91], v[100:103], v[40:43]\n"
    "ds_read_b64_tr_b16 v[126:127], v237 offset:6656\n"
    "v_mfma_f32_16x16x32_bf16 v[44:47], v[92:95], v[100:103], v[44:47]\n"
    "ds_read_b64_tr_b16 v[128:129], v237 offset:4096\n"
    "v_mfma_f32_16x16x32_bf16 v[48:51], v[72:75], v[104:107], v[48:51]\n"
    "ds_read_b64_tr_b16 v[130:131], v237 offset:4608\n"
    "v_mfma_f32_16x16x32_bf16 v[52:55], v[76:79], v[104:107], v[52:55]\n"
    "ds_read_b128 v[136:139], v185 offset:7168\n"
    "v_mfma_f32_16x16x32_bf16 v[56:59], v[80:83], v[104:107], v[56:59]\n"
    "ds_read_b128 v[140:143], v185 offset:8192\n"
    "v_mfma_f32_16x16x32_bf16 v[60:63], v[84:87], v[104:107], v[60:63]\n"
    "v_mfma_f32_16x16x32_bf16 v[64:67], v[88:91], v[104:107], v[64:67]\n"
    "v_mfma_f32_16x16x32_bf16 v[68:71], v[92:95], v[104:107], v[68:71]\n"
    "s_waitcnt vmcnt(0)\n"
    "s_waitcnt lgkmcnt(0)\n"
    "s_barrier\n"
    "v_mfma_f32_16x16x32_bf16 v[0:3], v[108:111], v[132:135], v[0:3]\n"
    "ds_read_b64_tr_b16 v[72:73], v238 offset:1024\n"
    "v_mfma_f32_16x16x32_bf16 v[4:7], v[112:115], v[132:135], v[4:7]\n"
    "ds_read_b64_tr_b16 v[74:75], v238 offset:1536\n"
    "v_mfma_f32_16x16x32_bf16 v[8:11], v[116:119], v[132:135], v[8:11]\n"
    "ds_read_b128 v[96:99], v186 offset:3072\n"
    "v_mfma_f32_16x16x32_bf16 v[12:15], v[120:123], v[132:135], v[12:15]\n"
    "ds_read_b64_tr_b16 v[76:77], v238 offset:2048\n"
    "v_mfma_f32_16x16x32_bf16 v[16:19], v[124:127], v[132:135], v[16:19]\n"
    "ds_read_b64_tr_b16 v[78:79], v238 offset:2560\n"
    "v_mfma_f32_16x16x32_bf16 v[20:23], v[128:131], v[132:135], v[20:23]\n"
    "ds_read_b64_tr_b16 v[80:81], v238\n"
    "v_mfma_f32_16x16x32_bf16 v[24:27], v[108:111], v[136:139], v[24:27]\n"
    "ds_read_b64_tr_b16 v[82:83], v238 offset:512\n"
    "v_mfma_f32_16x16x32_bf16 v[28:31], v[112:115], v[136:139], v[28:31]\n"
    "ds_read_b64_tr_b16 v[84:85], v239 offset:1024\n"
    "v_mfma_f32_16x16x32_bf16 v[32:35], v[116:119], v[136:139], v[32:35]\n"
    "ds_read_b64_tr_b16 v[86:87], v239 offset:1536\n"
    "v_mfma_f32_16x16x32_bf16 v[36:39], v[120:123], v[136:139], v[36:39]\n"
    "ds_read_b64_tr_b16 v[88:89], v239 offset:2048\n"
    "v_mfma_f32_16x16x32_bf16 v[40:43], v[124:127], v[136:139], v[40:43]\n"
    "ds_read_b64_tr_b16 v[90:91], v239 offset:2560\n"
    "v_mfma_f32_16x16x32_bf16 v[44:47], v[128:131], v[136:139], v[44:47]\n"
    "ds_read_b64_tr_b16 v[92:93], v239\n"
    "v_mfma_f32_16x16x32_bf16 v[48:51], v[108:111], v[140:143], v[48:51]\n"
    "ds_read_b64_tr_b16 v[94:95], v239 offset:512\n"
    "v_mfma_f32_16x16x32_bf16 v[52:55], v[112:115], v[140:143], v[52:55]\n"
    "ds_read_b128 v[100:103], v186 offset:7168\n"
    "v_mfma_f32_16x16x32_bf16 v[56:59], v[116:119], v[140:143], v[56:59]\n"
    "ds_read_b128 v[104:107], v186 offset:8192\n"
    "v_mfma_f32_16x16x32_bf16 v[60:63], v[120:123], v[140:143], v[60:63]\n"
    "v_mfma_f32_16x16x32_bf16 v[64:67], v[124:127], v[140:143], v[64:67]\n"
    "v_mfma_f32_16x16x32_bf16 v[68:71], v[128:131], v[140:143], v[68:71]\n"
    "s_waitcnt lgkmcnt(6)\n"
    "v_mfma_f32_16x16x32_bf16 v[0:3], v[72:75], v[96:99], v[0:3]\n"
    "ds_read_b64_tr_b16 v[108:109], v238 offset:5120\n"
    "v_mfma_f32_16x16x32_bf16 v[4:7], v[76:79], v[96:99], v[4:7]\n"
    "ds_read_b64_tr_b16 v[110:111], v238 offset:5632\n"
    "v_mfma_f32_16x16x32_bf16 v[8:11], v[80:83], v[96:99], v[8:11]\n"
    "ds_read_b128 v[132:135], v187 offset:3072\n"
    "v_mfma_f32_16x16x32_bf16 v[12:15], v[84:87], v[96:99], v[12:15]\n"
    "ds_read_b64_tr_b16 v[112:113], v238 offset:6144\n"
    "s_waitcnt lgkmcnt(4)\n"
    "v_mfma_f32_16x16x32_bf16 v[16:19], v[88:91], v[96:99], v[16:19]\n"
    "ds_read_b64_tr_b16 v[114:115], v238 offset:6656\n"
    "v_mfma_f32_16x16x32_bf16 v[20:23], v[92:95], v[96:99], v[20:23]\n"
    "ds_read_b64_tr_b16 v[116:117], v238 offset:4096\n"
    "v_mfma_f32_16x16x32_bf16 v[24:27], v[72:75], v[100:103], v[24:27]\n"
    "ds_read_b64_tr_b16 v[118:119], v238 offset:4608\n"
    "v_mfma_f32_16x16x32_bf16 v[28:31], v[76:79], v[100:103], v[28:31]\n"
    "ds_read_b64_tr_b16 v[120:121], v239 offset:5120\n"
    "v_mfma_f32_16x16x32_bf16 v[32:35], v[80:83], v[100:103], v[32:35]\n"
    "ds_read_b64_tr_b16 v[122:123], v239 offset:5632\n"
    "v_mfma_f32_16x16x32_bf16 v[36:39], v[84:87], v[100:103], v[36:39]\n"
    "ds_read_b64_tr_b16 v[124:125], v239 offset:6144\n"
    "v_mfma_f32_16x16x32_bf16 v[40:43], v[88:91], v[100:103], v[40:43]\n"
    "ds_read_b64_tr_b16 v[126:127], v239 offset:6656\n"
    "v_mfma_f32_16x16x32_bf16 v[44:47], v[92:95], v[100:103], v[44:47]\n"
    "ds_read_b64_tr_b16 v[128:129], v239 offset:4096\n"
    "v_mfma_f32_16x16x32_bf16 v[48:51], v[72:75], v[104:107], v[48:51]\n"
    "ds_read_b64_tr_b16 v[130:131], v239 offset:4608\n"
    "v_mfma_f32_16x16x32_bf16 v[52:55], v[76:79], v[104:107], v[52:55]\n"
    "ds_read_b128 v[136:139], v187 offset:7168\n"
    "v_mfma_f32_16x16x32_bf16 v[56:59], v[80:83], v[104:107], v[56:59]\n"
    "ds_read_b128 v[140:143], v187 offset:8192\n"
    "v_mfma_f32_16x16x32_bf16 v[60:63], v[84:87], v[104:107], v[60:63]\n"
    "v_mfma_f32_16x16x32_bf16 v[64:67], v[88:91], v[104:107], v[64:67]\n"
    "v_mfma_f32_16x16x32_bf16 v[68:71], v[92:95], v[104:107], v[68:71]\n"
    "s_waitcnt lgkmcnt(0)\n"
    "s_barrier\n"
    "v_mfma_f32_16x16x32_bf16 v[0:3], v[108:111], v[132:135], v[0:3]\n"
    "ds_read_b64_tr_b16 v[72:73], v240 offset:1024\n"
    "v_mfma_f32_16x16x32_bf16 v[4:7], v[112:115], v[132:135], v[4:7]\n"
    "ds_read_b64_tr_b16 v[74:75], v240 offset:1536\n"
    "v_mfma_f32_16x16x32_bf16 v[8:11], v[116:119], v[132:135], v[8:11]\n"
    "ds_read_b128 v[96:99], v188 offset:3072\n"
    "v_mfma_f32_16x16x32_bf16 v[12:15], v[120:123], v[132:135], v[12:15]\n"
    "ds_read_b64_tr_b16 v[76:77], v240 offset:2048\n"
    "v_mfma_f32_16x16x32_bf16 v[16:19], v[124:127], v[132:135], v[16:19]\n"
    "ds_read_b64_tr_b16 v[78:79], v240 offset:2560\n"
    "v_mfma_f32_16x16x32_bf16 v[20:23], v[128:131], v[132:135], v[20:23]\n"
    "ds_read_b64_tr_b16 v[80:81], v240\n"
    "v_mfma_f32_16x16x32_bf16 v[24:27], v[108:111], v[136:139], v[24:27]\n"
    "ds_read_b64_tr_b16 v[82:83], v240 offset:512\n"
    "v_mfma_f32_16x16x32_bf16 v[28:31], v[112:115], v[136:139], v[28:31]\n"
    "ds_read_b64_tr_b16 v[84:85], v241 offset:1024\n"
    "v_mfma_f32_16x16x32_bf16 v[32:35], v[116:119], v[136:139], v[32:35]\n"
    "ds_read_b64_tr_b16 v[86:87], v241 offset:1536\n"
    "v_mfma_f32_16x16x32_bf16 v[36:39], v[120:123], v[136:139], v[36:39]\n"
    "ds_read_b64_tr_b16 v[88:89], v241 offset:2048\n"
    "v_mfma_f32_16x16x32_bf16 v[40:43], v[124:127], v[136:139], v[40:43]\n"
    "ds_read_b64_tr_b16 v[90:91], v241 offset:2560\n"
    "v_mfma_f32_16x16x32_bf16 v[44:47], v[128:131], v[136:139], v[44:47]\n"
    "ds_read_b64_tr_b16 v[92:93], v241\n"
    "v_mfma_f32_16x16x32_bf16 v[48:51], v[108:111], v[140:143], v[48:51]\n"
    "ds_read_b64_tr_b16 v[94:95], v241 offset:512\n"
    "v_mfma_f32_16x16x32_bf16 v[52:55], v[112:115], v[140:143], v[52:55]\n"
    "ds_read_b128 v[100:103], v188 offset:7168\n"
    "v_mfma_f32_16x16x32_bf16 v[56:59], v[116:119], v[140:143], v[56:59]\n"
    "ds_read_b128 v[104:107], v188 offset:8192\n"
    "v_mfma_f32_16x16x32_bf16 v[60:63], v[120:123], v[140:143], v[60:63]\n"
    "v_mfma_f32_16x16x32_bf16 v[64:67], v[124:127], v[140:143], v[64:67]\n"
    "v_mfma_f32_16x16x32_bf16 v[68:71], v[128:131], v[140:143], v[68:71]\n"
    "s_waitcnt lgkmcnt(6)\n"
    "v_mfma_f32_16x16x32_bf16 v[0:3], v[72:75], v[96:99], v[0:3]\n"
    "ds_read_b64_tr_b16 v[108:109], v240 offset:5120\n"
    "v_mfma_f32_16x16x32_bf16 v[4:7], v[76:79], v[96:99], v[4:7]\n"
    "ds_read_b64_tr_b16 v[110:111], v240 offset:5632\n"
    "v_mfma_f32_16x16x32_bf16 v[8:11], v[80:83], v[96:99], v[8:11]\n"
    "ds_read_b128 v[132:135], v189 offset:3072\n"
    "v_mfma_f32_16x16x32_bf16 v[12:15], v[84:87], v[96:99], v[12:15]\n"
    "ds_read_b64_tr_b16 v[112:113], v240 offset:6144\n"
    "s_waitcnt lgkmcnt(4)\n"
    "v_mfma_f32_16x16x32_bf16 v[16:19], v[88:91], v[96:99], v[16:19]\n"
    "ds_read_b64_tr_b16 v[114:115], v240 offset:6656\n"
    "v_mfma_f32_16x16x32_bf16 v[20:23], v[92:95], v[96:99], v[20:23]\n"
    "ds_read_b64_tr_b16 v[116:117], v240 offset:4096\n"
    "v_mfma_f32_16x16x32_bf16 v[24:27], v[72:75], v[100:103], v[24:27]\n"
    "ds_read_b64_tr_b16 v[118:119], v240 offset:4608\n"
    "v_mfma_f32_16x16x32_bf16 v[28:31], v[76:79], v[100:103], v[28:31]\n"
    "ds_read_b64_tr_b16 v[120:121], v241 offset:5120\n"
    "v_mfma_f32_16x16x32_bf16 v[32:35], v[80:83], v[100:103], v[32:35]\n"
    "ds_read_b64_tr_b16 v[122:123], v241 offset:5632\n"
    "v_mfma_f32_16x16x32_bf16 v[36:39], v[84:87], v[100:103], v[36:39]\n"
    "ds_read_b64_tr_b16 v[124:125], v241 offset:6144\n"
    "v_mfma_f32_16x16x32_bf16 v[40:43], v[88:91], v[100:103], v[40:43]\n"
    "ds_read_b64_tr_b16 v[126:127], v241 offset:6656\n"
    "v_mfma_f32_16x16x32_bf16 v[44:47], v[92:95], v[100:103], v[44:47]\n"
    "ds_read_b64_tr_b16 v[128:129], v241 offset:4096\n"
    "v_mfma_f32_16x16x32_bf16 v[48:51], v[72:75], v[104:107], v[48:51]\n"
    "ds_read_b64_tr_b16 v[130:131], v241 offset:4608\n"
    "v_mfma_f32_16x16x32_bf16 v[52:55], v[76:79], v[104:107], v[52:55]\n"
    "ds_read_b128 v[136:139], v189 offset:7168\n"
    "v_mfma_f32_16x16x32_bf16 v[56:59], v[80:83], v[104:107], v[56:59]\n"
    "ds_read_b128 v[140:143], v189 offset:8192\n"
    "v_mfma_f32_16x16x32_bf16 v[60:63], v[84:87], v[104:107], v[60:63]\n"
    "v_mfma_f32_16x16x32_bf16 v[64:67], v[88:91], v[104:107], v[64:67]\n"
    "v_mfma_f32_16x16x32_bf16 v[68:71], v[92:95], v[104:107], v[68:71]\n"
    "s_waitcnt lgkmcnt(0)\n"
    "s_barrier\n"
    "v_mfma_f32_16x16x32_bf16 v[48:51], v[108:111], v[140:143], v[48:51]\n"
    "ds_read_b128 v[104:107], v184 offset:8192\n"
    "ds_read_b64_tr_b16 v[72:73], v236 offset:1024\n"
    "v_mfma_f32_16x16x32_bf16 v[52:55], v[112:115], v[140:143], v[52:55]\n"
    "ds_read_b64_tr_b16 v[74:75], v236 offset:1536\n"
    "ds_read_b64_tr_b16 v[76:77], v236 offset:2048\n"
    "v_mfma_f32_16x16x32_bf16 v[0:3], v[108:111], v[132:135], v[0:3]\n"
    "ds_read_b64_tr_b16 v[78:79], v236 offset:2560\n"
    "s_movk_i32 s0, 4096\n"
    "v_mfma_f32_16x16x32_bf16 v[4:7], v[112:115], v[132:135], v[4:7]\n"
    "ds_read_b128 v[176:179], v185 offset:8192\n"
    "s_bitcmp1_b32 s77, 0\n"
    "v_mfma_f32_16x16x32_bf16 v[24:27], v[108:111], v[136:139], v[24:27]\n"
    "ds_read_b64_tr_b16 v[144:145], v236 offset:5120\n"
    "s_cselect_b64 vcc, -1, 0\n"
    "v_mfma_f32_16x16x32_bf16 v[28:31], v[112:115], v[136:139], v[28:31]\n"
    "ds_read_b64_tr_b16 v[146:147], v236 offset:5632\n"
    "s_cmp_ge_u32 s69, 2\n"
    "v_mfma_f32_16x16x32_bf16 v[56:59], v[116:119], v[140:143], v[56:59]\n"
    "ds_read_b64_tr_b16 v[148:149], v236 offset:6144\n"
    "v_mov_b32 v111, s68\n"
    "v_mfma_f32_16x16x32_bf16 v[8:11], v[116:119], v[132:135], v[8:11]\n"
    "ds_read_b64_tr_b16 v[150:151], v236 offset:6656\n"
    "v_mov_b32 v115, s68\n"
    "v_mfma_f32_16x16x32_bf16 v[32:35], v[116:119], v[136:139], v[32:35]\n"
    "ds_read_b128 v[96:99], v184 offset:3072\n"
    "v_mov_b32 v183, s68\n"
    "v_mfma_f32_16x16x32_bf16 v[60:63], v[120:123], v[140:143], v[60:63]\n"
    "ds_read_b128 v[168:171], v185 offset:3072\n"
    "v_mfma_f32_16x16x32_bf16 v[64:67], v[124:127], v[140:143], v[64:67]\n"
    "ds_read_b128 v[100:103], v184 offset:7168\n"
    "v_mov_b32 v119, s68\n"
    "v_mfma_f32_16x16x32_bf16 v[12:15], v[120:123], v[132:135], v[12:15]\n"
    "ds_read_b128 v[172:175], v185 offset:7168\n"
    "s_waitcnt lgkmcnt(11)\n"
    "v_mfma_f32_16x16x32_bf16 v[48:51], v[72:75], v[104:107], v[48:51]\n"
    "ds_read_b64_tr_b16 v[80:81], v236\n"
    "s_waitcnt lgkmcnt(10)\n"
    "v_mfma_f32_16x16x32_bf16 v[52:55], v[76:79], v[104:107], v[52:55]\n"
    "ds_read_b64_tr_b16 v[82:83], v236 offset:512\n"
    "s_waitcnt lgkmcnt(8)\n"
    "v_mfma_f32_16x16x32_bf16 v[48:51], v[144:147], v[176:179], v[48:51]\n"
    "ds_read_b64_tr_b16 v[152:153], v236 offset:4096\n"
    "s_waitcnt lgkmcnt(7)\n"
    "v_mfma_f32_16x16x32_bf16 v[52:55], v[148:151], v[176:179], v[52:55]\n"
    "ds_read_b64_tr_b16 v[154:155], v236 offset:4608\n"
    "s_waitcnt lgkmcnt(7)\n"
    "v_mfma_f32_16x16x32_bf16 v[0:3], v[72:75], v[96:99], v[0:3]\n"
    "ds_read_b64_tr_b16 v[84:85], v237 offset:1024\n"
    "v_mfma_f32_16x16x32_bf16 v[4:7], v[76:79], v[96:99], v[4:7]\n"
    "ds_read_b64_tr_b16 v[86:87], v237 offset:1536\n"
    "v_cvt_pk_bf16_f32 v108, v48, v49\n"
    "v_cvt_pk_bf16_f32 v109, v50, v51\n"
    "s_waitcnt lgkmcnt(8)\n"
    "v_mfma_f32_16x16x32_bf16 v[0:3], v[144:147], v[168:171], v[0:3]\n"
    "ds_read_b64_tr_b16 v[156:157], v237 offset:5120\n"
    "ds_write_b64 v216, v[108:109] offset:22848\n"
    "v_cvt_pk_bf16_f32 v110, v52, v53\n"
    "v_mfma_f32_16x16x32_bf16 v[4:7], v[148:151], v[168:171], v[4:7]\n"
    "ds_read_b64_tr_b16 v[158:159], v237 offset:5632\n"
    "buffer_store_dwordx4 v[108:111], v243, s[52:55], 0 offen sc1\n"
    "s_nop 1\n"
    "v_cvt_pk_bf16_f32 v111, v54, v55\n"
    "s_waitcnt lgkmcnt(10)\n"
    "v_mfma_f32_16x16x32_bf16 v[24:27], v[72:75], v[100:103], v[24:27]\n"
    "ds_read_b64_tr_b16 v[88:89], v237 offset:2048\n"
    "v_mfma_f32_16x16x32_bf16 v[28:31], v[76:79], v[100:103], v[28:31]\n"
    "ds_read_b64_tr_b16 v[90:91], v237 offset:2560\n"
    "v_cvt_pk_bf16_f32 v112, v0, v1\n"
    "v_cvt_pk_bf16_f32 v113, v2, v3\n"
    "s_waitcnt lgkmcnt(11)\n"
    "v_mfma_f32_16x16x32_bf16 v[24:27], v[144:147], v[172:175], v[24:27]\n"
    "ds_read_b64_tr_b16 v[160:161], v237 offset:6144\n"
    "ds_write_b64 v216, v[112:113] offset:17472\n"
    "v_cvt_pk_bf16_f32 v114, v4, v5\n"
    "v_mfma_f32_16x16x32_bf16 v[28:31], v[148:151], v[172:175], v[28:31]\n"
    "ds_read_b64_tr_b16 v[162:163], v237 offset:6656\n"
    "s_cbranch_vccz SK1%=\n"
    "buffer_store_dwordx4 v[112:115], v242, s[52:55], 0 offen sc1\n"
    "SK1%=:\n"
    "v_mov_b32 v75, s68\n"
    "s_waitcnt lgkmcnt(12)\n"
    "v_mfma_f32_16x16x32_bf16 v[56:59], v[80:83], v[104:107], v[56:59]\n"
    "ds_read_b64_tr_b16 v[92:93], v237\n"
    "v_mov_b32 v79, s68\n"
    "v_cvt_pk_bf16_f32 v182, v54, v55\n"
    "v_mfma_f32_16x16x32_bf16 v[8:11], v[80:83], v[96:99], v[8:11]\n"
    "ds_read_b64_tr_b16 v[94:95], v237 offset:512\n"
    "v_cvt_pk_bf16_f32 v74, v6, v7\n"
    "v_mov_b32 v147, s68\n"
    "v_mfma_f32_16x16x32_bf16 v[32:35], v[80:83], v[100:103], v[32:35]\n"
    "ds_read_b64_tr_b16 v[164:165], v237 offset:4096\n"
    "v_mov_b32 v151, s68\n"
    "v_cvt_pk_bf16_f32 v146, v54, v55\n"
    "s_waitcnt lgkmcnt(13)\n"
    "v_mfma_f32_16x16x32_bf16 v[56:59], v[152:155], v[176:179], v[56:59]\n"
    "ds_read_b64_tr_b16 v[166:167], v237 offset:4608\n"
    "v_cvt_pk_bf16_f32 v116, v24, v25\n"
    "v_mfma_f32_16x16x32_bf16 v[8:11], v[152:155], v[168:171], v[8:11]\n"
    "v_cvt_pk_bf16_f32 v117, v26, v27\n"
    "v_cvt_pk_bf16_f32 v118, v28, v29\n"
    "v_mfma_f32_16x16x32_bf16 v[32:35], v[152:155], v[172:175], v[32:35]\n"
    "ds_write_b64 v216, v[116:117] offset:20160\n"
    "s_cbranch_vccz SK2%=\n"
    "buffer_store_dwordx4 v[116:119], v242, s[52:55], 0 offen offset:2048 sc1\n"
    "SK2%=:\n"
    "s_waitcnt lgkmcnt(13)\n"
    "v_mfma_f32_16x16x32_bf16 v[60:63], v[84:87], v[104:107], v[60:63]\n"
    "v_cvt_pk_bf16_f32 v78, v30, v31\n"
    "v_cvt_pk_bf16_f32 v180, v56, v57\n"
    "s_waitcnt lgkmcnt(8)\n"
    "v_mfma_f32_16x16x32_bf16 v[64:67], v[88:91], v[104:107], v[64:67]\n"
    "s_waitcnt lgkmcnt(0)\n"
    "s_barrier\n"
    "v_cvt_pk_bf16_f32 v181, v58, v59\n"
    "v_mfma_f32_16x16x32_bf16 v[12:15], v[84:87], v[96:99], v[12:15]\n"
    "ds_write_b64 v216, v[110:111] offset:40320\n"
    "s_cbranch_vccz SK3%=\n"
    "buffer_store_dwordx4 v[180:183], v242, s[52:55], s0 offen offset:1024 sc1\n"
    "SK3%=:\n"
    "v_mfma_f32_16x16x32_bf16 v[60:63], v[156:159], v[176:179], v[60:63]\n"
    "v_cvt_pk_bf16_f32 v72, v8, v9\n"
    "v_cvt_pk_bf16_f32 v73, v10, v11\n"
    "v_mfma_f32_16x16x32_bf16 v[64:67], v[160:163], v[176:179], v[64:67]\n"
    "ds_write_b64 v216, v[180:181] offset:5376\n"
    "s_cbranch_vccz SK4%=\n"
    "buffer_store_dwordx4 v[72:75], v242, s[52:55], 0 offen offset:1024 sc1\n"
    "SK4%=:\n"
    "v_mfma_f32_16x16x32_bf16 v[12:15], v[156:159], v[168:171], v[12:15]\n"
    "v_cvt_pk_bf16_f32 v76, v32, v33\n"
    "v_cvt_pk_bf16_f32 v77, v34, v35\n"
    "v_mfma_f32_16x16x32_bf16 v[16:19], v[124:127], v[132:135], v[16:19]\n"
    "ds_write_b64 v216, v[72:73]\n"
    "s_cbranch_vccz SK5%=\n"
    "buffer_store_dwordx4 v[76:79], v242, s[52:55], 0 offen offset:3072 sc1\n"
    "SK5%=:\n"
    "v_mfma_f32_16x16x32_bf16 v[16:19], v[88:91], v[96:99], v[16:19]\n"
    "v_cvt_pk_bf16_f32 v144, v60, v61\n"
    "v_cvt_pk_bf16_f32 v145, v62, v63\n"
    "v_mfma_f32_16x16x32_bf16 v[16:19], v[160:163], v[168:171], v[16:19]\n"
    "ds_write_b64 v216, v[76:77] offset:2688\n"
    "buffer_store_dwordx4 v[144:147], v244, s[52:55], 0 offen offset:512 sc1\n"
    "v_mfma_f32_16x16x32_bf16 v[40:43], v[124:127], v[136:139], v[40:43]\n"
    "v_cvt_pk_bf16_f32 v148, v64, v65\n"
    "v_cvt_pk_bf16_f32 v149, v66, v67\n"
    "v_mfma_f32_16x16x32_bf16 v[40:43], v[88:91], v[100:103], v[40:43]\n"
    "ds_write_b64 v217, v[144:145] offset:22848\n"
    "s_cbranch_scc0 SK6%=\n"
    "buffer_store_dwordx4 v[148:151], v245, s[52:55], 0 offen offset:1024 sc1\n"
    "SK6%=:\n"
    "v_mfma_f32_16x16x32_bf16 v[40:43], v[160:163], v[172:175], v[40:43]\n"
    "v_cvt_pk_bf16_f32 v115, v6, v7\n"
    "v_cvt_pk_bf16_f32 v119, v30, v31\n"
    "v_mfma_f32_16x16x32_bf16 v[36:39], v[120:123], v[136:139], v[36:39]\n"
    "ds_write_b64 v216, v[114:115] offset:34944\n"
    "v_cvt_pk_bf16_f32 v12, v12, v13\n"
    "v_mfma_f32_16x16x32_bf16 v[36:39], v[84:87], v[100:103], v[36:39]\n"
    "v_cvt_pk_bf16_f32 v13, v14, v15\n"
    "v_cvt_pk_bf16_f32 v16, v16, v17\n"
    "v_mfma_f32_16x16x32_bf16 v[36:39], v[156:159], v[172:175], v[36:39]\n"
    "ds_write_b64 v216, v[118:119] offset:37632\n"
    "v_cvt_pk_bf16_f32 v17, v18, v19\n"
    "v_mfma_f32_16x16x32_bf16 v[20:23], v[128:131], v[132:135], v[20:23]\n"
    "v_cvt_pk_bf16_f32 v40, v40, v41\n"
    "v_cvt_pk_bf16_f32 v41, v42, v43\n"
    "v_mfma_f32_16x16x32_bf16 v[20:23], v[92:95], v[96:99], v[20:23]\n"
    "ds_write_b64 v217, v[148:149] offset:40320\n"
    "v_mfma_f32_16x16x32_bf16 v[20:23], v[164:167], v[168:171], v[20:23]\n"
    "v_cvt_pk_bf16_f32 v36, v36, v37\n"
    "v_cvt_pk_bf16_f32 v37, v38, v39\n"
    "v_mfma_f32_16x16x32_bf16 v[44:47], v[128:131], v[136:139], v[44:47]\n"
    "ds_write_b64 v217, v[12:13] offset:17472\n"
    "v_mfma_f32_16x16x32_bf16 v[44:47], v[92:95], v[100:103], v[44:47]\n"
    "ds_write_b64 v217, v[16:17] offset:34944\n"
    "v_mfma_f32_16x16x32_bf16 v[44:47], v[164:167], v[172:175], v[44:47]\n"
    "s_nop 0\n"
    "v_cvt_pk_bf16_f32 v20, v20, v21\n"
    "v_cvt_pk_bf16_f32 v21, v22, v23\n"
    "v_mfma_f32_16x16x32_bf16 v[68:71], v[128:131], v[140:143], v[68:71]\n"
    "ds_write_b64 v217, v[40:41] offset:37632\n"
    "v_mfma_f32_16x16x32_bf16 v[68:71], v[92:95], v[104:107], v[68:71]\n"
    "ds_write_b64 v217, v[36:37] offset:20160\n"
    "v_mfma_f32_16x16x32_bf16 v[68:71], v[164:167], v[176:179], v[68:71]\n"
    "v_cvt_pk_bf16_f32 v44, v44, v45\n"
    "v_cvt_pk_bf16_f32 v45, v46, v47\n"
    "ds_write_b64 v217, v[20:21]\n"
    "ds_write_b64 v217, v[44:45] offset:2688\n"
    "s_nop 3\n"
    "v_cvt_pk_bf16_f32 v68, v68, v69\n"
    "v_cvt_pk_bf16_f32 v69, v70, v71\n"
    "ds_write_b64 v217, v[68:69] offset:5376\n"
    "s_waitcnt lgkmcnt(0)\n"
    "s_barrier\n"
    "s_branch TAIL%=\n"
    "LOADER%=:\n"
    "s_mov_b32 s36, s94\n"
    "s_and_b32 s37, s95, 65535\n"
    "s_mov_b32 s38, 2457600\n"
    "s_mov_b32 s39, 131072\n"
    "s_add_u32 s40, s92, 4294964224\n"
    "s_addc_u32 s41, s93, -1\n"
    "s_and_b32 s41, s41, 65535\n"
    "s_mov_b32 s42, 2624512\n"
    "s_mov_b32 s43, 131072\n"
    "s_add_u32 s60, s96, 4956160\n"
    "s_addc_u32 s61, s97, 0\n"
    "s_and_b32 s61, s61, 65535\n"
    "s_mov_b32 s62, 141312\n"
    "s_mov_b32 s63, 131072\n"
    "s_and_b32 s45, s45, 65535\n"
    "s_mov_b32 s46, 819200\n"
    "s_mov_b32 s47, 131072\n"
    "s_mul_i32 s0, s70, 245760\n"
    "s_lshl_b32 s1, s70, 7\n"
    "s_load_dword s68, s[60:61], 141056\n"
    "s_add_u32 m0, s71, 40960\n"
    "s_nop 0\n"
    "buffer_load_dwordx4 v182, s[36:39], s0 offen lds\n"
    "buffer_load_dwordx4 v183, s[36:39], s0 offen offset:1024 lds\n"
    "buffer_load_dwordx4 v184, s[36:39], s0 offen offset:2048 lds\n"
    "buffer_load_dwordx4 v177, s[40:43], s1 offen offset:3072 lds\n"
    "s_add_u32 s70, s70, 1\n"
    "s_sub_u32 s2, s70, 10\n"
    "s_cselect_b32 s70, s70, s2\n"
    "s_mul_i32 s0, s70, 245760\n"
    "s_lshl_b32 s1, s70, 7\n"
    "s_add_u32 m0, s71, 73728\n"
    "s_nop 0\n"
    "buffer_load_dwordx4 v179, s[36:39], s0 offen lds\n"
    "buffer_load_dwordx4 v180, s[36:39], s0 offen offset:1024 lds\n"
    "buffer_load_dwordx4 v181, s[36:39], s0 offen offset:2048 lds\n"
    "buffer_load_dwordx4 v176, s[40:43], s1 offen offset:3072 lds\n"
    "s_add_u32 m0, s71, 81920\n"
    "s_nop 0\n"
    "buffer_load_dwordx4 v178, s[40:43], s1 offen lds\n"
    "s_add_u32 m0, s71, 77824\n"
    "s_nop 0\n"
    "buffer_load_dwordx4 v182, s[36:39], s0 offen lds\n"
    "buffer_load_dwordx4 v183, s[36:39], s0 offen offset:1024 lds\n"
    "buffer_load_dwordx4 v184, s[36:39], s0 offen offset:2048 lds\n"
    "buffer_load_dwordx4 v177, s[40:43], s1 offen offset:3072 lds\n"
    "s_add_u32 s70, s70, 1\n"
    "s_sub_u32 s2, s70, 10\n"
    "s_cselect_b32 s70, s70, s2\n"
    "s_mul_i32 s0, s70, 245760\n"
    "s_lshl_b32 s1, s70, 7\n"
    "s_mov_b32 m0, 158720\n"
    "s_nop 0\n"
    "buffer_load_dword v185, s[44:47], 0 offen lds\n"
    "s_waitcnt vmcnt(10)\n"
    "s_barrier\n"
    "s_add_u32 m0, s71, 0\n"
    "s_nop 0\n"
    "buffer_load_dwordx4 v179, s[36:39], s0 offen lds\n"
    "buffer_load_dwordx4 v180, s[36:39], s0 offen offset:1024 lds\n"
    "buffer_load_dwordx4 v181, s[36:39], s0 offen offset:2048 lds\n"
    "buffer_load_dwordx4 v176, s[40:43], s1 offen offset:3072 lds\n"
    "s_add_u32 m0, s71, 8192\n"
    "s_nop 0\n"
    "buffer_load_dwordx4 v178, s[40:43], s1 offen lds\n"
    "s_add_u32 m0, s71, 4096\n"
    "s_nop 0\n"
    "buffer_load_dwordx4 v182, s[36:39], s0 offen lds\n"
    "buffer_load_dwordx4 v183, s[36:39], s0 offen offset:1024 lds\n"
    "buffer_load_dwordx4 v184, s[36:39], s0 offen offset:2048 lds\n"
    "buffer_load_dwordx4 v177, s[40:43], s1 offen offset:3072 lds\n"
    "s_add_u32 s70, s70, 1\n"
    "s_sub_u32 s2, s70, 10\n"
    "s_cselect_b32 s70, s70, s2\n"
    "s_mul_i32 s0, s70, 245760\n"
    "s_lshl_b32 s1, s70, 7\n"
    "v_mbcnt_lo_u32_b32 v4, -1, 0\n"
    "v_mbcnt_hi_u32_b32 v4, -1, v4\n"
    "v_lshlrev_b32 v4, 4, v4\n"
    "v_add_u32 v4, 110592, v4\n"
    "ds_read_b128 v[192:195], v4\n"
    "ds_read_b128 v[196:199], v4 offset:1024\n"
    "ds_read_b128 v[200:203], v4 offset:2048\n"
    "ds_read_b128 v[204:207], v4 offset:3072\n"
    "ds_read_b128 v[16:19], v4 offset:4096\n"
    "ds_read_b128 v[20:23], v4 offset:5120\n"
    "ds_read_b128 v[224:227], v4 offset:6144\n"
    "ds_read_b128 v[228:231], v4 offset:7168\n"
    "ds_read_b128 v[232:235], v4 offset:8192\n"
    "ds_read_b128 v[236:239], v4 offset:9216\n"
    "ds_read_b128 v[240:243], v4 offset:10240\n"
    "ds_read_b128 v[244:247], v4 offset:11264\n"
    "ds_read_b128 v[248:251], v4 offset:12288\n"
    "s_waitcnt vmcnt(10)\n"
    "s_barrier\n"
    "s_add_u32 m0, s71, 36864\n"
    "s_nop 0\n"
    "buffer_load_dwordx4 v179, s[36:39], s0 offen lds\n"
    "buffer_load_dwordx4 v180, s[36:39], s0 offen offset:1024 lds\n"
    "buffer_load_dwordx4 v181, s[36:39], s0 offen offset:2048 lds\n"
    "buffer_load_dwordx4 v176, s[40:43], s1 offen offset:3072 lds\n"
    "s_add_u32 m0, s71, 45056\n"
    "s_nop 0\n"
    "buffer_load_dwordx4 v178, s[40:43], s1 offen lds\n"
    "s_add_u32 m0, s71, 40960\n"
    "s_nop 0\n"
    "buffer_load_dwordx4 v182, s[36:39], s0 offen lds\n"
    "buffer_load_dwordx4 v183, s[36:39], s0 offen offset:1024 lds\n"
    "buffer_load_dwordx4 v184, s[36:39], s0 offen offset:2048 lds\n"
    "buffer_load_dwordx4 v177, s[40:43], s1 offen offset:3072 lds\n"
    "s_add_u32 s70, s70, 1\n"
    "s_sub_u32 s2, s70, 10\n"
    "s_cselect_b32 s70, s70, s2\n"
    "s_mul_i32 s0, s70, 245760\n"
    "s_lshl_b32 s1, s70, 7\n"
    "s_waitcnt lgkmcnt(0)\n"
    "s_add_u32 s68, s68, 0x9E3779B9\n"
    "s_mov_b32 s48, s96\n"
    "s_and_b32 s49, s97, 65535\n"
    "s_mov_b32 s50, 2703360\n"
    "s_mov_b32 s51, 131072\n"
    "s_add_u32 s52, s96, 2703360\n"
    "s_addc_u32 s53, s97, 0\n"
    "s_and_b32 s53, s53, 65535\n"
    "s_mov_b32 s54, 2252800\n"
    "s_mov_b32 s55, 131072\n"
    "s_mov_b32 s56, s98\n"
    "s_and_b32 s57, s99, 65535\n"
    "s_mov_b32 s58, 2621440\n"
    "s_mov_b32 s59, 131072\n"
    "s_add_u32 s64, s96, 5097472\n"
    "s_addc_u32 s65, s97, 0\n"
    "s_and_b32 s65, s65, 65535\n"
    "s_mov_b32 s66, 276480\n"
    "s_mov_b32 s67, 131072\n"
    "s_lshl_b32 s85, s85, 7\n"
    "s_add_u32 s85, s85, 108288\n"
    "s_lshl_b32 s20, s69, 2\n"
    "s_and_b32 s23, s69, 1\n"
    "s_lshl_b32 s22, s23, 6\n"
    "v_mov_b32 v6, 0x7fff0000\n"
    "s_waitcnt vmcnt(9)\n"
    "s_barrier\n"
    "s_add_u32 m0, s71, 73728\n"
    "s_nop 0\n"
    "buffer_load_dwordx4 v179, s[36:39], s0 offen lds\n"
    "buffer_load_dwordx4 v180, s[36:39], s0 offen offset:1024 lds\n"
    "buffer_load_dwordx4 v181, s[36:39], s0 offen offset:2048 lds\n"
    "buffer_load_dwordx4 v176, s[40:43], s1 offen offset:3072 lds\n"
    "s_add_u32 m0, s71, 81920\n"
    "s_nop 0\n"
    "buffer_load_dwordx4 v178, s[40:43], s1 offen lds\n"
    "s_add_u32 m0, s71, 77824\n"
    "s_nop 0\n"
    "buffer_load_dwordx4 v182, s[36:39], s0 offen lds\n"
    "buffer_load_dwordx4 v183, s[36:39], s0 offen offset:1024 lds\n"
    "buffer_load_dwordx4 v184, s[36:39], s0 offen offset:2048 lds\n"
    "buffer_load_dwordx4 v177, s[40:43], s1 offen offset:3072 lds\n"
    "s_add_u32 s70, s70, 1\n"
    "s_sub_u32 s2, s70, 10\n"
    "s_cselect_b32 s70, s70, s2\n"
    "s_mul_i32 s0, s70, 245760\n"
    "s_lshl_b32 s1, s70, 7\n"
    "v_mov_b32 v7, 0xff800000\n"
    "v_readlane_b32 s21, v21, s20\n"
    "v_readlane_b32 s72, v22, s20\n"
    "s_add_u32 s20, s20, 1\n"
    "v_add_u32 v208, s21, v16\n"
    "v_add_u32 v209, s21, v17\n"
    "v_mad_u32_u24 v210, s21, v20, v18\n"
    "v_add_u32 v211, s21, v19\n"
    "v_readlane_b32 s21, v21, s20\n"
    "v_readlane_b32 s73, v22, s20\n"
    "s_add_u32 s20, s20, 1\n"
    "v_add_u32 v212, s21, v16\n"
    "v_add_u32 v213, s21, v17\n"
    "v_mad_u32_u24 v214, s21, v20, v18\n"
    "v_add_u32 v215, s21, v19\n"
    "v_readlane_b32 s21, v21, s20\n"
    "v_readlane_b32 s74, v22, s20\n"
    "s_add_u32 s20, s20, 1\n"
    "v_add_u32 v216, s21, v16\n"
    "v_add_u32 v217, s21, v17\n"
    "v_mad_u32_u24 v218, s21, v20, v18\n"
    "s_waitcnt vmcnt(9)\n"
    "s_barrier\n"
    "s_add_u32 m0, s71, 0\n"
    "s_nop 0\n"
    "buffer_load_dwordx4 v179, s[36:39], s0 offen lds\n"
    "buffer_load_dwordx4 v180, s[36:39], s0 offen offset:1024 lds\n"
    "buffer_load_dwordx4 v181, s[36:39], s0 offen offset:2048 lds\n"
    "buffer_load_dwordx4 v176, s[40:43], s1 offen offset:3072 lds\n"
    "s_add_u32 m0, s71, 8192\n"
    "s_nop 0\n"
    "buffer_load_dwordx4 v178, s[40:43], s1 offen lds\n"
    "s_add_u32 m0, s71, 4096\n"
    "s_nop 0\n"
    "buffer_load_dwordx4 v182, s[36:39], s0 offen lds\n"
    "buffer_load_dwordx4 v183, s[36:39], s0 offen offset:1024 lds\n"
    "buffer_load_dwordx4 v184, s[36:39], s0 offen offset:2048 lds\n"
    "buffer_load_dwordx4 v177, s[40:43], s1 offen offset:3072 lds\n"
    "s_add_u32 s70, s70, 1\n"
    "s_sub_u32 s2, s70, 10\n"
    "s_cselect_b32 s70, s70, s2\n"
    "s_mul_i32 s0, s70, 245760\n"
    "s_lshl_b32 s1, s70, 7\n"
    "v_add_u32 v219, s21, v19\n"
    "s_bitcmp1_b32 s77, 3\n"
    "s_cselect_b32 s21, 2688, s21\n"
    "s_cmp_lt_u32 s21, 2688\n"
    "s_cselect_b64 vcc, -1, 0\n"
    "v_cndmask_b32 v168, v192, v7, vcc\n"
    "v_cndmask_b32 v169, v193, v7, vcc\n"
    "v_cndmask_b32 v170, v194, v7, vcc\n"
    "v_cndmask_b32 v171, v195, v7, vcc\n"
    "s_cmp_lt_i32 s74, 0\n"
    "s_cselect_b64 vcc, -1, 0\n"
    "s_max_i32 s74, s74, 0\n"
    "v_cndmask_b32 v186, v206, v6, vcc\n"
    "v_cndmask_b32 v187, v207, v6, vcc\n"
    "v_readlane_b32 s21, v21, s20\n"
    "v_readlane_b32 s88, v22, s20\n"
    "s_add_u32 s20, s20, 1\n"
    "v_add_u32 v220, s21, v16\n"
    "v_add_u32 v221, s21, v17\n"
    "v_mad_u32_u24 v222, s21, v20, v18\n"
    "v_add_u32 v223, s21, v19\n"
    "s_waitcnt vmcnt(9)\n"
    "s_barrier\n"
    "s_add_u32 m0, s71, 36864\n"
    "s_nop 0\n"
    "buffer_load_dwordx4 v179, s[36:39], s0 offen lds\n"
    "buffer_load_dwordx4 v180, s[36:39], s0 offen offset:1024 lds\n"
    "buffer_load_dwordx4 v181, s[36:39], s0 offen offset:2048 lds\n"
    "buffer_load_dwordx4 v176, s[40:43], s1 offen offset:3072 lds\n"
    "s_add_u32 m0, s71, 45056\n"
    "s_nop 0\n"
    "buffer_load_dwordx4 v178, s[40:43], s1 offen lds\n"
    "s_add_u32 m0, s71, 40960\n"
    "s_nop 0\n"
    "buffer_load_dwordx4 v182, s[36:39], s0 offen lds\n"
    "buffer_load_dwordx4 v183, s[36:39], s0 offen offset:1024 lds\n"
    "buffer_load_dwordx4 v184, s[36:39], s0 offen offset:2048 lds\n"
    "buffer_load_dwordx4 v177, s[40:43], s1 offen offset:3072 lds\n"
    "s_add_u32 s70, s70, 1\n"
    "s_sub_u32 s2, s70, 10\n"
    "s_cselect_b32 s70, s70, s2\n"
    "s_mul_i32 s0, s70, 245760\n"
    "s_lshl_b32 s1, s70, 7\n"
    "s_bitcmp1_b32 s77, 3\n"
    "s_cselect_b32 s21, 2688, s21\n"
    "s_cmp_lt_u32 s21, 2688\n"
    "s_cselect_b64 vcc, -1, 0\n"
    "v_cndmask_b32 v172, v192, v7, vcc\n"
    "v_cndmask_b32 v173, v193, v7, vcc\n"
    "v_cndmask_b32 v174, v194, v7, vcc\n"
    "v_cndmask_b32 v175, v195, v7, vcc\n"
    "s_cmp_lt_i32 s88, 0\n"
    "s_cselect_b64 vcc, -1, 0\n"
    "s_max_i32 s88, s88, 0\n"
    "v_cndmask_b32 v188, v206, v6, vcc\n"
    "v_cndmask_b32 v189, v207, v6, vcc\n"
    "s_mul_i32 s21, s23, 40960\n"
    "v_mov_b32 v8, s21\n"
    "v_xad_u32 v224, v224, s22, v8\n"
    "v_xad_u32 v225, v225, s22, v8\n"
    "v_xad_u32 v226, v226, s22, v8\n"
    "v_xad_u32 v227, v227, s22, v8\n"
    "s_lshl_b32 s21, s83, 11\n"
    "v_mov_b32 v8, s21\n"
    "s_waitcnt vmcnt(9)\n"
    "s_barrier\n"
    "s_add_u32 m0, s71, 73728\n"
    "s_nop 0\n"
    "buffer_load_dwordx4 v179, s[36:39], s0 offen lds\n"
    "buffer_load_dwordx4 v180, s[36:39], s0 offen offset:1024 lds\n"
    "buffer_load_dwordx4 v181, s[36:39], s0 offen offset:2048 lds\n"
    "buffer_load_dwordx4 v176, s[40:43], s1 offen offset:3072 lds\n"
    "s_add_u32 m0, s71, 81920\n"
    "s_nop 0\n"
    "buffer_load_dwordx4 v178, s[40:43], s1 offen lds\n"
    "s_add_u32 m0, s71, 77824\n"
    "s_nop 0\n"
    "buffer_load_dwordx4 v182, s[36:39], s0 offen lds\n"
    "buffer_load_dwordx4 v183, s[36:39], s0 offen offset:1024 lds\n"
    "buffer_load_dwordx4 v184, s[36:39], s0 offen offset:2048 lds\n"
    "buffer_load_dwordx4 v177, s[40:43], s1 offen offset:3072 lds\n"
    "s_add_u32 s70, s70, 1\n"
    "s_sub_u32 s2, s70, 10\n"
    "s_cselect_b32 s70, s70, s2\n"
    "s_mul_i32 s0, s70, 245760\n"
    "s_lshl_b32 s1, s70, 7\n"
    "v_xad_u32 v228, v228, s22, v8\n"
    "v_xad_u32 v229, v229, s22, v8\n"
    "v_xad_u32 v230, v230, s22, v8\n"
    "v_xad_u32 v231, v231, s22, v8\n"
    "v_xad_u32 v232, v232, s22, v8\n"
    "s_mul_i32 s21, s83, 40960\n"
    "s_add_u32 s21, s21, s22\n"
    "v_add_u32 v233, s21, v233\n"
    "s_lshl_b32 s21, s83, 7\n"
    "v_add_u32 v234, s21, v234\n"
    "s_lshl_b32 s21, s79, 1\n"
    "v_add_u32 v235, s21, v235\n"
    "s_bfe_u32 s21, s69, 65537\n"
    "s_mul_i32 s21, s21, 80\n"
    "s_add_u32 s21, s21, s23\n"
    "s_mul_i32 s21, s21, 5120\n"
    "v_mov_b32 v8, s21\n"
    "v_xad_u32 v236, v236, s22, v8\n"
    "v_xor_b32 v237, s22, v237\n"
    "s_mul_i32 s21, s79, 25\n"
    "s_add_u32 s20, s21, 307200\n"
    "s_cmp_eq_u32 s79, 0\n"
    "s_cselect_b32 s20, 0, s20\n"
    "s_waitcnt vmcnt(9)\n"
    "s_barrier\n"
    "s_add_u32 m0, s71, 0\n"
    "s_nop 0\n"
    "buffer_load_dwordx4 v179, s[36:39], s0 offen lds\n"
    "buffer_load_dwordx4 v180, s[36:39], s0 offen offset:1024 lds\n"
    "buffer_load_dwordx4 v181, s[36:39], s0 offen offset:2048 lds\n"
    "buffer_load_dwordx4 v176, s[40:43], s1 offen offset:3072 lds\n"
    "s_add_u32 m0, s71, 8192\n"
    "s_nop 0\n"
    "buffer_load_dwordx4 v178, s[40:43], s1 offen lds\n"
    "s_add_u32 m0, s71, 4096\n"
    "s_nop 0\n"
    "buffer_load_dwordx4 v182, s[36:39], s0 offen lds\n"
    "buffer_load_dwordx4 v183, s[36:39], s0 offen offset:1024 lds\n"
    "buffer_load_dwordx4 v184, s[36:39], s0 offen offset:2048 lds\n"
    "buffer_load_dwordx4 v177, s[40:43], s1 offen offset:3072 lds\n"
    "s_add_u32 s70, s70, 1\n"
    "s_sub_u32 s2, s70, 10\n"
    "s_cselect_b32 s70, s70, s2\n"
    "s_mul_i32 s0, s70, 245760\n"
    "s_lshl_b32 s1, s70, 7\n"
    "v_add_u32 v238, s20, v237\n"
    "s_mul_i32 s21, s21, 3\n"
    "v_add_u32 v237, s21, v237\n"
    "v_mov_b32 v252, 0x3f803f80\n"
    "v_mov_b32 v253, 0x3f803f80\n"
    "v_mov_b32 v254, 0x3f803f80\n"
    "v_mov_b32 v255, 0x3f803f80\n"
    "v_mov_b32 v85, s68\n"
    "v_and_b32 v86, 240, v4\n"
    "v_lshrrev_b32 v86, 1, v86\n"
    "v_add_u32 v86, s84, v86\n"
    "v_mov_b32 v87, s85\n"
    "s_waitcnt vmcnt(9)\n"
    "s_barrier\n"
    "s_add_u32 m0, s71, 36864\n"
    "s_nop 0\n"
    "buffer_load_dwordx4 v179, s[36:39], s0 offen lds\n"
    "buffer_load_dwordx4 v180, s[36:39], s0 offen offset:1024 lds\n"
    "buffer_load_dwordx4 v181, s[36:39], s0 offen offset:2048 lds\n"
    "buffer_load_dwordx4 v176, s[40:43], s1 offen offset:3072 lds\n"
    "s_add_u32 m0, s71, 45056\n"
    "s_nop 0\n"
    "buffer_load_dwordx4 v178, s[40:43], s1 offen lds\n"
    "s_add_u32 m0, s71, 40960\n"
    "s_nop 0\n"
    "buffer_load_dwordx4 v182, s[36:39], s0 offen lds\n"
    "buffer_load_dwordx4 v183, s[36:39], s0 offen offset:1024 lds\n"
    "buffer_load_dwordx4 v184, s[36:39], s0 offen offset:2048 lds\n"
    "buffer_load_dwordx4 v177, s[40:43], s1 offen offset:3072 lds\n"
    "s_waitcnt vmcnt(9)\n"
    "s_barrier\n"
    "s_cmp_lg_u32 s69, 6\n"
    "s_cbranch_scc1 NH1%=\n"
    "s_memrealtime s[2:3]\n"
    "s_waitcnt lgkmcnt(0)\n"
    "v_mov_b32 v84, s2\n"
    "buffer_store_dwordx2 v[84:85], v86, s[60:63], 0 offen sc1\n"
    "s_add_u32 m0, s79, 73728\n"
    "s_mov_b32 s2, 25600\n"
    "buffer_load_dwordx4 v236, s[44:47], s2 offen lds\n"
    "s_add_u32 m0, s79, 77824\n"
    "s_mov_b32 s2, 102400\n"
    "buffer_load_dwordx4 v236, s[44:47], s2 offen lds\n"
    "s_add_u32 m0, s79, 81920\n"
    "s_mov_b32 s2, 179200\n"
    "buffer_load_dwordx4 v236, s[44:47], s2 offen lds\n"
    "s_add_u32 m0, s79, 86016\n"
    "s_mov_b32 s2, 256000\n"
    "buffer_load_dwordx4 v236, s[44:47], s2 offen lds\n"
    "s_add_u32 m0, s79, 90112\n"
    "s_mov_b32 s2, 25600\n"
    "buffer_load_dwordx4 v237, s[44:47], s2 offen lds\n"
    "s_add_u32 m0, s79, 94208\n"
    "s_mov_b32 s2, 51200\n"
    "buffer_load_dwordx4 v236, s[44:47], s2 offen lds\n"
    "s_add_u32 m0, s79, 98304\n"
    "s_mov_b32 s2, 128000\n"
    "buffer_load_dwordx4 v236, s[44:47], s2 offen lds\n"
    "s_add_u32 m0, s79, 102400\n"
    "s_mov_b32 s2, 204800\n"
    "buffer_load_dwordx4 v236, s[44:47], s2 offen lds\n"
    "s_add_u32 m0, s79, 106496\n"
    "s_mov_b32 s2, 281600\n"
    "buffer_load_dwordx4 v236, s[44:47], s2 offen lds\n"
    "s_add_u32 m0, s79, 110592\n"
    "s_mov_b32 s2, 51200\n"
    "buffer_load_dwordx4 v237, s[44:47], s2 offen lds\n"
    "s_waitcnt vmcnt(11)\n"
    "s_branch NH2%=\n"
    "NH1%=:\n"
    "s_add_u32 m0, s79, 73728\n"
    "s_mov_b32 s2, 25600\n"
    "buffer_load_dwordx4 v236, s[44:47], s2 offen lds\n"
    "s_add_u32 m0, s79, 77824\n"
    "s_mov_b32 s2, 102400\n"
    "buffer_load_dwordx4 v236, s[44:47], s2 offen lds\n"
    "s_add_u32 m0, s79, 81920\n"
    "s_mov_b32 s2, 179200\n"
    "buffer_load_dwordx4 v236, s[44:47], s2 offen lds\n"
    "s_add_u32 m0, s79, 86016\n"
    "s_mov_b32 s2, 256000\n"
    "buffer_load_dwordx4 v236, s[44:47], s2 offen lds\n"
    "s_add_u32 m0, s79, 90112\n"
    "s_mov_b32 s2, 25600\n"
    "buffer_load_dwordx4 v237, s[44:47], s2 offen lds\n"
    "s_add_u32 m0, s79, 94208\n"
    "s_mov_b32 s2, 51200\n"
    "buffer_load_dwordx4 v236, s[44:47], s2 offen lds\n"
    "s_add_u32 m0, s79, 98304\n"
    "s_mov_b32 s2, 128000\n"
    "buffer_load_dwordx4 v236, s[44:47], s2 offen lds\n"
    "s_add_u32 m0, s79, 102400\n"
    "s_mov_b32 s2, 204800\n"
    "buffer_load_dwordx4 v236, s[44:47], s2 offen lds\n"
    "s_add_u32 m0, s79, 106496\n"
    "s_mov_b32 s2, 281600\n"
    "buffer_load_dwordx4 v236, s[44:47], s2 offen lds\n"
    "s_add_u32 m0, s79, 110592\n"
    "s_mov_b32 s2, 51200\n"
    "buffer_load_dwordx4 v237, s[44:47], s2 offen lds\n"
    "s_waitcnt vmcnt(10)\n"
    "NH2%=:\n"
    "s_barrier\n"
    "s_add_u32 m0, s79, 114688\n"
    "s_mov_b32 s2, 0\n"
    "buffer_load_dwordx4 v236, s[44:47], s2 offen lds\n"
    "s_add_u32 m0, s79, 118784\n"
    "s_mov_b32 s2, 332800\n"
    "buffer_load_dwordx4 v236, s[44:47], s2 offen lds\n"
    "s_add_u32 m0, s79, 122880\n"
    "s_mov_b32 s2, 358400\n"
    "buffer_load_dwordx4 v236, s[44:47], s2 offen lds\n"
    "s_add_u32 m0, s79, 126976\n"
    "s_mov_b32 s2, 384000\n"
    "buffer_load_dwordx4 v236, s[44:47], s2 offen lds\n"
    "s_add_u32 m0, s79, 131072\n"
    "s_mov_b32 s2, 0\n"
    "buffer_load_dwordx4 v238, s[44:47], s2 offen lds\n"
    "s_cmp_eq_u32 s69, 7\n"
    "s_cbranch_scc1 RX%=\n"
    "s_cmp_lg_u32 s69, 5\n"
    "s_cbranch_scc1 NS2%=\n"
    "s_mov_b32 m0, 155648\n"
    "s_nop 0\n"
    "buffer_load_dwordx4 v239, s[60:63], 0 offen lds\n"
    "NS2%=:\n"
    "s_barrier\n"
    "s_waitcnt vmcnt(0)\n"
    "s_cmp_lg_u32 s69, 5\n"
    "s_cbranch_scc1 NV2%=\n"
    "s_mov_b32 s3, 262144\n"
    "v_mbcnt_lo_u32_b32 v4, -1, 0\n"
    "v_mbcnt_hi_u32_b32 v4, -1, v4\n"
    "v_lshlrev_b32 v4, 4, v4\n"
    "v_add_u32 v4, 155648, v4\n"
    "VF2%=:\n"
    "ds_read_b128 v[0:3], v4\n"
    "s_waitcnt lgkmcnt(0)\n"
    "v_xor_b32 v1, s68, v1\n"
    "v_xor_b32 v3, s68, v3\n"
    "v_or_b32 v1, v1, v3\n"
    "v_cmp_ne_u32 vcc, 0, v1\n"
    "s_cbranch_vccz VF2D%=\n"
    "s_sleep 8\n"
    "s_mov_b32 m0, 155648\n"
    "s_sub_u32 s3, s3, 1\n"
    "buffer_load_dwordx4 v239, s[60:63], 0 offen sc1 lds\n"
    "s_waitcnt vmcnt(0)\n"
    "s_cbranch_scc0 VF2%=\n"
    "VF2D%=:\n"
    "NV2%=:\n"
    "s_barrier\n"
    "s_cmp_lg_u32 s69, 6\n"
    "s_cbranch_scc1 TAIL%=\n"
    "buffer_store_dword v85, v87, s[60:63], 0 offen sc1\n"
    "TAIL%=:\n"
    "s_mov_b32 s4, 0x3ea52b5b\n"
    "s_cmp_lt_i32 s72, 0\n"
    "s_cbranch_scc1 PRE%=\n"
    "ds_read_b64 v[100:101], v208\n"
    "ds_read_b64 v[102:103], v210\n"
    "ds_read_b64 v[108:109], v208 offset:17472\n"
    "ds_read_b64 v[110:111], v209 offset:17472\n"
    "ds_read_b64 v[104:105], v208 offset:14784\n"
    "ds_read_b64 v[106:107], v209 offset:14784\n"
    "ds_read_b64 v[132:133], v212\n"
    "ds_read_b64 v[134:135], v214\n"
    "ds_read_b64 v[140:141], v212 offset:17472\n"
    "ds_read_b64 v[142:143], v213 offset:17472\n"
    "ds_read_b64 v[136:137], v212 offset:14784\n"
    "ds_read_b64 v[138:139], v213 offset:14784\n"
    "ds_read_b64_tr_b16 v[112:113], v211\n"
    "ds_read_b64_tr_b16 v[114:115], v211 offset:2688\n"
    "ds_read_b64_tr_b16 v[116:117], v211 offset:16\n"
    "ds_read_b64_tr_b16 v[118:119], v211 offset:2704\n"
    "ds_read_b64_tr_b16 v[144:145], v215\n"
    "ds_read_b64_tr_b16 v[146:147], v215 offset:2688\n"
    "ds_read_b64_tr_b16 v[148:149], v215 offset:16\n"
    "ds_read_b64_tr_b16 v[150:151], v215 offset:2704\n"
    "s_waitcnt lgkmcnt(15)\n"
    "v_mfma_f32_16x16x32_bf16 v[120:123], v[108:111], v[100:103], v[196:199]\n"
    "s_waitcnt lgkmcnt(14)\n"
    "v_mfma_f32_16x16x32_bf16 v[124:127], v[104:107], v[100:103], v[192:195]\n"
    "s_waitcnt lgkmcnt(10)\n"
    "v_mfma_f32_16x16x32_bf16 v[152:155], v[140:143], v[132:135], v[196:199]\n"
    "s_waitcnt lgkmcnt(8)\n"
    "v_mfma_f32_16x16x32_bf16 v[156:159], v[136:139], v[132:135], v[192:195]\n"
    "s_nop 3\n"
    "v_maximum3_f32 v120, v120, v124, v124\n"
    "v_maximum3_f32 v121, v121, v125, v125\n"
    "v_maximum3_f32 v122, v122, v126, v126\n"
    "v_maximum3_f32 v128, v120, v121, v122\n"
    "v_maximum3_f32 v123, v123, v127, v127\n"
    "v_maximum3_f32 v129, v128, v123, v123\n"
    "v_maximum3_f32 v128, v128, v123, v123\n"
    "v_maximum3_f32 v152, v152, v156, v156\n"
    "v_maximum3_f32 v153, v153, v157, v157\n"
    "v_permlane32_swap_b32 v128, v129\n"
    "v_maximum3_f32 v124, v128, v129, v129\n"
    "v_maximum3_f32 v129, v128, v129, v129\n"
    "v_maximum3_f32 v154, v154, v158, v158\n"
    "v_maximum3_f32 v155, v155, v159, v159\n"
    "v_permlane16_swap_b32 v124, v129\n"
    "v_maximum3_f32 v128, v124, v129, v129\n"
    "v_mul_f32 v128, 0xbea52b5b, v128\n"
    "v_pk_fma_f32 v[120:121], v[120:121], s[4:5], v[128:129] op_sel_hi:[1,0,0]\n"
    "v_pk_fma_f32 v[122:123], v[122:123], s[4:5], v[128:129] op_sel_hi:[1,0,0]\n"
    "v_exp_f32 v120, v120\n"
    "v_exp_f32 v121, v121\n"
    "v_exp_f32 v122, v122\n"
    "v_exp_f32 v123, v123\n"
    "v_cvt_pk_bf16_f32 v120, v120, v121\n"
    "v_cvt_pk_bf16_f32 v122, v122, v123\n"
    "v_and_b32 v100, v200, v120\n"
    "v_and_b32 v102, v202, v120\n"
    "v_and_b32 v101, v201, v122\n"
    "v_and_b32 v103, v203, v122\n"
    "v_maximum3_f32 v160, v152, v153, v154\n"
    "v_maximum3_f32 v161, v160, v155, v155\n"
    "v_mfma_f32_16x16x32_bf16 v[104:107], v[252:255], v[100:103], 0\n"
    "v_maximum3_f32 v160, v160, v155, v155\n"
    "s_waitcnt lgkmcnt(6)\n"
    "v_mfma_f32_16x16x32_bf16 v[108:111], v[112:115], v[100:103], 0\n"
    "v_permlane32_swap_b32 v160, v161\n"
    "s_waitcnt lgkmcnt(4)\n"
    "v_mfma_f32_16x16x32_bf16 v[124:127], v[116:119], v[100:103], 0\n"
    "v_maximum3_f32 v156, v160, v161, v161\n"
    "v_maximum3_f32 v161, v160, v161, v161\n"
    "s_nop 1\n"
    "v_permlane16_swap_b32 v156, v161\n"
    "v_maximum3_f32 v160, v156, v161, v161\n"
    "v_rcp_f32 v128, v104\n"
    "v_mul_f32 v160, 0xbea52b5b, v160\n"
    "v_pk_mul_f32 v[108:109], v[108:109], v[128:129] op_sel_hi:[1,0]\n"
    "v_pk_mul_f32 v[110:111], v[110:111], v[128:129] op_sel_hi:[1,0]\n"
    "v_pk_mul_f32 v[124:125], v[124:125], v[128:129] op_sel_hi:[1,0]\n"
    "v_pk_mul_f32 v[126:127], v[126:127], v[128:129] op_sel_hi:[1,0]\n"
    "v_pk_fma_f32 v[152:153], v[152:153], s[4:5], v[160:161] op_sel_hi:[1,0,0]\n"
    "v_pk_fma_f32 v[154:155], v[154:155], s[4:5], v[160:161] op_sel_hi:[1,0,0]\n"
    "v_cvt_pk_bf16_f32 v120, v108, v109\n"
    "v_cvt_pk_bf16_f32 v121, v110, v111\n"
    "v_cvt_pk_bf16_f32 v122, v124, v125\n"
    "v_cvt_pk_bf16_f32 v123, v126, v127\n"
    "v_exp_f32 v152, v152\n"
    "v_exp_f32 v153, v153\n"
    "v_exp_f32 v154, v154\n"
    "v_exp_f32 v155, v155\n"
    "buffer_store_dwordx2 v[120:121], v207, s[48:51], s72 offen sc1\n"
    "buffer_store_dwordx4 v[120:123], v206, s[48:51], s72 offen sc1\n"
    "v_cvt_pk_bf16_f32 v152, v152, v153\n"
    "v_cvt_pk_bf16_f32 v154, v154, v155\n"
    "v_and_b32 v132, v200, v152\n"
    "v_and_b32 v134, v202, v152\n"
    "v_and_b32 v133, v201, v154\n"
    "v_and_b32 v135, v203, v154\n"
    "s_nop 1\n"
    "v_mfma_f32_16x16x32_bf16 v[136:139], v[252:255], v[132:135], 0\n"
    "s_waitcnt lgkmcnt(2)\n"
    "v_mfma_f32_16x16x32_bf16 v[140:143], v[144:147], v[132:135], 0\n"
    "s_waitcnt lgkmcnt(0)\n"
    "v_mfma_f32_16x16x32_bf16 v[156:159], v[148:151], v[132:135], 0\n"
    "s_nop 3\n"
    "v_rcp_f32 v160, v136\n"
    "s_nop 0\n"
    "v_pk_mul_f32 v[140:141], v[140:141], v[160:161] op_sel_hi:[1,0]\n"
    "v_cvt_pk_bf16_f32 v152, v140, v141\n"
    "v_pk_mul_f32 v[142:143], v[142:143], v[160:161] op_sel_hi:[1,0]\n"
    "v_cvt_pk_bf16_f32 v153, v142, v143\n"
    "buffer_store_dwordx2 v[152:153], v207, s[48:51], s73 offen sc1\n"
    "v_pk_mul_f32 v[156:157], v[156:157], v[160:161] op_sel_hi:[1,0]\n"
    "v_cvt_pk_bf16_f32 v154, v156, v157\n"
    "v_pk_mul_f32 v[158:159], v[158:159], v[160:161] op_sel_hi:[1,0]\n"
    "v_cvt_pk_bf16_f32 v155, v158, v159\n"
    "buffer_store_dwordx4 v[152:155], v206, s[48:51], s73 offen sc1\n"
    "s_branch PRE%=\n"
    "RX%=:\n"
    "buffer_load_dwordx2 v[102:103], v240, s[60:63], 0 offen sc1\n"
    "s_barrier\n"
    "s_add_u32 m0, s79, 135168\n"
    "s_mov_b32 s2, 76800\n"
    "buffer_load_dwordx4 v236, s[44:47], s2 offen lds\n"
    "s_add_u32 m0, s79, 139264\n"
    "s_mov_b32 s2, 153600\n"
    "buffer_load_dwordx4 v236, s[44:47], s2 offen lds\n"
    "s_add_u32 m0, s79, 143360\n"
    "s_mov_b32 s2, 230400\n"
    "buffer_load_dwordx4 v236, s[44:47], s2 offen lds\n"
    "s_add_u32 m0, s79, 147456\n"
    "s_mov_b32 s2, 307200\n"
    "buffer_load_dwordx4 v236, s[44:47], s2 offen lds\n"
    "s_add_u32 m0, s79, 151552\n"
    "s_mov_b32 s2, 76800\n"
    "buffer_load_dwordx4 v237, s[44:47], s2 offen lds\n"
    "s_add_u32 m0, s80, 58368\n"
    "s_nop 0\n"
    "buffer_load_dwordx4 v176, s[40:43], s78 offen offset:3072 lds\n"
    "s_add_u32 m0, s80, 60416\n"
    "s_nop 0\n"
    "buffer_load_dwordx4 v177, s[40:43], s78 offen offset:3072 lds\n"
    "s_add_u32 m0, s80, 65536\n"
    "s_nop 0\n"
    "buffer_load_dwordx4 v178, s[40:43], s78 offen lds\n"
    "s_add_u32 s8, s75, 0\n"
    "s_add_u32 s9, s75, 7680\n"
    "s_add_u32 s10, s75, 61440\n"
    "s_add_u32 s11, s75, 15360\n"
    "s_add_u32 s12, s75, 23040\n"
    "s_add_u32 s13, s75, 76800\n"
    "s_add_u32 s14, s76, 0\n"
    "s_add_u32 s15, s76, 7680\n"
    "s_add_u32 s16, s76, 61440\n"
    "s_add_u32 s17, s76, 15360\n"
    "s_add_u32 s18, s76, 23040\n"
    "s_add_u32 s19, s76, 76800\n"
    "s_movk_i32 s6, 4096\n"
    "s_mov_b32 s3, 262144\n"
    "s_mov_b32 s4, 0x3ea52b5b\n"
    "s_bitcmp1_b32 s77, 3\n"
    "s_cselect_b32 s7, s68, 0\n"
    "v_and_b32 v251, s68, v251\n"
    "s_waitcnt vmcnt(8)\n"
    "s_memrealtime s[0:1]\n"
    "s_waitcnt lgkmcnt(0)\n"
    "v_subrev_u32 v102, s0, v102\n"
    "s_nop 1\n"
    "v_max_i32_dpp v102, v102, v102 quad_perm:[1,0,3,2] row_mask:0xf bank_mask:0xf\n"
    "s_nop 1\n"
    "v_max_i32_dpp v102, v102, v102 quad_perm:[2,3,0,1] row_mask:0xf bank_mask:0xf\n"
    "s_nop 1\n"
    "v_max_i32_dpp v102, v102, v102 row_half_mirror row_mask:0xf bank_mask:0xf\n"
    "s_nop 1\n"
    "v_max_i32_dpp v102, v102, v102 row_mirror row_mask:0xf bank_mask:0xf\n"
    "s_nop 0\n"
    "v_readlane_b32 s1, v102, 0\n"
    "v_readlane_b32 s2, v102, 16\n"
    "s_nop 0\n"
    "s_max_i32 s1, s1, s2\n"
    "s_min_i32 s1, s1, 0\n"
    "s_add_u32 s34, s0, s1\n"
    "v_cmp_ne_u32 vcc, s68, v103\n"
    "s_barrier\n"
    "s_setprio 3\n"
    "s_cbranch_vccz HPD%=\n"
    "HPL%=:\n"
    "buffer_load_dwordx2 v[102:103], v240, s[60:63], 0 offen sc1\n"
    "s_waitcnt vmcnt(0)\n"
    "v_cmp_ne_u32 vcc, s68, v103\n"
    "s_cbranch_vccz HPE%=\n"
    "s_sleep 2\n"
    "s_sub_u32 s3, s3, 1\n"
    "s_cbranch_scc0 HPL%=\n"
    "HPE%=:\n"
    "s_memrealtime s[0:1]\n"
    "s_waitcnt lgkmcnt(0)\n"
    "v_subrev_u32 v102, s0, v102\n"
    "s_nop 1\n"
    "v_max_i32_dpp v102, v102, v102 quad_perm:[1,0,3,2] row_mask:0xf bank_mask:0xf\n"
    "s_nop 1\n"
    "v_max_i32_dpp v102, v102, v102 quad_perm:[2,3,0,1] row_mask:0xf bank_mask:0xf\n"
    "s_nop 1\n"
    "v_max_i32_dpp v102, v102, v102 row_half_mirror row_mask:0xf bank_mask:0xf\n"
    "s_nop 1\n"
    "v_max_i32_dpp v102, v102, v102 row_mirror row_mask:0xf bank_mask:0xf\n"
    "s_nop 0\n"
    "v_readlane_b32 s1, v102, 0\n"
    "v_readlane_b32 s2, v102, 16\n"
    "s_nop 0\n"
    "s_max_i32 s1, s1, s2\n"
    "s_min_i32 s1, s1, 0\n"
    "s_add_u32 s34, s0, s1\n"
    "HPD%=:\n"
    "s_add_u32 s2, s34, 130\n"
    "URX%=:\n"
    "s_memrealtime s[0:1]\n"
    "s_waitcnt lgkmcnt(0)\n"
    "s_sub_u32 s0, s0, s2\n"
    "s_cmp_lt_i32 s0, 0\n"
    "s_cbranch_scc1 URX%=\n"
    "buffer_load_dword v144, v242, s[60:63], 0 offen\n"
    "buffer_load_dwordx4 v[100:103], v244, s[52:55], 0 offen sc1\n"
    "buffer_load_dwordx4 v[104:107], v245, s[52:55], 0 offen offset:512 sc1\n"
    "buffer_load_dwordx4 v[108:111], v245, s[52:55], 0 offen offset:1024 sc1\n"
    "buffer_load_dwordx4 v[112:115], v246, s[52:55], 0 offen sc1\n"
    "buffer_load_dwordx4 v[116:119], v246, s[52:55], 0 offen offset:512 sc1\n"
    "buffer_load_dwordx4 v[120:123], v243, s[52:55], s6 offen sc1\n"
    "buffer_load_dwordx4 v[124:127], v243, s[52:55], s6 offen offset:1024 sc1\n"
    "buffer_load_dwordx4 v[128:131], v243, s[52:55], 0 offen sc1\n"
    "buffer_load_dwordx4 v[132:135], v243, s[52:55], 0 offen offset:1024 sc1\n"
    "buffer_load_dwordx4 v[136:139], v243, s[52:55], 0 offen offset:2048 sc1\n"
    "buffer_load_dwordx4 v[140:143], v243, s[52:55], 0 offen offset:3072 sc1\n"
    "v_mov_b32 v152, 156544\n"
    "ds_read_b128 v[156:159], v152\n"
    "s_waitcnt lgkmcnt(0)\n"
    "v_readfirstlane_b32 s20, v156\n"
    "v_readfirstlane_b32 s21, v158\n"
    "s_or_b32 s34, s34, 1\n"
    "v_mov_b32 v154, 156708\n"
    "v_mov_b32 v155, s34\n"
    "RXP%=:\n"
    "s_waitcnt vmcnt(8)\n"
    "ds_write_b64 v248, v[100:101]\n"
    "v_xor_b32 v151, s7, v103\n"
    "v_xor_b32 v152, s7, v107\n"
    "v_mov_b32 v103, v106\n"
    "v_or_b32 v150, v151, v152\n"
    "ds_write_b64 v248, v[102:103] offset:17472\n"
    "ds_write_b64 v249, v[104:105]\n"
    "ds_write_b64 v249, v[108:109] offset:17472\n"
    "v_xor_b32 v151, s7, v111\n"
    "v_or_b32 v150, v150, v151\n"
    "v_xor_b32 v153, s68, v144\n"
    "s_waitcnt vmcnt(6)\n"
    "ds_write_b64 v250, v[112:113]\n"
    "v_xor_b32 v151, v251, v115\n"
    "v_xor_b32 v152, v251, v119\n"
    "v_mov_b32 v115, v118\n"
    "v_or3_b32 v150, v150, v151, v152\n"
    "ds_write_b64 v250, v[114:115] offset:17472\n"
    "s_waitcnt vmcnt(4)\n"
    "ds_write_b64 v247, v[120:121] offset:22848\n"
    "v_xor_b32 v151, s68, v123\n"
    "v_xor_b32 v152, s68, v127\n"
    "v_mov_b32 v123, v126\n"
    "v_or3_b32 v150, v150, v151, v152\n"
    "ds_write_b64 v247, v[122:123] offset:40320\n"
    "ds_write_b64 v247, v[124:125] offset:5376\n"
    "s_waitcnt vmcnt(2)\n"
    "ds_write_b64 v247, v[128:129] offset:17472\n"
    "v_xor_b32 v151, s68, v131\n"
    "v_xor_b32 v152, s68, v135\n"
    "v_mov_b32 v131, v134\n"
    "v_or3_b32 v150, v150, v151, v152\n"
    "ds_write_b64 v247, v[130:131] offset:34944\n"
    "ds_write_b64 v247, v[132:133]\n"
    "s_waitcnt vmcnt(0)\n"
    "ds_write_b64 v247, v[136:137] offset:20160\n"
    "v_xor_b32 v151, s68, v139\n"
    "v_xor_b32 v152, s68, v143\n"
    "v_mov_b32 v139, v142\n"
    "v_or3_b32 v150, v150, v151, v152\n"
    "ds_write_b64 v247, v[138:139] offset:37632\n"
    "ds_write_b64 v247, v[140:141] offset:2688\n"
    "v_or_b32 v153, v153, v150\n"
    "v_cmp_ne_u32 vcc, 0, v153\n"
    "s_cbranch_vccz RXD%=\n"
    "s_waitcnt lgkmcnt(0)\n"
    "s_sub_u32 s3, s3, 1\n"
    "s_cbranch_scc1 RXD%=\n"
    "v_cmp_ne_u32 vcc, s68, v144\n"
    "s_cbranch_vccz RXA%=\n"
    "M2L%=:\n"
    "s_sleep 6\n"
    "buffer_load_dword v144, v242, s[60:63], 0 offen sc1\n"
    "s_waitcnt vmcnt(0)\n"
    "v_cmp_ne_u32 vcc, s68, v144\n"
    "s_cbranch_vccz M2D%=\n"
    "s_sub_u32 s3, s3, 1\n"
    "s_cbranch_scc0 M2L%=\n"
    "M2D%=:\n"
    "v_cmp_ne_u32 vcc, 0, v150\n"
    "s_cbranch_vccz RXD%=\n"
    "RXA%=:\n"
    "buffer_load_dword v144, v242, s[60:63], 0 offen sc1\n"
    "buffer_load_dwordx4 v[100:103], v244, s[52:55], 0 offen sc1\n"
    "buffer_load_dwordx4 v[104:107], v245, s[52:55], 0 offen offset:512 sc1\n"
    "buffer_load_dwordx4 v[108:111], v245, s[52:55], 0 offen offset:1024 sc1\n"
    "buffer_load_dwordx4 v[112:115], v246, s[52:55], 0 offen sc1\n"
    "buffer_load_dwordx4 v[116:119], v246, s[52:55], 0 offen offset:512 sc1\n"
    "buffer_load_dwordx4 v[120:123], v243, s[52:55], s6 offen sc1\n"
    "buffer_load_dwordx4 v[124:127], v243, s[52:55], s6 offen offset:1024 sc1\n"
    "buffer_load_dwordx4 v[128:131], v243, s[52:55], 0 offen sc1\n"
    "buffer_load_dwordx4 v[132:135], v243, s[52:55], 0 offen offset:1024 sc1\n"
    "buffer_load_dwordx4 v[136:139], v243, s[52:55], 0 offen offset:2048 sc1\n"
    "buffer_load_dwordx4 v[140:143], v243, s[52:55], 0 offen offset:3072 sc1\n"
    "s_waitcnt vmcnt(0)\n"
    "s_branch RXP%=\n"
    "RXD%=:\n"
    "ds_write_b32 v154, v155\n"
    "s_branch LATE%=\n"
    "PRE%=:\n"
    "s_cmp_lt_u32 s69, 4\n"
    "s_cbranch_scc1 FULL%=\n"
    "s_setprio 3\n"
    "s_add_u32 m0, s79, 135168\n"
    "s_mov_b32 s2, 76800\n"
    "buffer_load_dwordx4 v236, s[44:47], s2 offen lds\n"
    "s_add_u32 m0, s79, 139264\n"
    "s_mov_b32 s2, 153600\n"
    "buffer_load_dwordx4 v236, s[44:47], s2 offen lds\n"
    "s_add_u32 m0, s79, 143360\n"
    "s_mov_b32 s2, 230400\n"
    "buffer_load_dwordx4 v236, s[44:47], s2 offen lds\n"
    "s_add_u32 m0, s79, 147456\n"
    "s_mov_b32 s2, 307200\n"
    "buffer_load_dwordx4 v236, s[44:47], s2 offen lds\n"
    "s_add_u32 m0, s79, 151552\n"
    "s_mov_b32 s2, 76800\n"
    "buffer_load_dwordx4 v237, s[44:47], s2 offen lds\n"
    "s_add_u32 m0, s80, 58368\n"
    "s_nop 0\n"
    "buffer_load_dwordx4 v176, s[40:43], s78 offen offset:3072 lds\n"
    "s_add_u32 m0, s80, 60416\n"
    "s_nop 0\n"
    "buffer_load_dwordx4 v177, s[40:43], s78 offen offset:3072 lds\n"
    "s_add_u32 m0, s80, 65536\n"
    "s_nop 0\n"
    "buffer_load_dwordx4 v178, s[40:43], s78 offen lds\n"
    "ds_read_b64_tr_b16 v[0:1], v224\n"
    "ds_read_b64_tr_b16 v[2:3], v224 offset:512\n"
    "ds_read_b64_tr_b16 v[4:5], v225\n"
    "ds_read_b64_tr_b16 v[6:7], v225 offset:512\n"
    "ds_read_b64_tr_b16 v[8:9], v226\n"
    "ds_read_b64_tr_b16 v[10:11], v226 offset:512\n"
    "ds_read_b64_tr_b16 v[12:13], v227\n"
    "ds_read_b64_tr_b16 v[14:15], v227 offset:512\n"
    "ds_read_b64_tr_b16 v[16:17], v224 offset:4096\n"
    "ds_read_b64_tr_b16 v[18:19], v224 offset:4608\n"
    "ds_read_b64_tr_b16 v[20:21], v225 offset:4096\n"
    "ds_read_b64_tr_b16 v[22:23], v225 offset:4608\n"
    "ds_read_b64_tr_b16 v[24:25], v226 offset:4096\n"
    "ds_read_b64_tr_b16 v[26:27], v226 offset:4608\n"
    "ds_read_b64_tr_b16 v[28:29], v227 offset:4096\n"
    "ds_read_b64_tr_b16 v[30:31], v227 offset:4608\n"
    "ds_read_b64_tr_b16 v[32:33], v224 offset:8192\n"
    "ds_read_b64_tr_b16 v[34:35], v224 offset:8704\n"
    "ds_read_b64_tr_b16 v[36:37], v225 offset:8192\n"
    "ds_read_b64_tr_b16 v[38:39], v225 offset:8704\n"
    "ds_read_b64_tr_b16 v[40:41], v226 offset:8192\n"
    "ds_read_b64_tr_b16 v[42:43], v226 offset:8704\n"
    "ds_read_b64_tr_b16 v[44:45], v227 offset:8192\n"
    "ds_read_b64_tr_b16 v[46:47], v227 offset:8704\n"
    "ds_read_b64_tr_b16 v[48:49], v224 offset:12288\n"
    "ds_read_b64_tr_b16 v[50:51], v224 offset:12800\n"
    "ds_read_b64_tr_b16 v[52:53], v225 offset:12288\n"
    "ds_read_b64_tr_b16 v[54:55], v225 offset:12800\n"
    "ds_read_b64_tr_b16 v[56:57], v226 offset:12288\n"
    "ds_read_b64_tr_b16 v[58:59], v226 offset:12800\n"
    "ds_read_b64_tr_b16 v[60:61], v227 offset:12288\n"
    "ds_read_b64_tr_b16 v[62:63], v227 offset:12800\n"
    "ds_read_b64_tr_b16 v[64:65], v224 offset:16384\n"
    "ds_read_b64_tr_b16 v[66:67], v224 offset:16896\n"
    "ds_read_b64_tr_b16 v[68:69], v225 offset:16384\n"
    "ds_read_b64_tr_b16 v[70:71], v225 offset:16896\n"
    "ds_read_b64_tr_b16 v[72:73], v226 offset:16384\n"
    "ds_read_b64_tr_b16 v[74:75], v226 offset:16896\n"
    "ds_read_b64_tr_b16 v[76:77], v227 offset:16384\n"
    "ds_read_b64_tr_b16 v[78:79], v227 offset:16896\n"
    "v_mov_b32 v140, 156544\n"
    "ds_read_b128 v[144:147], v140\n"
    "s_add_u32 s8, s75, 0\n"
    "s_add_u32 s9, s75, 7680\n"
    "s_add_u32 s10, s75, 61440\n"
    "s_add_u32 s11, s75, 15360\n"
    "s_add_u32 s12, s75, 23040\n"
    "s_add_u32 s13, s75, 76800\n"
    "s_add_u32 s14, s76, 0\n"
    "s_add_u32 s15, s76, 7680\n"
    "s_add_u32 s16, s76, 61440\n"
    "s_add_u32 s17, s76, 15360\n"
    "s_add_u32 s18, s76, 23040\n"
    "s_add_u32 s19, s76, 76800\n"
    "s_waitcnt lgkmcnt(0)\n"
    "v_readfirstlane_b32 s20, v144\n"
    "v_readfirstlane_b32 s21, v146\n"
    "s_mov_b32 s3, 16777216\n"
    "v_mov_b32 v141, 156708\n"
    "RXW%=:\n"
    "ds_read_b32 v140, v141\n"
    "s_waitcnt lgkmcnt(0)\n"
    "v_cmp_ne_u32 vcc, 0, v140\n"
    "s_cbranch_vccnz RXWD%=\n"
    "s_sleep 1\n"
    "s_sub_u32 s3, s3, 1\n"
    "s_cbranch_scc0 RXW%=\n"
    "RXWD%=:\n"
    "LATE%=:\n"
    "s_setprio 3\n"
    "ds_read_b64 v[100:101], v216\n"
    "ds_read_b64 v[102:103], v218\n"
    "ds_read_b64 v[108:109], v216 offset:17472\n"
    "ds_read_b64 v[110:111], v217 offset:17472\n"
    "ds_read_b64 v[104:105], v216 offset:14784\n"
    "ds_read_b64 v[106:107], v217 offset:14784\n"
    "ds_read_b64 v[132:133], v220\n"
    "ds_read_b64 v[134:135], v222\n"
    "ds_read_b64 v[140:141], v220 offset:17472\n"
    "ds_read_b64 v[142:143], v221 offset:17472\n"
    "ds_read_b64 v[136:137], v220 offset:14784\n"
    "ds_read_b64 v[138:139], v221 offset:14784\n"
    "ds_read_b64_tr_b16 v[112:113], v219\n"
    "ds_read_b64_tr_b16 v[114:115], v219 offset:2688\n"
    "ds_read_b64_tr_b16 v[144:145], v223\n"
    "ds_read_b64_tr_b16 v[146:147], v223 offset:2688\n"
    "ds_read_b64_tr_b16 v[116:117], v219 offset:16\n"
    "ds_read_b64_tr_b16 v[118:119], v219 offset:2704\n"
    "ds_read_b64_tr_b16 v[148:149], v223 offset:16\n"
    "ds_read_b64_tr_b16 v[150:151], v223 offset:2704\n"
    "s_waitcnt lgkmcnt(15)\n"
    "v_mfma_f32_16x16x32_bf16 v[120:123], v[108:111], v[100:103], v[196:199]\n"
    "s_waitcnt lgkmcnt(14)\n"
    "v_mfma_f32_16x16x32_bf16 v[124:127], v[104:107], v[100:103], v[168:171]\n"
    "s_waitcnt lgkmcnt(10)\n"
    "v_mfma_f32_16x16x32_bf16 v[152:155], v[140:143], v[132:135], v[196:199]\n"
    "s_waitcnt lgkmcnt(8)\n"
    "v_mfma_f32_16x16x32_bf16 v[156:159], v[136:139], v[132:135], v[172:175]\n"
    "s_nop 3\n"
    "v_maximum3_f32 v120, v120, v124, v124\n"
    "v_maximum3_f32 v121, v121, v125, v125\n"
    "v_maximum3_f32 v122, v122, v126, v126\n"
    "v_maximum3_f32 v128, v120, v121, v122\n"
    "v_maximum3_f32 v123, v123, v127, v127\n"
    "v_maximum3_f32 v129, v128, v123, v123\n"
    "v_maximum3_f32 v128, v128, v123, v123\n"
    "v_maximum3_f32 v152, v152, v156, v156\n"
    "v_maximum3_f32 v153, v153, v157, v157\n"
    "v_maximum3_f32 v154, v154, v158, v158\n"
    "v_maximum3_f32 v155, v155, v159, v159\n"
    "v_maximum3_f32 v160, v152, v153, v154\n"
    "v_maximum3_f32 v161, v160, v155, v155\n"
    "v_maximum3_f32 v160, v160, v155, v155\n"
    "v_permlane32_swap_b32 v128, v129\n"
    "v_maximum3_f32 v124, v128, v129, v129\n"
    "v_permlane32_swap_b32 v160, v161\n"
    "v_maximum3_f32 v156, v160, v161, v161\n"
    "v_maximum3_f32 v129, v128, v129, v129\n"
    "v_maximum3_f32 v161, v160, v161, v161\n"
    "s_nop 0\n"
    "v_permlane16_swap_b32 v124, v129\n"
    "v_maximum3_f32 v128, v124, v129, v129\n"
    "v_permlane16_swap_b32 v156, v161\n"
    "v_maximum3_f32 v160, v156, v161, v161\n"
    "v_mul_f32 v128, 0xbea52b5b, v128\n"
    "v_mul_f32 v160, 0xbea52b5b, v160\n"
    "v_pk_fma_f32 v[120:121], v[120:121], s[4:5], v[128:129] op_sel_hi:[1,0,0]\n"
    "v_pk_fma_f32 v[122:123], v[122:123], s[4:5], v[128:129] op_sel_hi:[1,0,0]\n"
    "v_pk_fma_f32 v[152:153], v[152:153], s[4:5], v[160:161] op_sel_hi:[1,0,0]\n"
    "v_pk_fma_f32 v[154:155], v[154:155], s[4:5], v[160:161] op_sel_hi:[1,0,0]\n"
    "v_exp_f32 v120, v120\n"
    "v_exp_f32 v121, v121\n"
    "v_exp_f32 v122, v122\n"
    "v_exp_f32 v123, v123\n"
    "v_exp_f32 v152, v152\n"
    "v_exp_f32 v153, v153\n"
    "v_exp_f32 v154, v154\n"
    "v_exp_f32 v155, v155\n"
    "v_cvt_pk_bf16_f32 v120, v120, v121\n"
    "v_cvt_pk_bf16_f32 v122, v122, v123\n"
    "v_cvt_pk_bf16_f32 v152, v152, v153\n"
    "v_cvt_pk_bf16_f32 v154, v154, v155\n"
    "v_and_b32 v100, v200, v120\n"
    "v_and_b32 v102, v202, v120\n"
    "v_and_b32 v101, v201, v122\n"
    "v_and_b32 v103, v203, v122\n"
    "v_and_b32 v132, v200, v152\n"
    "v_and_b32 v134, v202, v152\n"
    "v_mfma_f32_16x16x32_bf16 v[104:107], v[252:255], v[100:103], 0\n"
    "v_and_b32 v133, v201, v154\n"
    "v_and_b32 v135, v203, v154\n"
    "s_waitcnt lgkmcnt(6)\n"
    "v_mfma_f32_16x16x32_bf16 v[108:111], v[112:115], v[100:103], 0\n"
    "v_mfma_f32_16x16x32_bf16 v[136:139], v[252:255], v[132:135], 0\n"
    "s_waitcnt lgkmcnt(4)\n"
    "v_mfma_f32_16x16x32_bf16 v[140:143], v[144:147], v[132:135], 0\n"
    "s_waitcnt lgkmcnt(2)\n"
    "v_mfma_f32_16x16x32_bf16 v[124:127], v[116:119], v[100:103], 0\n"
    "s_waitcnt lgkmcnt(0)\n"
    "v_mfma_f32_16x16x32_bf16 v[156:159], v[148:151], v[132:135], 0\n"
    "v_rcp_f32 v128, v104\n"
    "s_nop 0\n"
    "v_pk_mul_f32 v[108:109], v[108:109], v[128:129] op_sel_hi:[1,0]\n"
    "v_cvt_pk_bf16_f32 v120, v108, v109\n"
    "v_pk_mul_f32 v[110:111], v[110:111], v[128:129] op_sel_hi:[1,0]\n"
    "v_cvt_pk_bf16_f32 v121, v110, v111\n"
    "v_rcp_f32 v160, v136\n"
    "buffer_store_dwordx2 v[120:121], v187, s[48:51], s74 offen sc1\n"
    "v_pk_mul_f32 v[124:125], v[124:125], v[128:129] op_sel_hi:[1,0]\n"
    "v_pk_mul_f32 v[126:127], v[126:127], v[128:129] op_sel_hi:[1,0]\n"
    "v_pk_mul_f32 v[140:141], v[140:141], v[160:161] op_sel_hi:[1,0]\n"
    "v_pk_mul_f32 v[142:143], v[142:143], v[160:161] op_sel_hi:[1,0]\n"
    "v_pk_mul_f32 v[156:157], v[156:157], v[160:161] op_sel_hi:[1,0]\n"
    "v_pk_mul_f32 v[158:159], v[158:159], v[160:161] op_sel_hi:[1,0]\n"
    "v_cvt_pk_bf16_f32 v122, v124, v125\n"
    "v_cvt_pk_bf16_f32 v123, v126, v127\n"
    "v_cvt_pk_bf16_f32 v152, v140, v141\n"
    "v_cvt_pk_bf16_f32 v153, v142, v143\n"
    "v_cvt_pk_bf16_f32 v154, v156, v157\n"
    "v_cvt_pk_bf16_f32 v155, v158, v159\n"
    "buffer_store_dwordx4 v[120:123], v186, s[48:51], s74 offen sc1\n"
    "buffer_store_dwordx2 v[152:153], v189, s[48:51], s88 offen sc1\n"
    "buffer_store_dwordx4 v[152:155], v188, s[48:51], s88 offen sc1\n"
    "v_mov_b32 v160, 0\n"
    "v_mov_b32 v161, 0\n"
    "v_mov_b32 v162, 0\n"
    "v_mov_b32 v163, 0\n"
    "v_mov_b32 v164, 0\n"
    "v_mov_b32 v165, 0\n"
    "v_mov_b32 v166, 0\n"
    "v_mov_b32 v167, 0\n"
    "v_mov_b32 v168, 0\n"
    "v_mov_b32 v169, 0\n"
    "v_mov_b32 v170, 0\n"
    "v_mov_b32 v171, 0\n"
    "v_mov_b32 v172, 0\n"
    "v_mov_b32 v173, 0\n"
    "v_mov_b32 v174, 0\n"
    "v_mov_b32 v175, 0\n"
    "v_mov_b32 v181, 156700\n"
    "v_mov_b32 v180, 1\n"
    "s_cmp_eq_u32 s69, 4\n"
    "s_cbranch_scc1 H4%=\n"
    "s_cmp_eq_u32 s69, 5\n"
    "s_cbranch_scc1 H5%=\n"
    "s_setprio 0\n"
    "buffer_load_dwordx4 v[100:103], v204, s[48:51], s14 offen sc1\n"
    "buffer_load_dwordx4 v[104:107], v204, s[48:51], s14 offen offset:3840 sc1\n"
    "buffer_load_dwordx4 v[108:111], v204, s[48:51], s15 offen sc1\n"
    "buffer_load_dwordx4 v[112:115], v204, s[48:51], s15 offen offset:3840 sc1\n"
    "buffer_load_dwordx2 v[116:117], v205, s[48:51], s14 offen sc1\n"
    "buffer_load_dwordx2 v[118:119], v205, s[48:51], s16 offen sc1\n"
    "s_cmp_eq_u32 s69, 7\n"
    "s_cbranch_scc1 H7%=\n"
    "buffer_load_dwordx4 v[140:143], v204, s[48:51], s17 offen sc1\n"
    "buffer_load_dwordx4 v[144:147], v204, s[48:51], s17 offen offset:3840 sc1\n"
    "buffer_load_dwordx4 v[148:151], v204, s[48:51], s18 offen sc1\n"
    "buffer_load_dwordx4 v[152:155], v204, s[48:51], s18 offen offset:3840 sc1\n"
    "buffer_load_dwordx2 v[156:157], v205, s[48:51], s17 offen sc1\n"
    "buffer_load_dwordx2 v[158:159], v205, s[48:51], s19 offen sc1\n"
    "s_waitcnt vmcnt(16)\n"
    "ds_add_u32 v181, v180\n"
    "s_andn2_b32 s24, 2047, s20\n"
    "s_lshr_b32 s22, s24, 1\n"
    "s_sub_u32 s22, s22, s82\n"
    "s_and_b32 s24, s24, 1\n"
    "s_cmp_eq_u32 s24, s81\n"
    "s_cselect_b32 s22, s22, -1\n"
    "s_andn2_b32 s24, 2047, s21\n"
    "s_lshr_b32 s23, s24, 1\n"
    "s_sub_u32 s23, s23, s82\n"
    "s_and_b32 s24, s24, 1\n"
    "s_cmp_eq_u32 s24, s81\n"
    "s_cselect_b32 s23, s23, -1\n"
    "s_min_u32 s24, s22, s23\n"
    "s_cmp_lt_u32 s24, 96\n"
    "s_cbranch_scc1 YJOB%=\n"
    "s_mov_b32 s3, 16777216\n"
    "v_mov_b32 v181, 156700\n"
    "GO6%=:\n"
    "ds_read_b32 v180, v181\n"
    "s_waitcnt lgkmcnt(0)\n"
    "v_cmp_le_u32 vcc, 256, v180\n"
    "s_cbranch_vccnz GO6D%=\n"
    "s_sleep 1\n"
    "s_sub_u32 s3, s3, 1\n"
    "s_cbranch_scc0 GO6%=\n"
    "GO6D%=:\n"
    "s_branch COPYB%=\n"
    "H7%=:\n"
    "ds_add_u32 v181, v180\n"
    "ds_read_b64_tr_b16 v[0:1], v224\n"
    "ds_read_b64_tr_b16 v[2:3], v224 offset:512\n"
    "ds_read_b64_tr_b16 v[4:5], v225\n"
    "ds_read_b64_tr_b16 v[6:7], v225 offset:512\n"
    "ds_read_b64_tr_b16 v[8:9], v226\n"
    "ds_read_b64_tr_b16 v[10:11], v226 offset:512\n"
    "ds_read_b64_tr_b16 v[12:13], v227\n"
    "ds_read_b64_tr_b16 v[14:15], v227 offset:512\n"
    "ds_read_b64_tr_b16 v[16:17], v224 offset:4096\n"
    "ds_read_b64_tr_b16 v[18:19], v224 offset:4608\n"
    "ds_read_b64_tr_b16 v[20:21], v225 offset:4096\n"
    "ds_read_b64_tr_b16 v[22:23], v225 offset:4608\n"
    "ds_read_b64_tr_b16 v[24:25], v226 offset:4096\n"
    "ds_read_b64_tr_b16 v[26:27], v226 offset:4608\n"
    "ds_read_b64_tr_b16 v[28:29], v227 offset:4096\n"
    "ds_read_b64_tr_b16 v[30:31], v227 offset:4608\n"
    "ds_read_b64_tr_b16 v[32:33], v224 offset:8192\n"
    "ds_read_b64_tr_b16 v[34:35], v224 offset:8704\n"
    "ds_read_b64_tr_b16 v[36:37], v225 offset:8192\n"
    "ds_read_b64_tr_b16 v[38:39], v225 offset:8704\n"
    "ds_read_b64_tr_b16 v[40:41], v226 offset:8192\n"
    "ds_read_b64_tr_b16 v[42:43], v226 offset:8704\n"
    "ds_read_b64_tr_b16 v[44:45], v227 offset:8192\n"
    "ds_read_b64_tr_b16 v[46:47], v227 offset:8704\n"
    "ds_read_b64_tr_b16 v[48:49], v224 offset:12288\n"
    "ds_read_b64_tr_b16 v[50:51], v224 offset:12800\n"
    "ds_read_b64_tr_b16 v[52:53], v225 offset:12288\n"
    "ds_read_b64_tr_b16 v[54:55], v225 offset:12800\n"
    "ds_read_b64_tr_b16 v[56:57], v226 offset:12288\n"
    "ds_read_b64_tr_b16 v[58:59], v226 offset:12800\n"
    "ds_read_b64_tr_b16 v[60:61], v227 offset:12288\n"
    "ds_read_b64_tr_b16 v[62:63], v227 offset:12800\n"
    "ds_read_b64_tr_b16 v[64:65], v224 offset:16384\n"
    "ds_read_b64_tr_b16 v[66:67], v224 offset:16896\n"
    "ds_read_b64_tr_b16 v[68:69], v225 offset:16384\n"
    "ds_read_b64_tr_b16 v[70:71], v225 offset:16896\n"
    "ds_read_b64_tr_b16 v[72:73], v226 offset:16384\n"
    "ds_read_b64_tr_b16 v[74:75], v226 offset:16896\n"
    "ds_read_b64_tr_b16 v[76:77], v227 offset:16384\n"
    "ds_read_b64_tr_b16 v[78:79], v227 offset:16896\n"
    "s_branch GOW%=\n"
    "H4%=:\n"
    "s_sleep 10\n"
    "H5%=:\n"
    "s_waitcnt vmcnt(4)\n"
    "ds_add_u32 v181, v180\n"
    "s_branch GOW%=\n"
    "FULL%=:\n"
    "ds_read_b64_tr_b16 v[0:1], v224\n"
    "ds_read_b64_tr_b16 v[2:3], v224 offset:512\n"
    "ds_read_b64_tr_b16 v[4:5], v225\n"
    "ds_read_b64_tr_b16 v[6:7], v225 offset:512\n"
    "ds_read_b64_tr_b16 v[8:9], v226\n"
    "ds_read_b64_tr_b16 v[10:11], v226 offset:512\n"
    "ds_read_b64_tr_b16 v[12:13], v227\n"
    "ds_read_b64_tr_b16 v[14:15], v227 offset:512\n"
    "ds_read_b64_tr_b16 v[16:17], v224 offset:4096\n"
    "ds_read_b64_tr_b16 v[18:19], v224 offset:4608\n"
    "ds_read_b64_tr_b16 v[20:21], v225 offset:4096\n"
    "ds_read_b64_tr_b16 v[22:23], v225 offset:4608\n"
    "ds_read_b64_tr_b16 v[24:25], v226 offset:4096\n"
    "ds_read_b64_tr_b16 v[26:27], v226 offset:4608\n"
    "ds_read_b64_tr_b16 v[28:29], v227 offset:4096\n"
    "ds_read_b64_tr_b16 v[30:31], v227 offset:4608\n"
    "ds_read_b64_tr_b16 v[32:33], v224 offset:8192\n"
    "ds_read_b64_tr_b16 v[34:35], v224 offset:8704\n"
    "ds_read_b64_tr_b16 v[36:37], v225 offset:8192\n"
    "ds_read_b64_tr_b16 v[38:39], v225 offset:8704\n"
    "ds_read_b64_tr_b16 v[40:41], v226 offset:8192\n"
    "ds_read_b64_tr_b16 v[42:43], v226 offset:8704\n"
    "ds_read_b64_tr_b16 v[44:45], v227 offset:8192\n"
    "ds_read_b64_tr_b16 v[46:47], v227 offset:8704\n"
    "ds_read_b64_tr_b16 v[48:49], v224 offset:12288\n"
    "ds_read_b64_tr_b16 v[50:51], v224 offset:12800\n"
    "ds_read_b64_tr_b16 v[52:53], v225 offset:12288\n"
    "ds_read_b64_tr_b16 v[54:55], v225 offset:12800\n"
    "ds_read_b64_tr_b16 v[56:57], v226 offset:12288\n"
    "ds_read_b64_tr_b16 v[58:59], v226 offset:12800\n"
    "ds_read_b64_tr_b16 v[60:61], v227 offset:12288\n"
    "ds_read_b64_tr_b16 v[62:63], v227 offset:12800\n"
    "ds_read_b64_tr_b16 v[64:65], v224 offset:16384\n"
    "ds_read_b64_tr_b16 v[66:67], v224 offset:16896\n"
    "ds_read_b64_tr_b16 v[68:69], v225 offset:16384\n"
    "ds_read_b64_tr_b16 v[70:71], v225 offset:16896\n"
    "ds_read_b64_tr_b16 v[72:73], v226 offset:16384\n"
    "ds_read_b64_tr_b16 v[74:75], v226 offset:16896\n"
    "ds_read_b64_tr_b16 v[76:77], v227 offset:16384\n"
    "ds_read_b64_tr_b16 v[78:79], v227 offset:16896\n"
    "v_mov_b32 v140, 156544\n"
    "ds_read_b128 v[144:147], v140\n"
    "s_add_u32 s8, s75, 0\n"
    "s_add_u32 s9, s75, 7680\n"
    "s_add_u32 s10, s75, 61440\n"
    "s_add_u32 s11, s75, 15360\n"
    "s_add_u32 s12, s75, 23040\n"
    "s_add_u32 s13, s75, 76800\n"
    "s_add_u32 s14, s76, 0\n"
    "s_add_u32 s15, s76, 7680\n"
    "s_add_u32 s16, s76, 61440\n"
    "s_add_u32 s17, s76, 15360\n"
    "s_add_u32 s18, s76, 23040\n"
    "s_add_u32 s19, s76, 76800\n"
    "s_waitcnt lgkmcnt(0)\n"
    "v_readfirstlane_b32 s20, v144\n"
    "v_readfirstlane_b32 s21, v146\n"
    "s_mov_b32 s3, 16777216\n"
    "v_mov_b32 v141, 156708\n"
    "RXF%=:\n"
    "ds_read_b32 v140, v141\n"
    "s_waitcnt lgkmcnt(0)\n"
    "v_cmp_ne_u32 vcc, 0, v140\n"
    "s_cbranch_vccnz RXFD%=\n"
    "s_sleep 1\n"
    "s_sub_u32 s3, s3, 1\n"
    "s_cbranch_scc0 RXF%=\n"
    "RXFD%=:\n"
    "v_readfirstlane_b32 s34, v140\n"
    "s_nop 0\n"
    "s_add_u32 s2, s34, 195\n"
    "U0%=:\n"
    "s_memrealtime s[0:1]\n"
    "s_waitcnt lgkmcnt(0)\n"
    "s_sub_u32 s0, s0, s2\n"
    "s_cmp_lt_i32 s0, 0\n"
    "s_cbranch_scc1 U0%=\n"
    "buffer_load_dwordx4 v[80:83], v204, s[48:51], s8 offen sc1\n"
    "buffer_load_dwordx4 v[84:87], v204, s[48:51], s8 offen offset:3840 sc1\n"
    "buffer_load_dwordx4 v[88:91], v204, s[48:51], s9 offen sc1\n"
    "buffer_load_dwordx4 v[92:95], v204, s[48:51], s9 offen offset:3840 sc1\n"
    "buffer_load_dwordx2 v[96:97], v205, s[48:51], s8 offen sc1\n"
    "buffer_load_dwordx2 v[98:99], v205, s[48:51], s10 offen sc1\n"
    "buffer_load_dwordx4 v[120:123], v204, s[48:51], s11 offen sc1\n"
    "buffer_load_dwordx4 v[124:127], v204, s[48:51], s11 offen offset:3840 sc1\n"
    "buffer_load_dwordx4 v[128:131], v204, s[48:51], s12 offen sc1\n"
    "buffer_load_dwordx4 v[132:135], v204, s[48:51], s12 offen offset:3840 sc1\n"
    "buffer_load_dwordx2 v[136:137], v205, s[48:51], s11 offen sc1\n"
    "buffer_load_dwordx2 v[138:139], v205, s[48:51], s13 offen sc1\n"
    "s_cmp_eq_u32 s69, 0\n"
    "s_cbranch_scc1 W0A%=\n"
    "buffer_load_dwordx4 v[100:103], v204, s[48:51], s14 offen sc1\n"
    "buffer_load_dwordx4 v[104:107], v204, s[48:51], s14 offen offset:3840 sc1\n"
    "buffer_load_dwordx4 v[108:111], v204, s[48:51], s15 offen sc1\n"
    "buffer_load_dwordx4 v[112:115], v204, s[48:51], s15 offen offset:3840 sc1\n"
    "buffer_load_dwordx2 v[116:117], v205, s[48:51], s14 offen sc1\n"
    "buffer_load_dwordx2 v[118:119], v205, s[48:51], s16 offen sc1\n"
    "s_waitcnt vmcnt(12)\n"
    "s_branch W0B%=\n"
    "W0A%=:\n"
    "s_waitcnt vmcnt(6)\n"
    "W0B%=:\n"
    "v_max3_u32 v180, v80, v81, v81\n"
    "v_max3_u32 v180, v82, v83, v180\n"
    "v_max3_u32 v180, v84, v85, v180\n"
    "v_max3_u32 v180, v86, v87, v180\n"
    "v_max3_u32 v180, v88, v89, v180\n"
    "v_max3_u32 v180, v90, v91, v180\n"
    "v_max3_u32 v180, v92, v93, v180\n"
    "v_max3_u32 v180, v94, v95, v180\n"
    "v_max3_u32 v180, v96, v97, v180\n"
    "v_max3_u32 v180, v98, v99, v180\n"
    "v_cmp_eq_u32 vcc, -1, v180\n"
    "s_cbranch_vccnz FIX0%=\n"
    "OK0%=:\n"
    "v_mfma_f32_16x16x32_bf16 v[160:163], v[0:3], v[80:83], 0\n"
    "v_mfma_f32_16x16x32_bf16 v[164:167], v[4:7], v[80:83], 0\n"
    "ds_read_b64_tr_b16 v[0:1], v224 offset:20480\n"
    "ds_read_b64_tr_b16 v[2:3], v224 offset:20992\n"
    "v_mfma_f32_16x16x32_bf16 v[168:171], v[8:11], v[80:83], 0\n"
    "ds_read_b64_tr_b16 v[4:5], v225 offset:20480\n"
    "ds_read_b64_tr_b16 v[6:7], v225 offset:20992\n"
    "v_mfma_f32_16x16x32_bf16 v[172:175], v[12:15], v[80:83], 0\n"
    "ds_read_b64_tr_b16 v[8:9], v226 offset:20480\n"
    "ds_read_b64_tr_b16 v[10:11], v226 offset:20992\n"
    "v_mfma_f32_16x16x32_bf16 v[160:163], v[16:19], v[84:87], v[160:163]\n"
    "ds_read_b64_tr_b16 v[12:13], v227 offset:20480\n"
    "ds_read_b64_tr_b16 v[14:15], v227 offset:20992\n"
    "v_mfma_f32_16x16x32_bf16 v[164:167], v[20:23], v[84:87], v[164:167]\n"
    "ds_read_b64_tr_b16 v[16:17], v224 offset:24576\n"
    "ds_read_b64_tr_b16 v[18:19], v224 offset:25088\n"
    "v_mfma_f32_16x16x32_bf16 v[168:171], v[24:27], v[84:87], v[168:171]\n"
    "ds_read_b64_tr_b16 v[20:21], v225 offset:24576\n"
    "ds_read_b64_tr_b16 v[22:23], v225 offset:25088\n"
    "v_mfma_f32_16x16x32_bf16 v[172:175], v[28:31], v[84:87], v[172:175]\n"
    "ds_read_b64_tr_b16 v[24:25], v226 offset:24576\n"
    "ds_read_b64_tr_b16 v[26:27], v226 offset:25088\n"
    "v_mfma_f32_16x16x32_bf16 v[160:163], v[32:35], v[88:91], v[160:163]\n"
    "ds_read_b64_tr_b16 v[28:29], v227 offset:24576\n"
    "ds_read_b64_tr_b16 v[30:31], v227 offset:25088\n"
    "v_mfma_f32_16x16x32_bf16 v[164:167], v[36:39], v[88:91], v[164:167]\n"
    "ds_read_b64_tr_b16 v[32:33], v224 offset:28672\n"
    "ds_read_b64_tr_b16 v[34:35], v224 offset:29184\n"
    "v_mfma_f32_16x16x32_bf16 v[168:171], v[40:43], v[88:91], v[168:171]\n"
    "ds_read_b64_tr_b16 v[36:37], v225 offset:28672\n"
    "ds_read_b64_tr_b16 v[38:39], v225 offset:29184\n"
    "v_mfma_f32_16x16x32_bf16 v[172:175], v[44:47], v[88:91], v[172:175]\n"
    "ds_read_b64_tr_b16 v[40:41], v226 offset:28672\n"
    "ds_read_b64_tr_b16 v[42:43], v226 offset:29184\n"
    "v_mfma_f32_16x16x32_bf16 v[160:163], v[48:51], v[92:95], v[160:163]\n"
    "ds_read_b64_tr_b16 v[44:45], v227 offset:28672\n"
    "ds_read_b64_tr_b16 v[46:47], v227 offset:29184\n"
    "v_mfma_f32_16x16x32_bf16 v[164:167], v[52:55], v[92:95], v[164:167]\n"
    "ds_read_b64_tr_b16 v[48:49], v224 offset:32768\n"
    "ds_read_b64_tr_b16 v[50:51], v224 offset:33280\n"
    "v_mfma_f32_16x16x32_bf16 v[168:171], v[56:59], v[92:95], v[168:171]\n"
    "ds_read_b64_tr_b16 v[52:53], v225 offset:32768\n"
    "ds_read_b64_tr_b16 v[54:55], v225 offset:33280\n"
    "v_mfma_f32_16x16x32_bf16 v[172:175], v[60:63], v[92:95], v[172:175]\n"
    "ds_read_b64_tr_b16 v[56:57], v226 offset:32768\n"
    "ds_read_b64_tr_b16 v[58:59], v226 offset:33280\n"
    "v_mfma_f32_16x16x32_bf16 v[160:163], v[64:67], v[96:99], v[160:163]\n"
    "ds_read_b64_tr_b16 v[60:61], v227 offset:32768\n"
    "ds_read_b64_tr_b16 v[62:63], v227 offset:33280\n"
    "v_mfma_f32_16x16x32_bf16 v[164:167], v[68:71], v[96:99], v[164:167]\n"
    "ds_read_b64_tr_b16 v[64:65], v224 offset:36864\n"
    "ds_read_b64_tr_b16 v[66:67], v224 offset:37376\n"
    "v_mfma_f32_16x16x32_bf16 v[168:171], v[72:75], v[96:99], v[168:171]\n"
    "ds_read_b64_tr_b16 v[68:69], v225 offset:36864\n"
    "ds_read_b64_tr_b16 v[70:71], v225 offset:37376\n"
    "v_mfma_f32_16x16x32_bf16 v[172:175], v[76:79], v[96:99], v[172:175]\n"
    "ds_read_b64_tr_b16 v[72:73], v226 offset:36864\n"
    "ds_read_b64_tr_b16 v[74:75], v226 offset:37376\n"
    "ds_read_b64_tr_b16 v[76:77], v227 offset:36864\n"
    "ds_read_b64_tr_b16 v[78:79], v227 offset:37376\n"
    "s_cmp_eq_u32 s69, 0\n"
    "s_cbranch_scc1 W0C%=\n"
    "s_waitcnt vmcnt(6)\n"
    "s_branch W0D%=\n"
    "W0C%=:\n"
    "s_waitcnt vmcnt(0)\n"
    "W0D%=:\n"
    "v_max3_u32 v180, v120, v121, v121\n"
    "v_max3_u32 v180, v122, v123, v180\n"
    "v_max3_u32 v180, v124, v125, v180\n"
    "v_max3_u32 v180, v126, v127, v180\n"
    "v_max3_u32 v180, v128, v129, v180\n"
    "v_max3_u32 v180, v130, v131, v180\n"
    "v_max3_u32 v180, v132, v133, v180\n"
    "v_max3_u32 v180, v134, v135, v180\n"
    "v_max3_u32 v180, v136, v137, v180\n"
    "v_max3_u32 v180, v138, v139, v180\n"
    "v_cmp_eq_u32 vcc, -1, v180\n"
    "s_cbranch_vccnz FIX1%=\n"
    "OK1%=:\n"
    "s_waitcnt lgkmcnt(14)\n"
    "v_mfma_f32_16x16x32_bf16 v[160:163], v[0:3], v[120:123], v[160:163]\n"
    "v_add_u32 v224, 40960, v224\n"
    "v_mfma_f32_16x16x32_bf16 v[164:167], v[4:7], v[120:123], v[164:167]\n"
    "v_add_u32 v225, 40960, v225\n"
    "ds_read_b64_tr_b16 v[0:1], v224\n"
    "ds_read_b64_tr_b16 v[2:3], v224 offset:512\n"
    "v_mfma_f32_16x16x32_bf16 v[168:171], v[8:11], v[120:123], v[168:171]\n"
    "v_add_u32 v226, 40960, v226\n"
    "ds_read_b64_tr_b16 v[4:5], v225\n"
    "ds_read_b64_tr_b16 v[6:7], v225 offset:512\n"
    "v_mfma_f32_16x16x32_bf16 v[172:175], v[12:15], v[120:123], v[172:175]\n"
    "v_add_u32 v227, 40960, v227\n"
    "ds_read_b64_tr_b16 v[8:9], v226\n"
    "ds_read_b64_tr_b16 v[10:11], v226 offset:512\n"
    "s_waitcnt lgkmcnt(14)\n"
    "v_mfma_f32_16x16x32_bf16 v[160:163], v[16:19], v[124:127], v[160:163]\n"
    "ds_read_b64_tr_b16 v[12:13], v227\n"
    "ds_read_b64_tr_b16 v[14:15], v227 offset:512\n"
    "v_mfma_f32_16x16x32_bf16 v[164:167], v[20:23], v[124:127], v[164:167]\n"
    "ds_read_b64_tr_b16 v[16:17], v224 offset:4096\n"
    "ds_read_b64_tr_b16 v[18:19], v224 offset:4608\n"
    "v_mfma_f32_16x16x32_bf16 v[168:171], v[24:27], v[124:127], v[168:171]\n"
    "ds_read_b64_tr_b16 v[20:21], v225 offset:4096\n"
    "ds_read_b64_tr_b16 v[22:23], v225 offset:4608\n"
    "v_mfma_f32_16x16x32_bf16 v[172:175], v[28:31], v[124:127], v[172:175]\n"
    "ds_read_b64_tr_b16 v[24:25], v226 offset:4096\n"
    "ds_read_b64_tr_b16 v[26:27], v226 offset:4608\n"
    "s_waitcnt lgkmcnt(14)\n"
    "v_mfma_f32_16x16x32_bf16 v[160:163], v[32:35], v[128:131], v[160:163]\n"
    "ds_read_b64_tr_b16 v[28:29], v227 offset:4096\n"
    "ds_read_b64_tr_b16 v[30:31], v227 offset:4608\n"
    "v_mfma_f32_16x16x32_bf16 v[164:167], v[36:39], v[128:131], v[164:167]\n"
    "ds_read_b64_tr_b16 v[32:33], v224 offset:8192\n"
    "ds_read_b64_tr_b16 v[34:35], v224 offset:8704\n"
    "v_mfma_f32_16x16x32_bf16 v[168:171], v[40:43], v[128:131], v[168:171]\n"
    "ds_read_b64_tr_b16 v[36:37], v225 offset:8192\n"
    "ds_read_b64_tr_b16 v[38:39], v225 offset:8704\n"
    "v_mfma_f32_16x16x32_bf16 v[172:175], v[44:47], v[128:131], v[172:175]\n"
    "ds_read_b64_tr_b16 v[40:41], v226 offset:8192\n"
    "ds_read_b64_tr_b16 v[42:43], v226 offset:8704\n"
    "s_waitcnt lgkmcnt(14)\n"
    "v_mfma_f32_16x16x32_bf16 v[160:163], v[48:51], v[132:135], v[160:163]\n"
    "ds_read_b64_tr_b16 v[44:45], v227 offset:8192\n"
    "ds_read_b64_tr_b16 v[46:47], v227 offset:8704\n"
    "v_mfma_f32_16x16x32_bf16 v[164:167], v[52:55], v[132:135], v[164:167]\n"
    "ds_read_b64_tr_b16 v[48:49], v224 offset:12288\n"
    "ds_read_b64_tr_b16 v[50:51], v224 offset:12800\n"
    "v_mfma_f32_16x16x32_bf16 v[168:171], v[56:59], v[132:135], v[168:171]\n"
    "ds_read_b64_tr_b16 v[52:53], v225 offset:12288\n"
    "ds_read_b64_tr_b16 v[54:55], v225 offset:12800\n"
    "v_mfma_f32_16x16x32_bf16 v[172:175], v[60:63], v[132:135], v[172:175]\n"
    "ds_read_b64_tr_b16 v[56:57], v226 offset:12288\n"
    "ds_read_b64_tr_b16 v[58:59], v226 offset:12800\n"
    "s_waitcnt lgkmcnt(14)\n"
    "v_mfma_f32_16x16x32_bf16 v[160:163], v[64:67], v[136:139], v[160:163]\n"
    "ds_read_b64_tr_b16 v[60:61], v227 offset:12288\n"
    "ds_read_b64_tr_b16 v[62:63], v227 offset:12800\n"
    "v_mfma_f32_16x16x32_bf16 v[164:167], v[68:71], v[136:139], v[164:167]\n"
    "ds_read_b64_tr_b16 v[64:65], v224 offset:16384\n"
    "ds_read_b64_tr_b16 v[66:67], v224 offset:16896\n"
    "v_mfma_f32_16x16x32_bf16 v[168:171], v[72:75], v[136:139], v[168:171]\n"
    "ds_read_b64_tr_b16 v[68:69], v225 offset:16384\n"
    "ds_read_b64_tr_b16 v[70:71], v225 offset:16896\n"
    "v_mfma_f32_16x16x32_bf16 v[172:175], v[76:79], v[136:139], v[172:175]\n"
    "ds_read_b64_tr_b16 v[72:73], v226 offset:16384\n"
    "ds_read_b64_tr_b16 v[74:75], v226 offset:16896\n"
    "ds_read_b64_tr_b16 v[76:77], v227 offset:16384\n"
    "ds_read_b64_tr_b16 v[78:79], v227 offset:16896\n"
    "GOW%=:\n"
    "s_mov_b32 s3, 16777216\n"
    "v_mov_b32 v181, 156700\n"
    "GOL%=:\n"
    "ds_read_b32 v180, v181\n"
    "s_waitcnt lgkmcnt(0)\n"
    "v_cmp_le_u32 vcc, 256, v180\n"
    "s_cbranch_vccnz GOLD%=\n"
    "s_sleep 1\n"
    "s_sub_u32 s3, s3, 1\n"
    "s_cbranch_scc0 GOL%=\n"
    "GOLD%=:\n"
    "s_bitcmp1_b32 49, s69\n"
    "s_cbranch_scc0 H6%=\n"
    "buffer_load_dwordx4 v[100:103], v204, s[48:51], s14 offen sc1\n"
    "buffer_load_dwordx4 v[104:107], v204, s[48:51], s14 offen offset:3840 sc1\n"
    "buffer_load_dwordx4 v[108:111], v204, s[48:51], s15 offen sc1\n"
    "buffer_load_dwordx4 v[112:115], v204, s[48:51], s15 offen offset:3840 sc1\n"
    "buffer_load_dwordx2 v[116:117], v205, s[48:51], s14 offen sc1\n"
    "buffer_load_dwordx2 v[118:119], v205, s[48:51], s16 offen sc1\n"
    "H6%=:\n"
    "buffer_load_dwordx4 v[140:143], v204, s[48:51], s17 offen sc1\n"
    "buffer_load_dwordx4 v[144:147], v204, s[48:51], s17 offen offset:3840 sc1\n"
    "buffer_load_dwordx4 v[148:151], v204, s[48:51], s18 offen sc1\n"
    "buffer_load_dwordx4 v[152:155], v204, s[48:51], s18 offen offset:3840 sc1\n"
    "buffer_load_dwordx2 v[156:157], v205, s[48:51], s17 offen sc1\n"
    "buffer_load_dwordx2 v[158:159], v205, s[48:51], s19 offen sc1\n"
    "COPYB%=:\n"
    "s_andn2_b32 s24, 2047, s20\n"
    "s_lshr_b32 s22, s24, 1\n"
    "s_sub_u32 s22, s22, s82\n"
    "s_and_b32 s24, s24, 1\n"
    "s_cmp_eq_u32 s24, s81\n"
    "s_cselect_b32 s22, s22, -1\n"
    "s_andn2_b32 s24, 2047, s21\n"
    "s_lshr_b32 s23, s24, 1\n"
    "s_sub_u32 s23, s23, s82\n"
    "s_and_b32 s24, s24, 1\n"
    "s_cmp_eq_u32 s24, s81\n"
    "s_cselect_b32 s23, s23, -1\n"
    "ds_read_b32 v176, v234\n"
    "ds_read_b64 v[192:193], v228\n"
    "ds_read_b64 v[194:195], v229\n"
    "ds_read_b64 v[196:197], v230\n"
    "ds_read_b64 v[198:199], v231\n"
    "s_waitcnt lgkmcnt(4)\n"
    "v_sub_f32 v178, 1.0, v176\n"
    "s_waitcnt lgkmcnt(3)\n"
    "v_lshlrev_b32 v208, 16, v192\n"
    "v_and_b32 v209, 0xffff0000, v192\n"
    "v_pk_mul_f32 v[208:209], v[208:209], v[178:179] op_sel_hi:[1,0]\n"
    "v_lshlrev_b32 v210, 16, v193\n"
    "v_and_b32 v211, 0xffff0000, v193\n"
    "v_pk_mul_f32 v[210:211], v[210:211], v[178:179] op_sel_hi:[1,0]\n"
    "s_waitcnt lgkmcnt(2)\n"
    "v_lshlrev_b32 v212, 16, v194\n"
    "v_and_b32 v213, 0xffff0000, v194\n"
    "v_pk_mul_f32 v[212:213], v[212:213], v[178:179] op_sel_hi:[1,0]\n"
    "v_lshlrev_b32 v214, 16, v195\n"
    "v_and_b32 v215, 0xffff0000, v195\n"
    "v_pk_mul_f32 v[214:215], v[214:215], v[178:179] op_sel_hi:[1,0]\n"
    "s_waitcnt lgkmcnt(1)\n"
    "v_lshlrev_b32 v216, 16, v196\n"
    "v_and_b32 v217, 0xffff0000, v196\n"
    "v_pk_mul_f32 v[216:217], v[216:217], v[178:179] op_sel_hi:[1,0]\n"
    "v_lshlrev_b32 v218, 16, v197\n"
    "v_and_b32 v219, 0xffff0000, v197\n"
    "v_pk_mul_f32 v[218:219], v[218:219], v[178:179] op_sel_hi:[1,0]\n"
    "s_waitcnt lgkmcnt(0)\n"
    "v_lshlrev_b32 v220, 16, v198\n"
    "v_and_b32 v221, 0xffff0000, v198\n"
    "v_pk_mul_f32 v[220:221], v[220:221], v[178:179] op_sel_hi:[1,0]\n"
    "v_lshlrev_b32 v222, 16, v199\n"
    "v_and_b32 v223, 0xffff0000, v199\n"
    "v_pk_mul_f32 v[222:223], v[222:223], v[178:179] op_sel_hi:[1,0]\n"
    "s_min_u32 s25, s22, s23\n"
    "s_waitcnt vmcnt(6)\n"
    "COPYC%=:\n"
    "v_max3_u32 v180, v100, v101, v101\n"
    "v_max3_u32 v180, v102, v103, v180\n"
    "v_max3_u32 v180, v104, v105, v180\n"
    "v_max3_u32 v180, v106, v107, v180\n"
    "v_max3_u32 v180, v108, v109, v180\n"
    "v_max3_u32 v180, v110, v111, v180\n"
    "v_max3_u32 v180, v112, v113, v180\n"
    "v_max3_u32 v180, v114, v115, v180\n"
    "v_max3_u32 v180, v116, v117, v180\n"
    "v_max3_u32 v180, v118, v119, v180\n"
    "v_cmp_eq_u32 vcc, -1, v180\n"
    "s_cbranch_vccnz FIX2%=\n"
    "OK2%=:\n"
    "v_mfma_f32_16x16x32_bf16 v[160:163], v[0:3], v[100:103], v[160:163]\n"
    "v_mfma_f32_16x16x32_bf16 v[164:167], v[4:7], v[100:103], v[164:167]\n"
    "ds_read_b64_tr_b16 v[0:1], v224 offset:20480\n"
    "ds_read_b64_tr_b16 v[2:3], v224 offset:20992\n"
    "v_mfma_f32_16x16x32_bf16 v[168:171], v[8:11], v[100:103], v[168:171]\n"
    "ds_read_b64_tr_b16 v[4:5], v225 offset:20480\n"
    "ds_read_b64_tr_b16 v[6:7], v225 offset:20992\n"
    "v_mfma_f32_16x16x32_bf16 v[172:175], v[12:15], v[100:103], v[172:175]\n"
    "ds_read_b64_tr_b16 v[8:9], v226 offset:20480\n"
    "ds_read_b64_tr_b16 v[10:11], v226 offset:20992\n"
    "v_mfma_f32_16x16x32_bf16 v[160:163], v[16:19], v[104:107], v[160:163]\n"
    "ds_read_b64_tr_b16 v[12:13], v227 offset:20480\n"
    "ds_read_b64_tr_b16 v[14:15], v227 offset:20992\n"
    "v_mfma_f32_16x16x32_bf16 v[164:167], v[20:23], v[104:107], v[164:167]\n"
    "ds_read_b64_tr_b16 v[16:17], v224 offset:24576\n"
    "ds_read_b64_tr_b16 v[18:19], v224 offset:25088\n"
    "v_mfma_f32_16x16x32_bf16 v[168:171], v[24:27], v[104:107], v[168:171]\n"
    "ds_read_b64_tr_b16 v[20:21], v225 offset:24576\n"
    "ds_read_b64_tr_b16 v[22:23], v225 offset:25088\n"
    "v_mfma_f32_16x16x32_bf16 v[172:175], v[28:31], v[104:107], v[172:175]\n"
    "ds_read_b64_tr_b16 v[24:25], v226 offset:24576\n"
    "ds_read_b64_tr_b16 v[26:27], v226 offset:25088\n"
    "v_mfma_f32_16x16x32_bf16 v[160:163], v[32:35], v[108:111], v[160:163]\n"
    "ds_read_b64_tr_b16 v[28:29], v227 offset:24576\n"
    "ds_read_b64_tr_b16 v[30:31], v227 offset:25088\n"
    "v_mfma_f32_16x16x32_bf16 v[164:167], v[36:39], v[108:111], v[164:167]\n"
    "ds_read_b64_tr_b16 v[32:33], v224 offset:28672\n"
    "ds_read_b64_tr_b16 v[34:35], v224 offset:29184\n"
    "v_mfma_f32_16x16x32_bf16 v[168:171], v[40:43], v[108:111], v[168:171]\n"
    "ds_read_b64_tr_b16 v[36:37], v225 offset:28672\n"
    "ds_read_b64_tr_b16 v[38:39], v225 offset:29184\n"
    "v_mfma_f32_16x16x32_bf16 v[172:175], v[44:47], v[108:111], v[172:175]\n"
    "ds_read_b64_tr_b16 v[40:41], v226 offset:28672\n"
    "ds_read_b64_tr_b16 v[42:43], v226 offset:29184\n"
    "s_waitcnt lgkmcnt(14)\n"
    "v_mfma_f32_16x16x32_bf16 v[160:163], v[48:51], v[112:115], v[160:163]\n"
    "ds_read_b64_tr_b16 v[44:45], v227 offset:28672\n"
    "ds_read_b64_tr_b16 v[46:47], v227 offset:29184\n"
    "v_mfma_f32_16x16x32_bf16 v[164:167], v[52:55], v[112:115], v[164:167]\n"
    "ds_read_b64_tr_b16 v[48:49], v224 offset:32768\n"
    "ds_read_b64_tr_b16 v[50:51], v224 offset:33280\n"
    "v_mfma_f32_16x16x32_bf16 v[168:171], v[56:59], v[112:115], v[168:171]\n"
    "ds_read_b64_tr_b16 v[52:53], v225 offset:32768\n"
    "ds_read_b64_tr_b16 v[54:55], v225 offset:33280\n"
    "v_mfma_f32_16x16x32_bf16 v[172:175], v[60:63], v[112:115], v[172:175]\n"
    "ds_read_b64_tr_b16 v[56:57], v226 offset:32768\n"
    "ds_read_b64_tr_b16 v[58:59], v226 offset:33280\n"
    "s_waitcnt lgkmcnt(14)\n"
    "v_mfma_f32_16x16x32_bf16 v[160:163], v[64:67], v[116:119], v[160:163]\n"
    "ds_read_b64_tr_b16 v[60:61], v227 offset:32768\n"
    "ds_read_b64_tr_b16 v[62:63], v227 offset:33280\n"
    "v_mfma_f32_16x16x32_bf16 v[164:167], v[68:71], v[116:119], v[164:167]\n"
    "ds_read_b64_tr_b16 v[64:65], v224 offset:36864\n"
    "ds_read_b64_tr_b16 v[66:67], v224 offset:37376\n"
    "v_mfma_f32_16x16x32_bf16 v[168:171], v[72:75], v[116:119], v[168:171]\n"
    "ds_read_b64_tr_b16 v[68:69], v225 offset:36864\n"
    "ds_read_b64_tr_b16 v[70:71], v225 offset:37376\n"
    "v_mfma_f32_16x16x32_bf16 v[172:175], v[76:79], v[116:119], v[172:175]\n"
    "ds_read_b64_tr_b16 v[72:73], v226 offset:36864\n"
    "ds_read_b64_tr_b16 v[74:75], v226 offset:37376\n"
    "ds_read_b64_tr_b16 v[76:77], v227 offset:36864\n"
    "ds_read_b64_tr_b16 v[78:79], v227 offset:37376\n"
    "s_cmp_lt_u32 s25, 96\n"
    "s_cbranch_scc1 YW%=\n"
    "YBACK%=:\n"
    "s_waitcnt vmcnt(3)\n"
    "v_max3_u32 v180, v140, v141, v141\n"
    "v_max3_u32 v180, v142, v143, v180\n"
    "v_max3_u32 v180, v144, v145, v180\n"
    "v_max3_u32 v180, v146, v147, v180\n"
    "v_max3_u32 v180, v148, v149, v180\n"
    "v_max3_u32 v180, v150, v151, v180\n"
    "v_cmp_eq_u32 vcc, -1, v180\n"
    "s_cbranch_vccnz FIX3a%=\n"
    "OK3a%=:\n"
    "s_waitcnt lgkmcnt(14)\n"
    "v_mfma_f32_16x16x32_bf16 v[160:163], v[0:3], v[140:143], v[160:163]\n"
    "v_mfma_f32_16x16x32_bf16 v[164:167], v[4:7], v[140:143], v[164:167]\n"
    "v_mfma_f32_16x16x32_bf16 v[168:171], v[8:11], v[140:143], v[168:171]\n"
    "v_mfma_f32_16x16x32_bf16 v[172:175], v[12:15], v[140:143], v[172:175]\n"
    "v_mfma_f32_16x16x32_bf16 v[160:163], v[16:19], v[144:147], v[160:163]\n"
    "v_mfma_f32_16x16x32_bf16 v[164:167], v[20:23], v[144:147], v[164:167]\n"
    "s_waitcnt vmcnt(0)\n"
    "v_mfma_f32_16x16x32_bf16 v[168:171], v[24:27], v[144:147], v[168:171]\n"
    "v_max3_u32 v180, v152, v153, v153\n"
    "v_mfma_f32_16x16x32_bf16 v[172:175], v[28:31], v[144:147], v[172:175]\n"
    "v_max3_u32 v180, v154, v155, v180\n"
    "v_mfma_f32_16x16x32_bf16 v[160:163], v[32:35], v[148:151], v[160:163]\n"
    "v_max3_u32 v180, v156, v157, v180\n"
    "v_mfma_f32_16x16x32_bf16 v[164:167], v[36:39], v[148:151], v[164:167]\n"
    "v_max3_u32 v180, v158, v159, v180\n"
    "v_mfma_f32_16x16x32_bf16 v[168:171], v[40:43], v[148:151], v[168:171]\n"
    "v_cmp_eq_u32 vcc, -1, v180\n"
    "v_mfma_f32_16x16x32_bf16 v[172:175], v[44:47], v[148:151], v[172:175]\n"
    "s_cbranch_vccnz FIX3b%=\n"
    "OK3b%=:\n"
    "s_waitcnt lgkmcnt(8)\n"
    "v_mfma_f32_16x16x32_bf16 v[160:163], v[48:51], v[152:155], v[160:163]\n"
    "v_mfma_f32_16x16x32_bf16 v[164:167], v[52:55], v[152:155], v[164:167]\n"
    "v_mfma_f32_16x16x32_bf16 v[168:171], v[56:59], v[152:155], v[168:171]\n"
    "v_mfma_f32_16x16x32_bf16 v[172:175], v[60:63], v[152:155], v[172:175]\n"
    "s_waitcnt lgkmcnt(0)\n"
    "v_mfma_f32_16x16x32_bf16 v[160:163], v[64:67], v[156:159], v[160:163]\n"
    "v_mfma_f32_16x16x32_bf16 v[164:167], v[68:71], v[156:159], v[164:167]\n"
    "v_mfma_f32_16x16x32_bf16 v[168:171], v[72:75], v[156:159], v[168:171]\n"
    "v_mfma_f32_16x16x32_bf16 v[172:175], v[76:79], v[156:159], v[172:175]\n"
    "s_cmp_ge_u32 s69, 4\n"
    "s_cbranch_scc1 EPH%=\n"
    "s_nop 2\n"
    "v_pk_fma_f32 v[160:161], v[160:161], v[176:177], v[208:209] op_sel_hi:[1,0,1]\n"
    "v_pk_fma_f32 v[162:163], v[162:163], v[176:177], v[210:211] op_sel_hi:[1,0,1]\n"
    "v_cvt_pk_bf16_f32 v160, v160, v161\n"
    "v_cvt_pk_bf16_f32 v161, v162, v163\n"
    "ds_write_b64 v228, v[160:161]\n"
    "v_pk_fma_f32 v[164:165], v[164:165], v[176:177], v[212:213] op_sel_hi:[1,0,1]\n"
    "v_pk_fma_f32 v[166:167], v[166:167], v[176:177], v[214:215] op_sel_hi:[1,0,1]\n"
    "v_cvt_pk_bf16_f32 v164, v164, v165\n"
    "v_cvt_pk_bf16_f32 v165, v166, v167\n"
    "ds_write_b64 v229, v[164:165]\n"
    "v_pk_fma_f32 v[168:169], v[168:169], v[176:177], v[216:217] op_sel_hi:[1,0,1]\n"
    "v_pk_fma_f32 v[170:171], v[170:171], v[176:177], v[218:219] op_sel_hi:[1,0,1]\n"
    "v_cvt_pk_bf16_f32 v168, v168, v169\n"
    "v_cvt_pk_bf16_f32 v169, v170, v171\n"
    "ds_write_b64 v230, v[168:169]\n"
    "v_pk_fma_f32 v[172:173], v[172:173], v[176:177], v[220:221] op_sel_hi:[1,0,1]\n"
    "v_pk_fma_f32 v[174:175], v[174:175], v[176:177], v[222:223] op_sel_hi:[1,0,1]\n"
    "v_cvt_pk_bf16_f32 v172, v172, v173\n"
    "v_cvt_pk_bf16_f32 v173, v174, v175\n"
    "ds_write_b64 v231, v[172:173]\n"
    "ds_read_b128 v[0:3], v232\n"
    "ds_read_b128 v[4:7], v235\n"
    "s_movk_i32 s0, 20480\n"
    "s_waitcnt lgkmcnt(1)\n"
    "buffer_store_dwordx4 v[0:3], v233, s[56:59], 0 offen\n"
    "s_waitcnt lgkmcnt(0)\n"
    "buffer_store_dwordx4 v[4:7], v233, s[56:59], s0 offen\n"
    "s_endpgm\n"
    "EPH%=:\n"
    "s_lshl_b32 s0, s69, 2\n"
    "s_add_u32 s0, s0, 156664\n"
    "v_mov_b32 v0, s0\n"
    "v_mov_b32 v1, 1\n"
    "v_xor_b32 v2, 4, v0\n"
    "v_xor_b32 v3, 2048, v235\n"
    "ds_write_b128 v235, v[168:171]\n"
    "ds_write_b128 v235, v[172:175] offset:1024\n"
    "ds_write_b32 v0, v1\n"
    "s_mov_b32 s3, 16777216\n"
    "EXW%=:\n"
    "ds_read_b32 v4, v2\n"
    "ds_read_b128 v[8:11], v3\n"
    "ds_read_b128 v[12:15], v3 offset:1024\n"
    "s_waitcnt lgkmcnt(0)\n"
    "v_cmp_ne_u32 vcc, 0, v4\n"
    "s_cbranch_vccnz EXD%=\n"
    "s_sub_u32 s3, s3, 1\n"
    "s_cbranch_scc0 EXW%=\n"
    "EXD%=:\n"
    "v_pk_add_f32 v[160:161], v[160:161], v[8:9]\n"
    "v_pk_add_f32 v[162:163], v[162:163], v[10:11]\n"
    "v_pk_add_f32 v[164:165], v[164:165], v[12:13]\n"
    "v_pk_add_f32 v[166:167], v[166:167], v[14:15]\n"
    "v_pk_fma_f32 v[160:161], v[160:161], v[176:177], v[208:209] op_sel_hi:[1,0,1]\n"
    "v_pk_fma_f32 v[162:163], v[162:163], v[176:177], v[210:211] op_sel_hi:[1,0,1]\n"
    "v_cvt_pk_bf16_f32 v160, v160, v161\n"
    "v_cvt_pk_bf16_f32 v161, v162, v163\n"
    "ds_write_b64 v228, v[160:161]\n"
    "v_pk_fma_f32 v[164:165], v[164:165], v[176:177], v[212:213] op_sel_hi:[1,0,1]\n"
    "v_pk_fma_f32 v[166:167], v[166:167], v[176:177], v[214:215] op_sel_hi:[1,0,1]\n"
    "v_cvt_pk_bf16_f32 v164, v164, v165\n"
    "v_cvt_pk_bf16_f32 v165, v166, v167\n"
    "ds_write_b64 v229, v[164:165]\n"
    "ds_read_b128 v[4:7], v232\n"
    "s_waitcnt lgkmcnt(0)\n"
    "buffer_store_dwordx4 v[4:7], v233, s[56:59], 0 offen\n"
    "s_endpgm\n"
    "FIX0%=:\n"
    "s_mov_b32 s3, 262144\n"
    "s_nop 7\n"
    "FL0%=:\n"
    "buffer_load_dwordx4 v[80:83], v204, s[48:51], s8 offen sc1\n"
    "buffer_load_dwordx4 v[84:87], v204, s[48:51], s8 offen offset:3840 sc1\n"
    "buffer_load_dwordx4 v[88:91], v204, s[48:51], s9 offen sc1\n"
    "buffer_load_dwordx4 v[92:95], v204, s[48:51], s9 offen offset:3840 sc1\n"
    "buffer_load_dwordx2 v[96:97], v205, s[48:51], s8 offen sc1\n"
    "buffer_load_dwordx2 v[98:99], v205, s[48:51], s10 offen sc1\n"
    "s_waitcnt vmcnt(0)\n"
    "v_max3_u32 v180, v80, v81, v81\n"
    "v_max3_u32 v180, v82, v83, v180\n"
    "v_max3_u32 v180, v84, v85, v180\n"
    "v_max3_u32 v180, v86, v87, v180\n"
    "v_max3_u32 v180, v88, v89, v180\n"
    "v_max3_u32 v180, v90, v91, v180\n"
    "v_max3_u32 v180, v92, v93, v180\n"
    "v_max3_u32 v180, v94, v95, v180\n"
    "v_max3_u32 v180, v96, v97, v180\n"
    "v_max3_u32 v180, v98, v99, v180\n"
    "v_cmp_eq_u32 vcc, -1, v180\n"
    "s_cbranch_vccz OK0%=\n"
    "s_sub_u32 s3, s3, 1\n"
    "s_cbranch_scc0 FL0%=\n"
    "s_branch OK0%=\n"
    "FIX1%=:\n"
    "s_mov_b32 s3, 262144\n"
    "s_nop 7\n"
    "FL1%=:\n"
    "buffer_load_dwordx4 v[120:123], v204, s[48:51], s11 offen sc1\n"
    "buffer_load_dwordx4 v[124:127], v204, s[48:51], s11 offen offset:3840 sc1\n"
    "buffer_load_dwordx4 v[128:131], v204, s[48:51], s12 offen sc1\n"
    "buffer_load_dwordx4 v[132:135], v204, s[48:51], s12 offen offset:3840 sc1\n"
    "buffer_load_dwordx2 v[136:137], v205, s[48:51], s11 offen sc1\n"
    "buffer_load_dwordx2 v[138:139], v205, s[48:51], s13 offen sc1\n"
    "s_waitcnt vmcnt(0)\n"
    "v_max3_u32 v180, v120, v121, v121\n"
    "v_max3_u32 v180, v122, v123, v180\n"
    "v_max3_u32 v180, v124, v125, v180\n"
    "v_max3_u32 v180, v126, v127, v180\n"
    "v_max3_u32 v180, v128, v129, v180\n"
    "v_max3_u32 v180, v130, v131, v180\n"
    "v_max3_u32 v180, v132, v133, v180\n"
    "v_max3_u32 v180, v134, v135, v180\n"
    "v_max3_u32 v180, v136, v137, v180\n"
    "v_max3_u32 v180, v138, v139, v180\n"
    "v_cmp_eq_u32 vcc, -1, v180\n"
    "s_cbranch_vccz OK1%=\n"
    "s_sub_u32 s3, s3, 1\n"
    "s_cbranch_scc0 FL1%=\n"
    "s_branch OK1%=\n"
    "FIX2%=:\n"
    "s_mov_b32 s3, 262144\n"
    "s_nop 7\n"
    "FL2%=:\n"
    "buffer_load_dwordx4 v[100:103], v204, s[48:51], s14 offen sc1\n"
    "buffer_load_dwordx4 v[104:107], v204, s[48:51], s14 offen offset:3840 sc1\n"
    "buffer_load_dwordx4 v[108:111], v204, s[48:51], s15 offen sc1\n"
    "buffer_load_dwordx4 v[112:115], v204, s[48:51], s15 offen offset:3840 sc1\n"
    "buffer_load_dwordx2 v[116:117], v205, s[48:51], s14 offen sc1\n"
    "buffer_load_dwordx2 v[118:119], v205, s[48:51], s16 offen sc1\n"
    "s_waitcnt vmcnt(0)\n"
    "v_max3_u32 v180, v100, v101, v101\n"
    "v_max3_u32 v180, v102, v103, v180\n"
    "v_max3_u32 v180, v104, v105, v180\n"
    "v_max3_u32 v180, v106, v107, v180\n"
    "v_max3_u32 v180, v108, v109, v180\n"
    "v_max3_u32 v180, v110, v111, v180\n"
    "v_max3_u32 v180, v112, v113, v180\n"
    "v_max3_u32 v180, v114, v115, v180\n"
    "v_max3_u32 v180, v116, v117, v180\n"
    "v_max3_u32 v180, v118, v119, v180\n"
    "v_cmp_eq_u32 vcc, -1, v180\n"
    "s_cbranch_vccz OK2%=\n"
    "s_sub_u32 s3, s3, 1\n"
    "s_cbranch_scc0 FL2%=\n"
    "s_branch OK2%=\n"
    "FIX3a%=:\n"
    "s_mov_b32 s3, 262144\n"
    "s_nop 7\n"
    "FL3a%=:\n"
    "buffer_load_dwordx4 v[140:143], v204, s[48:51], s17 offen sc1\n"
    "buffer_load_dwordx4 v[144:147], v204, s[48:51], s17 offen offset:3840 sc1\n"
    "buffer_load_dwordx4 v[148:151], v204, s[48:51], s18 offen sc1\n"
    "buffer_load_dwordx4 v[152:155], v204, s[48:51], s18 offen offset:3840 sc1\n"
    "buffer_load_dwordx2 v[156:157], v205, s[48:51], s17 offen sc1\n"
    "buffer_load_dwordx2 v[158:159], v205, s[48:51], s19 offen sc1\n"
    "s_waitcnt vmcnt(0)\n"
    "v_max3_u32 v180, v140, v141, v141\n"
    "v_max3_u32 v180, v142, v143, v180\n"
    "v_max3_u32 v180, v144, v145, v180\n"
    "v_max3_u32 v180, v146, v147, v180\n"
    "v_max3_u32 v180, v148, v149, v180\n"
    "v_max3_u32 v180, v150, v151, v180\n"
    "v_max3_u32 v180, v152, v153, v180\n"
    "v_max3_u32 v180, v154, v155, v180\n"
    "v_max3_u32 v180, v156, v157, v180\n"
    "v_max3_u32 v180, v158, v159, v180\n"
    "v_cmp_eq_u32 vcc, -1, v180\n"
    "s_cbranch_vccz OK3a%=\n"
    "s_sub_u32 s3, s3, 1\n"
    "s_cbranch_scc0 FL3a%=\n"
    "s_branch OK3a%=\n"
    "FIX3b%=:\n"
    "s_mov_b32 s3, 262144\n"
    "s_nop 7\n"
    "FL3b%=:\n"
    "buffer_load_dwordx4 v[140:143], v204, s[48:51], s17 offen sc1\n"
    "buffer_load_dwordx4 v[144:147], v204, s[48:51], s17 offen offset:3840 sc1\n"
    "buffer_load_dwordx4 v[148:151], v204, s[48:51], s18 offen sc1\n"
    "buffer_load_dwordx4 v[152:155], v204, s[48:51], s18 offen offset:3840 sc1\n"
    "buffer_load_dwordx2 v[156:157], v205, s[48:51], s17 offen sc1\n"
    "buffer_load_dwordx2 v[158:159], v205, s[48:51], s19 offen sc1\n"
    "s_waitcnt vmcnt(0)\n"
    "v_max3_u32 v180, v140, v141, v141\n"
    "v_max3_u32 v180, v142, v143, v180\n"
    "v_max3_u32 v180, v144, v145, v180\n"
    "v_max3_u32 v180, v146, v147, v180\n"
    "v_max3_u32 v180, v148, v149, v180\n"
    "v_max3_u32 v180, v150, v151, v180\n"
    "v_max3_u32 v180, v152, v153, v180\n"
    "v_max3_u32 v180, v154, v155, v180\n"
    "v_max3_u32 v180, v156, v157, v180\n"
    "v_max3_u32 v180, v158, v159, v180\n"
    "v_cmp_eq_u32 vcc, -1, v180\n"
    "s_cbranch_vccz OK3b%=\n"
    "s_sub_u32 s3, s3, 1\n"
    "s_cbranch_scc0 FL3b%=\n"
    "s_branch OK3b%=\n"
    "YJOB%=:\n"
    "v_mbcnt_lo_u32_b32 v208, -1, 0\n"
    "v_mbcnt_hi_u32_b32 v208, -1, v208\n"
    "v_lshrrev_b32 v210, 5, v208\n"
    "v_mul_u32_u24 v210, 10240, v210\n"
    "v_and_b32 v211, 24, v208\n"
    "v_and_b32 v212, 7, v208\n"
    "v_lshlrev_b32 v213, 5, v211\n"
    "v_lshl_add_u32 v213, v212, 4, v213\n"
    "s_lshl_b32 s26, s78, 3\n"
    "v_add_u32 v213, s26, v213\n"
    "v_add_u32 v210, v210, v213\n"
    "s_cmp_lt_u32 s22, 96\n"
    "s_cselect_b32 s27, 0, 128\n"
    "s_cselect_b32 s28, s22, s23\n"
    "s_cselect_b32 s29, s20, s21\n"
    "s_cselect_b32 s24, s23, -1\n"
    "s_add_u32 s26, s27, 0\n"
    "buffer_load_dwordx4 v[80:83], v210, s[64:67], s26 offen sc1\n"
    "s_add_u32 s26, s27, 20480\n"
    "buffer_load_dwordx4 v[84:87], v210, s[64:67], s26 offen sc1\n"
    "s_add_u32 s26, s27, 40960\n"
    "buffer_load_dwordx4 v[88:91], v210, s[64:67], s26 offen sc1\n"
    "s_add_u32 s26, s27, 61440\n"
    "buffer_load_dwordx4 v[92:95], v210, s[64:67], s26 offen sc1\n"
    "s_add_u32 s26, s27, 81920\n"
    "buffer_load_dwordx4 v[96:99], v210, s[64:67], s26 offen sc1\n"
    "s_add_u32 s26, s27, 102400\n"
    "buffer_load_dwordx4 v[120:123], v210, s[64:67], s26 offen sc1\n"
    "s_add_u32 s26, s27, 122880\n"
    "buffer_load_dwordx4 v[124:127], v210, s[64:67], s26 offen sc1\n"
    "s_add_u32 s26, s27, 143360\n"
    "buffer_load_dwordx4 v[128:131], v210, s[64:67], s26 offen sc1\n"
    "s_add_u32 s26, s27, 163840\n"
    "buffer_load_dwordx4 v[132:135], v210, s[64:67], s26 offen sc1\n"
    "s_add_u32 s26, s27, 184320\n"
    "buffer_load_dwordx4 v[136:139], v210, s[64:67], s26 offen sc1\n"
    "s_add_u32 s26, s27, 204800\n"
    "buffer_load_dwordx4 v[192:195], v210, s[64:67], s26 offen sc1\n"
    "s_add_u32 s26, s27, 225280\n"
    "buffer_load_dwordx4 v[196:199], v210, s[64:67], s26 offen sc1\n"
    "s_add_u32 s26, s27, 245760\n"
    "buffer_load_dwordx4 v[200:203], v210, s[64:67], s26 offen sc1\n"
    "s_add_u32 s26, s27, 266240\n"
    "buffer_load_dwordx4 v[220:223], v213, s[64:67], s26 offen sc1\n"
    "v_and_b32 v212, 6, v208\n"
    "v_lshl_add_u32 v211, v212, 4, v211\n"
    "v_and_b32 v212, 1, v208\n"
    "v_lshl_add_u32 v211, v212, 2, v211\n"
    "s_mov_b32 s25, 0\n"
    "s_waitcnt vmcnt(20)\n"
    "s_branch COPYC%=\n"
    "YW%=:\n"
    "s_cmp_lg_u32 s69, 6\n"
    "s_cbranch_scc1 YOWN%=\n"
    "s_mov_b32 s35, 262144\n"
    "YS%=:\n"
    "s_lshl_b32 s26, s28, 3\n"
    "s_and_b32 s26, s26, 112\n"
    "v_xor_b32 v209, s26, v211\n"
    "s_lshl_b32 s26, s28, 7\n"
    "v_add_u32 v209, s26, v209\n"
    "s_bitcmp1_b32 s29, 31\n"
    "s_cselect_b32 s26, 0x80000000, -1\n"
    "s_xor_b32 s26, s26, s29\n"
    "s_and_b32 s26, s26, 0xffff0000\n"
    "v_mov_b32 v212, s26\n"
    "v_mul_f32 v212, 0xbfb8aa3b, v212\n"
    "v_exp_f32 v212, v212\n"
    "s_nop 0\n"
    "v_add_f32 v212, 1.0, v212\n"
    "v_rcp_f32 v212, v212\n"
    "s_nop 0\n"
    "v_cvt_pk_bf16_f32 v212, v212, v212\n"
    "v_lshlrev_b32 v212, 16, v212\n"
    "s_mov_b32 s3, 16777216\n"
    "v_mov_b32 v181, 156700\n"
    "GOY%=:\n"
    "ds_read_b32 v180, v181\n"
    "s_waitcnt lgkmcnt(0)\n"
    "v_cmp_le_u32 vcc, 256, v180\n"
    "s_cbranch_vccnz GOYD%=\n"
    "s_sleep 1\n"
    "s_sub_u32 s3, s3, 1\n"
    "s_cbranch_scc0 GOY%=\n"
    "GOYD%=:\n"
    "ds_read_b32 v217, v209 offset:61440\n"
    "v_cmp_gt_u32 vcc, 32, v208\n"
    "v_mov_b32 v216, 0\n"
    "v_mov_b32 v214, 0\n"
    "v_mov_b32 v215, 0\n"
    "s_waitcnt vmcnt(0)\n"
    "v_xor_b32 v81, s68, v81\n"
    "v_xor_b32 v83, s68, v83\n"
    "v_or3_b32 v216, v216, v81, v83\n"
    "v_add_f32 v214, v214, v80\n"
    "v_add_f32 v215, v215, v82\n"
    "v_xor_b32 v85, s68, v85\n"
    "v_xor_b32 v87, s68, v87\n"
    "v_or3_b32 v216, v216, v85, v87\n"
    "v_add_f32 v214, v214, v84\n"
    "v_add_f32 v215, v215, v86\n"
    "v_xor_b32 v89, s68, v89\n"
    "v_xor_b32 v91, s68, v91\n"
    "v_or3_b32 v216, v216, v89, v91\n"
    "v_add_f32 v214, v214, v88\n"
    "v_add_f32 v215, v215, v90\n"
    "v_xor_b32 v93, s68, v93\n"
    "v_xor_b32 v95, s68, v95\n"
    "v_or3_b32 v216, v216, v93, v95\n"
    "v_add_f32 v214, v214, v92\n"
    "v_add_f32 v215, v215, v94\n"
    "v_xor_b32 v97, s68, v97\n"
    "v_xor_b32 v99, s68, v99\n"
    "v_or3_b32 v216, v216, v97, v99\n"
    "v_add_f32 v214, v214, v96\n"
    "v_add_f32 v215, v215, v98\n"
    "v_xor_b32 v121, s68, v121\n"
    "v_xor_b32 v123, s68, v123\n"
    "v_or3_b32 v216, v216, v121, v123\n"
    "v_add_f32 v214, v214, v120\n"
    "v_add_f32 v215, v215, v122\n"
    "v_xor_b32 v125, s68, v125\n"
    "v_xor_b32 v127, s68, v127\n"
    "v_or3_b32 v216, v216, v125, v127\n"
    "v_add_f32 v214, v214, v124\n"
    "v_add_f32 v215, v215, v126\n"
    "v_xor_b32 v129, s68, v129\n"
    "v_xor_b32 v131, s68, v131\n"
    "v_or3_b32 v216, v216, v129, v131\n"
    "v_add_f32 v214, v214, v128\n"
    "v_add_f32 v215, v215, v130\n"
    "v_xor_b32 v133, s68, v133\n"
    "v_xor_b32 v135, s68, v135\n"
    "v_or3_b32 v216, v216, v133, v135\n"
    "v_add_f32 v214, v214, v132\n"
    "v_add_f32 v215, v215, v134\n"
    "v_xor_b32 v137, s68, v137\n"
    "v_xor_b32 v139, s68, v139\n"
    "v_or3_b32 v216, v216, v137, v139\n"
    "v_add_f32 v214, v214, v136\n"
    "v_add_f32 v215, v215, v138\n"
    "v_xor_b32 v193, s68, v193\n"
    "v_xor_b32 v195, s68, v195\n"
    "v_or3_b32 v216, v216, v193, v195\n"
    "v_add_f32 v214, v214, v192\n"
    "v_add_f32 v215, v215, v194\n"
    "v_xor_b32 v197, s68, v197\n"
    "v_xor_b32 v199, s68, v199\n"
    "v_or3_b32 v216, v216, v197, v199\n"
    "v_add_f32 v214, v214, v196\n"
    "v_add_f32 v215, v215, v198\n"
    "v_xor_b32 v201, s68, v201\n"
    "v_xor_b32 v203, s68, v203\n"
    "v_or3_b32 v216, v216, v201, v203\n"
    "v_add_f32 v214, v214, v200\n"
    "v_add_f32 v215, v215, v202\n"
    "v_xor_b32 v221, s68, v221\n"
    "v_xor_b32 v223, s68, v223\n"
    "v_or3_b32 v216, v216, v221, v223\n"
    "v_cndmask_b32 v220, 0, v220, vcc\n"
    "v_cndmask_b32 v222, 0, v222, vcc\n"
    "v_add_f32 v214, v214, v220\n"
    "v_add_f32 v215, v215, v222\n"
    "s_waitcnt lgkmcnt(0)\n"
    "v_cmp_ne_u32 vcc, 0, v216\n"
    "s_cbranch_vccz YK%=\n"
    "s_sleep 4\n"
    "s_sub_u32 s35, s35, 1\n"
    "s_cbranch_scc0 YL%=\n"
    "YK%=:\n"
    "v_mov_b32 v216, v214\n"
    "s_nop 1\n"
    "v_permlane32_swap_b32 v214, v216\n"
    "v_add_f32 v214, v214, v216\n"
    "v_mov_b32 v216, v215\n"
    "s_nop 1\n"
    "v_permlane32_swap_b32 v215, v216\n"
    "v_add_f32 v215, v215, v216\n"
    "v_cvt_pk_bf16_f32 v214, v214, v215\n"
    "v_and_b32 v215, 0xffff0000, v214\n"
    "v_lshlrev_b32 v214, 16, v214\n"
    "v_mul_f32 v214, v214, v212\n"
    "v_mul_f32 v215, v215, v212\n"
    "v_cvt_pk_bf16_f32 v214, v214, v215\n"
    "v_and_b32 v215, 0xffff0000, v214\n"
    "v_lshlrev_b32 v214, 16, v214\n"
    "v_and_b32 v216, 0xffff0000, v217\n"
    "v_lshlrev_b32 v217, 16, v217\n"
    "v_add_f32 v214, v214, v217\n"
    "v_add_f32 v215, v215, v216\n"
    "v_cvt_pk_bf16_f32 v214, v214, v215\n"
    "ds_write_b32 v209, v214 offset:61440\n"
    "s_cmp_ge_u32 s24, 96\n"
    "s_cbranch_scc1 YD%=\n"
    "s_movk_i32 s27, 128\n"
    "s_mov_b32 s28, s24\n"
    "s_mov_b32 s29, s21\n"
    "s_mov_b32 s24, -1\n"
    "YL%=:\n"
    "s_add_u32 s26, s27, 0\n"
    "buffer_load_dwordx4 v[80:83], v210, s[64:67], s26 offen sc1\n"
    "s_add_u32 s26, s27, 20480\n"
    "buffer_load_dwordx4 v[84:87], v210, s[64:67], s26 offen sc1\n"
    "s_add_u32 s26, s27, 40960\n"
    "buffer_load_dwordx4 v[88:91], v210, s[64:67], s26 offen sc1\n"
    "s_add_u32 s26, s27, 61440\n"
    "buffer_load_dwordx4 v[92:95], v210, s[64:67], s26 offen sc1\n"
    "s_add_u32 s26, s27, 81920\n"
    "buffer_load_dwordx4 v[96:99], v210, s[64:67], s26 offen sc1\n"
    "s_add_u32 s26, s27, 102400\n"
    "buffer_load_dwordx4 v[120:123], v210, s[64:67], s26 offen sc1\n"
    "s_add_u32 s26, s27, 122880\n"
    "buffer_load_dwordx4 v[124:127], v210, s[64:67], s26 offen sc1\n"
    "s_add_u32 s26, s27, 143360\n"
    "buffer_load_dwordx4 v[128:131], v210, s[64:67], s26 offen sc1\n"
    "s_add_u32 s26, s27, 163840\n"
    "buffer_load_dwordx4 v[132:135], v210, s[64:67], s26 offen sc1\n"
    "s_add_u32 s26, s27, 184320\n"
    "buffer_load_dwordx4 v[136:139], v210, s[64:67], s26 offen sc1\n"
    "s_add_u32 s26, s27, 204800\n"
    "buffer_load_dwordx4 v[192:195], v210, s[64:67], s26 offen sc1\n"
    "s_add_u32 s26, s27, 225280\n"
    "buffer_load_dwordx4 v[196:199], v210, s[64:67], s26 offen sc1\n"
    "s_add_u32 s26, s27, 245760\n"
    "buffer_load_dwordx4 v[200:203], v210, s[64:67], s26 offen sc1\n"
    "s_add_u32 s26, s27, 266240\n"
    "buffer_load_dwordx4 v[220:223], v213, s[64:67], s26 offen sc1\n"
    "s_branch YS%=\n"
    "YD%=:\n"
    "v_mov_b32 v214, 156696\n"
    "ds_write_b32 v214, v214\n"
    "s_branch YCB%=\n"
    "YOWN%=:\n"
    "s_lshr_b32 s22, s22, 4\n"
    "s_lshr_b32 s23, s23, 4\n"
    "s_cmp_eq_u32 s22, s83\n"
    "s_cbranch_scc1 YOW2%=\n"
    "s_cmp_lg_u32 s23, s83\n"
    "s_cbranch_scc1 YBACK%=\n"
    "YOW2%=:\n"
    "s_mov_b32 s3, 16777216\n"
    "v_mov_b32 v181, 156696\n"
    "YWL%=:\n"
    "ds_read_b32 v180, v181\n"
    "s_waitcnt lgkmcnt(0)\n"
    "v_cmp_ne_u32 vcc, 0, v180\n"
    "s_cbranch_vccnz YWLD%=\n"
    "s_sleep 1\n"
    "s_sub_u32 s3, s3, 1\n"
    "s_cbranch_scc0 YWL%=\n"
    "YWLD%=:\n"
    "YCB%=:\n"
    "ds_read_b32 v176, v234\n"
    "ds_read_b64 v[192:193], v228\n"
    "ds_read_b64 v[194:195], v229\n"
    "ds_read_b64 v[196:197], v230\n"
    "ds_read_b64 v[198:199], v231\n"
    "s_waitcnt lgkmcnt(4)\n"
    "v_sub_f32 v178, 1.0, v176\n"
    "s_waitcnt lgkmcnt(3)\n"
    "v_lshlrev_b32 v208, 16, v192\n"
    "v_and_b32 v209, 0xffff0000, v192\n"
    "v_pk_mul_f32 v[208:209], v[208:209], v[178:179] op_sel_hi:[1,0]\n"
    "v_lshlrev_b32 v210, 16, v193\n"
    "v_and_b32 v211, 0xffff0000, v193\n"
    "v_pk_mul_f32 v[210:211], v[210:211], v[178:179] op_sel_hi:[1,0]\n"
    "s_waitcnt lgkmcnt(2)\n"
    "v_lshlrev_b32 v212, 16, v194\n"
    "v_and_b32 v213, 0xffff0000, v194\n"
    "v_pk_mul_f32 v[212:213], v[212:213], v[178:179] op_sel_hi:[1,0]\n"
    "v_lshlrev_b32 v214, 16, v195\n"
    "v_and_b32 v215, 0xffff0000, v195\n"
    "v_pk_mul_f32 v[214:215], v[214:215], v[178:179] op_sel_hi:[1,0]\n"
    "s_waitcnt lgkmcnt(1)\n"
    "v_lshlrev_b32 v216, 16, v196\n"
    "v_and_b32 v217, 0xffff0000, v196\n"
    "v_pk_mul_f32 v[216:217], v[216:217], v[178:179] op_sel_hi:[1,0]\n"
    "v_lshlrev_b32 v218, 16, v197\n"
    "v_and_b32 v219, 0xffff0000, v197\n"
    "v_pk_mul_f32 v[218:219], v[218:219], v[178:179] op_sel_hi:[1,0]\n"
    "s_waitcnt lgkmcnt(0)\n"
    "v_lshlrev_b32 v220, 16, v198\n"
    "v_and_b32 v221, 0xffff0000, v198\n"
    "v_pk_mul_f32 v[220:221], v[220:221], v[178:179] op_sel_hi:[1,0]\n"
    "v_lshlrev_b32 v222, 16, v199\n"
    "v_and_b32 v223, 0xffff0000, v199\n"
    "v_pk_mul_f32 v[222:223], v[222:223], v[178:179] op_sel_hi:[1,0]\n"
    "s_branch YBACK%=\n"
    ::"{v[192:207]}"(p0), "{v[208:223]}"(p1), "{v[224:239]}"(p2), "{v[240:255]}"(p3), "{v[176:191]}"(r0), "{s[68:83]}"(uc), "{s84}"(C_H + gb * 128), "{s85}"(b), "{s[90:91]}"(kp0)
    : "memory", "s99");
  __builtin_unreachable();
}

extern "C" void kernel_launch(void* const* d_in, const int* in_sizes, int n_in, void* d_out, int out_size, void* d_ws,
                              size_t ws_size, hipStream_t stream) {
  kf<<<256, 512, 0, stream>>>((const u16*)d_in[0], (const u16*)d_in[1], (const u16*)d_in[2], (const u16*)d_in[4],
                              (const u16*)d_in[8], (const u16*)d_in[5], (const u16*)d_in[6], (const u16*)d_in[7],
                              (char*)d_ws, (u16*)d_out);
}
```

```cpp
#include <hip/hip_runtime.h>
#include <stdint.h>

typedef unsigned short u16;
typedef unsigned long long u64;
typedef __attribute__((ext_vector_type(8))) __bf16 bf16x8;
typedef __attribute__((ext_vector_type(2))) __bf16 bf16x2;
typedef __attribute__((ext_vector_type(4))) short s16x4;
typedef __attribute__((ext_vector_type(4))) float f32x4;
typedef __attribute__((ext_vector_type(2))) float f32x2;
typedef __attribute__((ext_vector_type(4))) unsigned u32x4;
typedef __attribute__((ext_vector_type(2))) unsigned u32x2;
typedef __attribute__((ext_vector_type(8))) unsigned u32x8;
typedef __attribute__((ext_vector_type(16))) unsigned u32x16;
#define GAS __attribute__((address_space(1)))
#define LAS __attribute__((address_space(3)))
typedef GAS unsigned gu32;

#define S_LEN 2048
#define DM 640
#define NQKV 1920
#define FF 1728

#define WS_O 0
#define WS_O_B 2703360
#define WS_H WS_O_B
#define INB 10240
#define WS_H_B (220 * INB)
#define WS_C (WS_H + WS_H_B)
#define C_K 0
#define C_H 56320
#define C_R 84480
#define C_X (C_R + 256)
#define C_F (C_X + 5120)
#define C_A (C_F + 23552)
#define C_E (C_A + 32768)
#define C_B (C_E + 256)
static_assert(C_E == 146176, "(used in the assembly)");
#define WS_YP (WS_C + C_B)
#define TAG_STEP 0x9E3779B9u
#define SENT 0xFFFFFFFFu
#define SPIN_CAP (1u << 18)
#define OOB 0x7FFF0000
#define RLX_AGENT __ATOMIC_RELAXED, __HIP_MEMORY_SCOPE_AGENT

__device__ __forceinline__ float bflo(unsigned u) { return __uint_as_float(u << 16); }
__device__ __forceinline__ float bfhi(unsigned u) { return __uint_as_float(u & 0xFFFF0000u); }
__device__ __forceinline__ unsigned pk2(float a, float b) {
  f32x2 v = {a, b};
  return __builtin_bit_cast(unsigned, __builtin_convertvector(v, bf16x2));
}
__device__ __forceinline__ float rbf(float f) { return bflo(pk2(f, f)); }
__device__ __forceinline__ unsigned perm_lo(unsigned hi_src, unsigned lo_src) {
  return __builtin_amdgcn_perm(hi_src, lo_src, 0x05040100u);
}
__device__ __forceinline__ unsigned perm_hi(unsigned hi_src, unsigned lo_src) {
  return __builtin_amdgcn_perm(hi_src, lo_src, 0x07060302u);
}
__device__ __forceinline__ f32x4 mfma16(u32x4 a, u32x4 b, f32x4 c) {
  return __builtin_amdgcn_mfma_f32_16x16x32_bf16(__builtin_bit_cast(bf16x8, a), __builtin_bit_cast(bf16x8, b), c, 0, 0, 0);
}
__device__ __forceinline__ u32x2 tr_read(const char* p) {
  return __builtin_bit_cast(u32x2, __builtin_amdgcn_ds_read_tr16_b64_v4i16((LAS s16x4*)(p)));
}
#define BARRIER() asm volatile("s_barrier" ::: "memory")
#define WAITV(n) asm volatile("s_waitcnt vmcnt(" #n ")" ::: "memory")
#define WAITV_(n) asm volatile("s_waitcnt vmcnt(%0)" ::"i"(n) : "memory")
#define WAITL0() do { asm volatile("" ::: "memory"); __builtin_amdgcn_s_waitcnt(0xC07F); asm volatile("" ::: "memory"); } while (0)

__device__ __forceinline__ void top2_mrg(unsigned& k1, unsigned& k2, unsigned b1, unsigned b2) {
  k2 = max(max(k2, b2), min(k1, b1));
  k1 = max(k1, b1);
}
template <int CTRL>
__device__ __forceinline__ void top2_dpp(unsigned& k1, unsigned& k2) {
  const unsigned b1 = (unsigned)__builtin_amdgcn_update_dpp(0, (int)k1, CTRL, 0xF, 0xF, true);
  const unsigned b2 = (unsigned)__builtin_amdgcn_update_dpp(0, (int)k2, CTRL, 0xF, 0xF, true);
  top2_mrg(k1, k2, b1, b2);
}
__device__ __forceinline__ void top2_all(unsigned& k1, unsigned& k2) {
  top2_dpp<0xB1>(k1, k2);
  top2_dpp<0x4E>(k1, k2);
  top2_dpp<0x141>(k1, k2);
  top2_dpp<0x140>(k1, k2);
  {
    auto a = __builtin_amdgcn_permlane16_swap(k1, k1, false, false);
    auto c = __builtin_amdgcn_permlane16_swap(k2, k2, false, false);
    k1 = a[0];
    k2 = c[0];
    top2_mrg(k1, k2, a[1], c[1]);
  }
  {
    auto a = __builtin_amdgcn_permlane32_swap(k1, k1, false, false);
    auto c = __builtin_amdgcn_permlane32_swap(k2, k2, false, false);
    k1 = a[0];
    k2 = c[0];
    top2_mrg(k1, k2, a[1], c[1]);
  }
}
__device__ __forceinline__ void touch_line(const char* p, unsigned lds_off) {
  asm volatile("s_mov_b32 m0, %1\n\ts_nop 0\n\tglobal_load_lds_dword %0, off" ::"v"(p), "s"(lds_off) : "memory");
}
__device__ __forceinline__ void dma16(unsigned voff, const void* sbase, unsigned lds_off) {
  asm volatile("s_mov_b32 m0, %2\n\ts_nop 0\n\tglobal_load_lds_dwordx4 %0, %1" ::"v"(voff), "s"(sbase), "s"(lds_off) : "memory");
}
__device__ __forceinline__ void dma16_sc1(unsigned voff, const void* sbase, unsigned lds_off) {
  asm volatile("s_mov_b32 m0, %2\n\ts_nop 0\n\tglobal_load_lds_dwordx4 %0, %1 sc1" ::"v"(voff), "s"(sbase), "s"(lds_off) : "memory");
}
__device__ __forceinline__ void bdma16(unsigned voff, u32x4 srd, unsigned soff, unsigned lds_off) {
  asm volatile("s_mov_b32 m0, %3\n\ts_nop 0\n\tbuffer_load_dwordx4 %0, %1, %2 offen lds" ::"v"(voff), "s"(srd), "s"(soff), "s"(lds_off) : "memory");
}
__device__ __forceinline__ u32x4 make_srd(const void* p, unsigned bytes) {
  return (u32x4){(unsigned)(uintptr_t)p, (unsigned)((uintptr_t)p >> 32) & 0xFFFFu, bytes, 0x00020000u};
}
template <class T> __device__ __forceinline__ T* uni(T* p) {
  const unsigned lo = __builtin_amdgcn_readfirstlane((unsigned)(uintptr_t)p), hi = __builtin_amdgcn_readfirstlane((unsigned)((uintptr_t)p >> 32));
  return (T*)(((u64)hi << 32) | lo);
}
#define RSRC(p, bytes) __builtin_amdgcn_make_buffer_rsrc((void*)(p), 0, bytes, 0x00020000)
__device__ __forceinline__ float key_val(unsigned key) {
  unsigned u = key >> 16;
  u ^= (u & 0x8000u) ? 0x8000u : 0xFFFFu;
  return bflo(u);
}
__device__ __forceinline__ float fmx(float a, float b) { return __builtin_elementwise_maximum(a, b); }
#define SB() __builtin_amdgcn_sched_barrier(0)
__device__ __forceinline__ float dot2(unsigned a, unsigned b, float c) {
  return __builtin_amdgcn_fdot2_f32_bf16(__builtin_bit_cast(bf16x2, a), __builtin_bit_cast(bf16x2, b), c, false);
}
template <int CTRL>
__device__ __forceinline__ float dpp_add(float v) {
  return v + __int_as_float(__builtin_amdgcn_update_dpp(0, __float_as_int(v), CTRL, 0xF, 0xF, true));
}
__device__ __forceinline__ float row_sum(float v) {
  v = dpp_add<0xB1>(v);
  v = dpp_add<0x4E>(v);
  v = dpp_add<0x141>(v);
  return dpp_add<0x140>(v);
}
__device__ __forceinline__ unsigned max4(u32x4 v) { return max(max(v[0], v[1]), max(v[2], v[3])); }
template <int N> struct IC { static constexpr int value = N; };
#define PIN(v) asm volatile("" : "+v"(v))
#define PINS(v) do { v = __builtin_amdgcn_readfirstlane(v); asm volatile("" : "+s"(v)); } while (0)
#define AI __attribute__((always_inline))

#define BUF(q) ((q) % 3 == 2 ? 0 : (q) % 3 == 1 ? 51200 : 112640)
#define RS 168
#define TS (16 * RS)
#define SO (104 * RS)
#define L_WO 51200
#define L_TAB 88064
#define L_XT 133120
#define L_EX 147456
#define L_MISC 155648
#define L_SMP L_MISC
#define L_Z (L_MISC + 1024)
#define L_FL (L_Z + 8)
#define L_Y (L_Z + 64)
#define L_SM2 (L_MISC + 2048)
#define L_TOUCH (L_MISC + 3072)
#define L_TOTAL (L_MISC + 3584)
#define CODE_LINES 269
#define FOFF 0
#define CT64 1
#define LD_T 270
#define LD_S 2
#define KP_T 850
#define POS_OF(hl) ((int)((0xBA9F73E62D51C408ull >> ((hl) * 4)) & 15))
#define HL_OF(pos) ((int)((0xC963FED0B852A741ull >> ((pos) * 4)) & 15))

__global__ __launch_bounds__(512, 2) void kf(const u16*, const u16*, const u16*, const u16*, const u16*, const u16*, const u16*, const u16*, char*, u16*) {
  __shared__ __attribute__((aligned(16))) char smem[L_TOTAL];
  typedef const __attribute__((address_space(4))) u64* karg_t;
  const karg_t kp0 = (karg_t)__builtin_amdgcn_kernarg_segment_ptr();
  u64 pc;
  asm volatile("s_load_dword s100, %1, 0x0\n\ts_load_dword s101, %1, 0x40\n\ts_getpc_b64 %0" : "=s"(pc) : "s"(kp0) : "s100", "s101");
  const u16 *x, *wqkv, *wo, *w_r, *gate_w, *w_gate, *w_up, *w_down;
  char* ws;
  u16* out;
  auto args = [&]() AI {
    SB();
    karg_t kp = kp0;
    asm volatile("" : "+s"(kp));
    x = (const u16*)kp[0]; wqkv = (const u16*)kp[1]; wo = (const u16*)kp[2]; w_r = (const u16*)kp[3]; gate_w = (const u16*)kp[4];
    w_gate = (const u16*)kp[5]; w_up = (const u16*)kp[6]; w_down = (const u16*)kp[7]; ws = (char*)kp[8]; out = (u16*)kp[9];
  };
  const int tid = threadIdx.x, b = blockIdx.x;
  {
    const int ln = ((b >> 3) & 7) + 8 * (tid >> 1);
    if (ln < CODE_LINES)
      touch_line((const char*)((pc & ~255ull) + ln * 128 + (tid & 1) * 64), (unsigned)(uintptr_t)(LAS char*)smem + L_TOUCH);
  }
  unsigned tag;
  auto get_tag = [&]() AI {
    asm volatile("s_load_dword %0, %1, 0x0\n\ts_waitcnt lgkmcnt(0)" : "=s"(tag) : "s"(ws + WS_C + C_E) : "memory");
    tag += TAG_STEP;
  };
  const int lane = tid & 63;
  const int wave = __builtin_amdgcn_readfirstlane(tid >> 6);
  const int xcd = b & 7, jj = b >> 3;
  const int c15 = lane & 15, g4 = lane >> 4, q4 = c15 >> 2, p4 = lane & 3;
  const int nb = 30 - (xcd & 1) * 5, j5 = (jj * 205) >> 10;
  const int par = (xcd >> 1) & 1, hf = xcd >> 2;
  const int gi = j5 == 5 ? 10 : j5 + (xcd & 1) * 5, c = jj - j5 * 5;
  const int rg = par * 11 + gi, cg = hf * 5 + c, gb = rg * 10 + cg;
  const int tb = 96 * gi;
  const unsigned lds0 = (unsigned)(uintptr_t)(LAS char*)smem;
#define rc_ RSRC(ws + WS_C, C_B)

  if (__builtin_expect(jj >= nb, 0)) {
    const int f = (jj - nb) * 4 + (xcd >> 1) + ((xcd & 1) ? FOFF : 28 - FOFF * 7 / 2);
    args();
    if (f > 27) {
      const int mi = f - 28;
      const __amdgpu_buffer_rsrc_t r = RSRC(ws + WS_O, WS_O_B);
      const u32x4 sent = {SENT, SENT, SENT, SENT};
      const int l3 = lane < 8 ? lane * 16 + 3072 : OOB, l4 = lane < 48 ? lane * 16 + 3072 : OOB, l0 = lane < 40 ? lane * 16 : OOB;
#pragma unroll 1
      for (int k = mi * 66 + wave; k < mi * 66 + 66; k += 8) {
        const int k12 = (k * 1366) >> 14, so = (k + k12 * 4) * 3840 + 640;
#pragma unroll
        for (int j = 0; j < 3; ++j) __builtin_amdgcn_raw_buffer_store_b128(sent, r, lane * 16 + j * 1024, so, 16);
        __builtin_amdgcn_raw_buffer_store_b128(sent, r, l3, so, 16);
      }
      get_tag();
      const int m2 = tid < 176 ? C_F + 1024 + (tid >> 3) * 1024 + mi * 128 + (tid & 7) * 16 : OOB;
      if (wave < 3) __builtin_amdgcn_raw_buffer_store_b128((u32x4){~tag, ~tag, ~tag, ~tag}, rc_, m2, 0, 16);
#pragma unroll 1
      for (int k = mi * 22 + 7 - wave; k < mi * 22 + 22; k += 8) {
        const int so = ((k >> 2) * 16 + 12 + (k & 3)) * 3840;
#pragma unroll
        for (int j = 0; j < 3; ++j) {
          __builtin_amdgcn_raw_buffer_store_b128(sent, r, lane * 16 + j * 1024, so, 16);
          __builtin_amdgcn_raw_buffer_store_b128(sent, r, l0, so - (j + 1) * 4 * 3840, 16);
        }
        __builtin_amdgcn_raw_buffer_store_b128(sent, r, l4, so, 16);
        if (k < mi * 22 + 8) {
          WAITV(7);
          BARRIER();
          if (wave == 0) __builtin_amdgcn_raw_buffer_store_b128((u32x4){tag, tag, tag, tag}, rc_, lane < 8 ? C_F + mi * 128 + lane * 16 : lane == 8 ? C_A + b * 128 : OOB, 0, 16);
        }
      }
      WAITV(0);
      BARRIER();
      if (wave < 3) __builtin_amdgcn_raw_buffer_store_b128((u32x4){tag, tag, tag, tag}, rc_, m2, 0, 16);
      return;
    }
    if (f == 27) {
      const unsigned t0 = (unsigned)__builtin_amdgcn_s_memrealtime();
      auto until = [&](unsigned d) AI {
        while ((unsigned)__builtin_amdgcn_s_memrealtime() - t0 < d) __builtin_amdgcn_s_sleep(1);
      };
      get_tag();
      __builtin_amdgcn_raw_buffer_store_b128((u32x4){~tag, ~tag, ~tag, ~tag}, rc_, tid < 320 ? C_X + tid * 16 : OOB, 0, 16);
      u32x2 kv[3];
      int ko[3];
      ko[0] = (tid >> 2) * 256 + (tid & 2) * 32 + (tid & 1) * 16;
      ko[1] = tid < 368 ? ko[0] + 128 * 256 : OOB;
      ko[2] = tid < 176 ? ((tid >> 2) * 5 + ((tid >> 1) & 1)) * 256 + 128 + (tid & 1) * 16 : OOB;
      kv[0] = kv[1] = kv[2] = (u32x2){0u, 0u};
#pragma unroll 1
      for (unsigned spins = 0; spins < SPIN_CAP; ++spins) {
        asm volatile("" ::: "memory");
        u32x4 t_[3];
#pragma unroll
        for (int q = 0; q < 3; ++q) t_[q] = __builtin_amdgcn_raw_buffer_load_b128(rc_, ko[q], C_K, 16);
        bool ok = true;
#pragma unroll
        for (int q = 0; q < 3; ++q)
          if (ko[q] != OOB) {
            if ((t_[q][1] == tag) & (t_[q][3] == tag)) {
              kv[q] = (u32x2){t_[q][0], t_[q][2]};
              ko[q] = OOB;
            } else {
              ok = false;
            }
          }
        if (__all(ok)) break;
        until(LD_T);
        __builtin_amdgcn_s_sleep(LD_S);
      }
      unsigned k1 = max(kv[0][0], kv[0][1]), k2 = min(kv[0][0], kv[0][1]);
      top2_mrg(k1, k2, max(kv[1][0], kv[1][1]), min(kv[1][0], kv[1][1]));
      top2_mrg(k1, k2, max(kv[2][0], kv[2][1]), min(kv[2][0], kv[2][1]));
      top2_all(k1, k2);
      unsigned* lk = (unsigned*)smem;
      if (lane == 0) *(u32x2*)(lk + wave * 2) = (u32x2){k1, k2};
      WAITL0();
      BARRIER();
      k1 = 0u;
      k2 = 0u;
#pragma unroll
      for (int q = 0; q < 4; ++q) {
        const u32x4 a_ = *(const u32x4*)(lk + q * 4);
        top2_mrg(k1, k2, a_[0], a_[1]);
        top2_mrg(k1, k2, a_[2], a_[3]);
      }
      if (tid < 320) {
        const u32x2 v = *(const u32x2*)(x + (2047 - (int)((tid < 160 ? k1 : k2) & 2047u)) * DM + (tid < 160 ? tid : tid - 160) * 4);
        __builtin_amdgcn_raw_buffer_store_b128((u32x4){v[0], tag, v[1], tag}, rc_, C_X + tid * 16, 0, 16);
      } else if (tid < 328) {
        __builtin_amdgcn_raw_buffer_store_b128((u32x4){k1, tag, k2, tag}, rc_, C_R + (tid - 320) * 16, 0, 16);
      }
      until(KP_T);
      int ao = (tid < 256 && tid != b) ? C_A + tid * 128 : OOB;
#pragma unroll 1
      for (unsigned spins = 0; spins < SPIN_CAP; ++spins) {
        asm volatile("" ::: "memory");
        if (__builtin_amdgcn_raw_buffer_load_b32(rc_, ao, 0, 16) == tag) ao = OOB;
        if (__all(ao == OOB)) break;
        __builtin_amdgcn_s_sleep(16);
      }
      BARRIER();
      if (tid == 0) __hip_atomic_store((gu32*)(ws + WS_C + C_E), tag, RLX_AGENT);
      return;
    }
    float* red = (float*)(smem + 81920);
    u16* hs = (u16*)(smem + 90112);
    volatile LAS unsigned* cnt = (volatile LAS unsigned*)(LAS char*)(smem + 90624);
    if (tid < 3) cnt[tid] = 0u;
    WAITL0();
    BARRIER();
    auto await = [&](int i, unsigned n) AI {
#pragma unroll 1
      for (unsigned spins = 0; spins < (SPIN_CAP << 8); ++spins) {
        if (__builtin_amdgcn_readfirstlane(cnt[i]) >= n) break;
        __builtin_amdgcn_s_sleep(1);
      }
      asm volatile("" ::: "memory");
    };
    auto arrive = [&](int i) AI {
      if (lane == 0) __hip_atomic_fetch_add((LAS unsigned*)cnt + i, 1u, __ATOMIC_RELAXED, __HIP_MEMORY_SCOPE_WORKGROUP);
    };
    if (wave == 7) {
      get_tag();
      await(2, 7);
      const u32x4 rsd = make_srd(w_down, DM * FF * 2);
      const int kr = lane >> 3;
      int vd = kr * (DM * 2) + (((lane & 7) ^ (kr & 2)) * 16);
#pragma unroll 1
      for (int o = 0; o < 8; ++o) {
#pragma unroll
        for (int j = 0; j < 10; ++j)
          asm volatile("s_mov_b32 m0, %3\n\ts_nop 0\n\tbuffer_load_dwordx4 %0, %1, %2 offen nt lds" ::"v"(vd), "s"(rsd), "s"((f * 64 + o * 8) * (DM * 2) + j * 128),
                       "s"(lds0 + (o * 10 + j) * 1024)
                       : "memory");
        vd ^= 64;
      }
      WAITV(0);
    } else {
      u32x4 r1[3][8];
      {
        const char* pa = (const char*)(c15 < 8 ? w_gate : w_up) + f * 128 + (c15 & 7) * 16 + (wave * 96 + g4 * 8) * (FF * 2);
        const GAS char* pp[12];
#pragma unroll
        for (int j = 0; j < 12; ++j) {
          pp[j] = (const GAS char*)pa + ((j >> 2) * 32 + (j & 3) * 2) * (FF * 2) + FF;
          PIN(pp[j]);
        }
        SB();
#pragma unroll
        for (int j = 0; j < 24; ++j)
          if (j < 16 || wave < 6) r1[j >> 3][j & 7] = __builtin_nontemporal_load((const GAS u32x4*)(pp[j >> 1] + ((j & 1) ? FF : -FF)));
      }
      SB();
      arrive(2);
      get_tag();
      auto transp = [&](u32x4(&w)[8]) AI {
        u32x4 t[8];
#pragma unroll
        for (int d = 0; d < 4; ++d)
#pragma unroll
          for (int p = 0; p < 4; ++p) {
            t[2 * d][p] = perm_lo(w[2 * p + 1][d], w[2 * p][d]);
            t[2 * d + 1][p] = perm_hi(w[2 * p + 1][d], w[2 * p][d]);
          }
#pragma unroll
        for (int j = 0; j < 8; ++j) {
          w[j] = t[j];
          PIN(w[j]);
        }
      };
      transp(r1[0]);
      transp(r1[1]);
      if (wave < 6) transp(r1[2]);
      if (wave == 0) {
        u32x4 v[5];
#pragma unroll 1
        for (unsigned spins = 0; spins < SPIN_CAP; ++spins) {
          asm volatile("" ::: "memory");
          unsigned d = 0;
#pragma unroll
          for (int k = 0; k < 5; ++k) v[k] = __builtin_amdgcn_raw_buffer_load_b128(rc_, C_X + lane * 16 + k * 1024, 0, 16);
#pragma unroll
          for (int k = 0; k < 5; ++k) d |= (v[k][1] ^ tag) | (v[k][3] ^ tag);
          if (__all(d == 0)) break;
          __builtin_amdgcn_s_sleep(2);
        }
#pragma unroll
        for (int k = 0; k < 5; ++k) *(u32x2*)(smem + 92160 + (k * 64 + lane) * 8 + (k * 64 + lane >= 160 ? 16 : 0)) = (u32x2){v[k][0], v[k][2]};
        WAITL0();
        cnt[0] = 1u;
      }
      await(0, 1);
      u32x4 xf[3];
#pragma unroll
      for (int k = 0; k < 3; ++k) xf[k] = *(const u32x4*)(smem + 92160 + (c15 & 1) * 1296 + ((wave * 3 + (k < 2 || wave < 6 ? k : 0)) * 32 + g4 * 8) * 2);
      f32x4 acc[8];
#pragma unroll
      for (int ff = 0; ff < 8; ++ff) acc[ff] = (f32x4){0.f, 0.f, 0.f, 0.f};
#pragma unroll
      for (int k = 0; k < 3; ++k)
        if (k < 2 || wave < 6) {
#pragma unroll
          for (int j = 0; j < 8; ++j) acc[j] = mfma16(xf[k], r1[k][j], acc[j]);
        }
      f32x4 pv;
#pragma unroll
      for (int q = 0; q < 4; ++q) {
        const float lo = (g4 & 1) ? acc[2 + (q >> 1)][q & 1] : acc[q >> 1][q & 1], hi = (g4 & 1) ? acc[6 + (q >> 1)][q & 1] : acc[4 + (q >> 1)][q & 1];
        pv[q] = (g4 & 2) ? hi : lo;
      }
      *(f32x4*)(red + wave * 256 + c15 * 16 + g4 * 4) = pv;
      WAITL0();
      arrive(1);
      if (wave < 2) {
        await(1, 7);
        float gs = 0.f, us = 0.f;
#pragma unroll
        for (int w = 0; w < 7; ++w) {
          gs += red[w * 256 + tid];
          us += red[w * 256 + 128 + tid];
        }
        const float gv = rbf(gs), uv = rbf(us);
        const float hv = rbf(gv * __builtin_amdgcn_rcpf(1.0f + __expf(-gv))) * uv;
        hs[(tid & 1) * 64 + (tid >> 1)] = (u16)pk2(hv, hv);
      }
    }
    int da[4];
#pragma unroll
    for (int t = 0; t < 4; ++t) {
      da[t] = (g4 * 10 + wave * 2) * 1024 + q4 * 128 + ((t ^ (q4 >> 1) ^ ((g4 & 1) << 1)) * 32) + p4 * 8;
      PIN(da[t]);
    }
    WAITL0();
    BARRIER();
    if (wave < 5) {
      const u32x4 h0 = *(const u32x4*)(hs + (c15 >> 3) * 64 + g4 * 8), h1 = *(const u32x4*)(hs + (c15 >> 3) * 64 + 32 + g4 * 8);
      const __amdgpu_buffer_rsrc_t ryp = RSRC(ws + WS_YP, 27 * 10240);
#pragma unroll
      for (int e = 0; e < 2; ++e) {
        f32x4 a[4];
#pragma unroll
        for (int t = 0; t < 4; ++t) {
          const u32x2 w0 = tr_read(smem + da[t] + e * 1024), w1 = tr_read(smem + da[t] + e * 1024 + 512);
          const u32x2 w2 = tr_read(smem + da[t] + e * 1024 + 40960), w3 = tr_read(smem + da[t] + e * 1024 + 41472);
          a[t] = mfma16((u32x4){w0[0], w0[1], w1[0], w1[1]}, h0, (f32x4){0.f, 0.f, 0.f, 0.f});
          a[t] = mfma16((u32x4){w2[0], w2[1], w3[0], w3[1]}, h1, a[t]);
        }
        float y[2];
#pragma unroll
        for (int q = 0; q < 2; ++q) {
          float s_[4];
#pragma unroll
          for (int t = 0; t < 4; ++t) s_[t] = (lane & 1) ? a[t][2 + q] : a[t][q];
          y[q] = (lane & 4) ? ((lane & 2) ? s_[3] : s_[2]) : ((lane & 2) ? s_[1] : s_[0]);
        }
        __builtin_amdgcn_raw_buffer_store_b128((u32x4){__float_as_uint(y[0]), __float_as_uint(y[1]), tag, tag}, ryp, lane * 16 + (f * 10 + wave * 2 + e) * 1024, 0, 16);
        asm volatile("s_nop 1");
        SB();
      }
    } else if (tid == 320) {
      __hip_atomic_store((gu32*)(ws + WS_C + C_A + b * 128), tag, RLX_AGENT);
    }
    return;
  }

  u32x16 p0, p1, p2, p3, r0;
  unsigned sc[16];
#define UNI(x) __builtin_amdgcn_readfirstlane(x)
  const int kr = lane >> 3, hw = wave & 3;
  const int mt = wave < 4 ? wave + 1 : wave < 6 ? 0 : 5;

  if (__builtin_expect(wave >= 4, 1)) {
    asm volatile("" : "=v"(p0), "=v"(p1), "=v"(p2), "=v"(p3), "=v"(r0));
    const int sw16 = cg * 128 + (((lane & 7) ^ ((kr & 2) | ((hw & 1) << 2))) * 16);
#pragma unroll
    for (int v = 0; v < 3; ++v) {
      r0[v] = ((tb + (hw >> 1) * 48 + v * 16 + (hw & 1) * 8 + kr) * 2 + par) * (DM * 2) + (((lane & 7) ^ (((hw & 1) * 4 + g4) & 7)) * 16) + (v == 2 ? 3072 : 0);
      r0[3 + v] = (hw * 8 + kr) * (NQKV * 2) + sw16 + v * 256;
      r0[6 + v] = r0[3 + v] + 32 * (NQKV * 2);
    }
    int kc = j5 == 5 ? 1 : j5 * 2;
    args();
    asm volatile("s_mov_b32 m0, %9\n\ts_nop 0\n\tbuffer_load_dwordx4 %0, %5, %7 offen lds\n\tbuffer_load_dwordx4 %1, %5, %7 offen offset:1024 lds\n\t"
                 "buffer_load_dwordx4 %2, %5, %7 offen offset:2048 lds\n\tbuffer_load_dwordx4 %3, %6, %8 offen offset:3072 lds\n\t"
                 "s_add_u32 m0, m0, 8192\n\ts_nop 0\n\tbuffer_load_dwordx4 %4, %6, %8 offen lds" ::"v"(r0[3]),
                 "v"(r0[4]), "v"(r0[5]), "v"(r0[0]), "v"(r0[2]), "s"(make_srd(wqkv, DM * NQKV * 2)), "s"(make_srd((const char*)x - 3072, S_LEN * DM * 2 + 3072)),
                 "s"(kc * (64 * NQKV * 2)), "s"(kc * 128), "s"(BUF(0) + hw * 9216)
                 : "memory", "scc");
    SB();
    r0[9] = lane < 16 ? (gb * 4 + hw) * 1024 + lane * 64 : OOB;
    {
      const int sct = (gb * 4 + hw) * 22 + lane, ck = (sct * 17477) >> 26;
      r0[14] = lane < 22 ? (sct + (ck + 1) * 3840) * 64 : OOB;
    }
    tag = 0;
    sc[2] = kc;
    sc[4] = sc[5] = 0;
  } else {
    const bool wjob = wave < 2 || (wave == 2 && c < 2);
    const int tj = tb + (wave < 2 ? wave * 10 + cg : 20 + hf * 2 + c) * 4, tj0 = min(tj, 1020);
    u32x4 xs[5], wv[4];
    int jo0 = lane * 16, jo1 = g4 * 2560 + c15 * 16, jo2 = c15 * 16;
    PIN(jo0); PIN(jo1); PIN(jo2);
    args();
    if (wjob) {
      const u16* xr = x + (2 * tj0 + par) * DM;
      asm volatile("global_load_dwordx4 %0, %9, %12\n\tglobal_load_dwordx4 %1, %9, %12 offset:2560\n\t"
                   "global_load_dwordx4 %2, %9, %13\n\tglobal_load_dwordx4 %3, %9, %13 offset:2560\n\t"
                   "global_load_dwordx4 %4, %10, %12 offset:1024\n\tglobal_load_dwordx4 %5, %9, %14\n\t"
                   "global_load_dwordx4 %6, %11, %14 offset:1024\n\tglobal_load_dwordx4 %7, %9, %15\n\tglobal_load_dwordx4 %8, %11, %15 offset:1024"
                   : "=&v"(xs[0]), "=&v"(xs[1]), "=&v"(xs[2]), "=&v"(xs[3]), "=&v"(xs[4]), "=&v"(wv[0]), "=&v"(wv[1]), "=&v"(wv[2]), "=&v"(wv[3])
                   : "v"(jo0), "v"(jo1), "v"(jo2), "s"(xr), "s"(xr + 4 * DM), "s"(w_r), "s"(gate_w)
                   : "memory");
    }
    const int rh = wave >> 1, ch = wave & 1;
    {
      const int e0 = c15 - 4 * g4;
#pragma unroll
      for (int i = 0; i < 4; ++i) {
        p0[i] = e0 - i <= -9 ? 0u : 0xFF800000u;
        p0[4 + i] = (e0 - i >= 0 && e0 - i <= 7) ? 0u : 0xFF800000u;
      }
      p0[8] = (e0 <= -9 ? 0xFFFFu : 0u) | (e0 - 1 <= -9 ? 0xFFFF0000u : 0u);
      p0[9] = (e0 - 2 <= -9 ? 0xFFFFu : 0u) | (e0 - 3 <= -9 ? 0xFFFF0000u : 0u);
      p0[10] = ~p0[8];
      p0[11] = ~p0[9];
      p0[12] = (g4 >> 1) * 61440 + (lane & 31) * 16;
      p0[13] = g4 * 3840 + c15 * 8 + 512;
      p0[14] = lane < 32 ? lane * 16 : OOB;
      p0[15] = g4 == 2 ? c15 * 8 + 512 : OOB;
      const int sw = (q4 >> 1) | ((g4 & 1) << 1), ho = (rg * 2 + hf) * 16;
      const unsigned aq = c15 * RS + g4 * 8, akh = c15 * RS + 32, av = 2 * SO - TS + (4 * g4 + q4) * RS + (p4 < 2 ? p4 * 8 : 32);
      unsigned xa[4];
#pragma unroll
      for (int f = 0; f < 4; ++f) {
        p2[f] = L_WO + (8 * g4 + q4) * 128 + ((f ^ sw) * 32) + p4 * 8;
        xa[f] = L_XT + c15 * 128 + ((((f * 2 + (g4 >> 1)) ^ (c15 >> 1)) & 7) * 16) + (g4 & 1) * 8;
      }
      if (wave == 3) {
        u32x4* tp = (u32x4*)(smem + L_TAB + lane * 16);
#pragma unroll
        for (int k = 0; k < 4; ++k) tp[k * 64] = (u32x4){p0[4 * k], p0[4 * k + 1], p0[4 * k + 2], p0[4 * k + 3]};
        const int s = ((c ? 0x2FDFCFC9u : 0x20D0C0C9u) >> (c15 * 2)) & 3, m = ((c == 0 ? 0x53204215310455ull : c == 4 ? 0x3053204255310455ull : 0x3053204215310455ull) >> (c15 * 4)) & 7;
        const bool nv = ((c == 0 ? 0x4u : c == 4 ? 0x800u : 0x330u) >> c15) & 1;
        tp[4 * 64] = (u32x4){aq, akh, g4 == 0 ? akh : L_Z, av};
        tp[5 * 64] = (u32x4){g4 == 0 ? 1u : 0u, (unsigned)(m * TS + s * 40), nv ? ~0u : (unsigned)((ho + POS_OF(3 * c + s)) * 3840 + m * 640), 0u};
        tp[6 * 64] = (u32x4){p2[0], p2[1], p2[2], p2[3]};
        tp[7 * 64] = (u32x4){xa[0], xa[1], xa[2], xa[3]};
        const int row = lane >> 2, sw0 = cg * 128 + (((lane & 7) ^ (kr & 2)) * 16), j = lane >> 1, w = j / 10;
        tp[8 * 64] = (u32x4){(unsigned)(L_XT + row * 128 + ((p4 ^ (row >> 1)) * 16)), (unsigned)(((tb + row) * 2 + par) * (DM * 2) + cg * 128 + p4 * 16),
                             (unsigned)(L_SMP + c15 * 8), (unsigned)(L_EX + lane * 16)};
        tp[9 * 64] = (u32x4){(unsigned)((kr + (kr & 4)) * (DM * 2) + sw0), (unsigned)(((kr & 3) + (kr >> 2) * 320 + 16) * (DM * 2) + sw0), 0u,
                             (unsigned)(lane < 48 ? C_K + (rg * 10 + j - w * 10 + (j >= 22 ? 3 : 0)) * 256 + w * 64 + 32 + (lane & 1) * 16 : lane < 56 ? C_F + (lane - 48) * 128 : C_R)};
        const int hi = lane >> 5, g = kr & 3, r8 = lane & 7;
        const unsigned g2 = gi ? gb * INB + 6144 + hi * 1024 + lane * 16 : OOB;
        const unsigned g3 = hi ? OOB : g >= 4 - c ? gb * INB + 9216 + lane * 16 : gi ? (gb + 1) * INB + 6144 + lane * 16 : OOB;
        tp[10 * 64] = (u32x4){(unsigned)(C_H + (hi ? gb + (p4 == 0 ? -(c > 0) : (p4 & 1) ? c < 4 : 0) - (gi && p4 >= 2 ? 10 : 0) : rg * 10 + min(c15, 9) - (gi && g4 ? 10 : 0)) * 128), 0u, (unsigned)(C_F + 1024 + rg * 1024 + r8 * 128), (unsigned)(gb * INB + lane * 16)};
        tp[11 * 64] = (u32x4){(gi && hi && c < 4 && g >= 3 - c) ? (gb - 9) * INB + 4096 + (g * 16 + 8 + r8) * 16 : g2, g2, g3,
                              (unsigned)(g4 < 4 - c ? c15 * RS + 8 * c + 128 + 8 * g4 : (48 + c15) * RS + 8 * (g4 - 4 + c))};
        tp[12 * 64] = (u32x4){(unsigned)(SO + (r8 - 8) * RS + 8 * c + 8 * (hi * 12 + g)), (unsigned)(SO + (r8 - 8) * RS + 8 * c + 8 * (hi * 4 + 4 + g)),
                              (unsigned)(hi ? 160 : g >= 4 - c ? SO + (40 + r8) * RS + 8 * (g - 4 + c) : SO + (r8 - 8) * RS + 8 * c + 128 + 8 * g), g3 == OOB ? 0u : ~0u};
        if (lane < 16) *(unsigned*)(smem + L_Z + lane * 4) = 0u;
        *(unsigned*)(smem + 2 * SO + (32 + kr) * RS + (lane & 7) * 4) = 0u;
      }
      SB();
      const int swz = (c15 >> 1) & 7;
      asm volatile("" : "=v"(r0), "=v"(p3), "=v"(p1));
#pragma unroll
      for (int h = 0; h < 2; ++h) {
        const int tl = ch ? 3 - h : h, tba = mt * TS + 40 + 40 * h;
        p1[4 * h] = aq + tba;
        p1[4 * h + 1] = akh + tba;
        p1[4 * h + 2] = g4 == 0 ? akh + tba : L_Z;
        p1[4 * h + 3] = av + tba;
        sc[4 + h] = (ho + (c < 4 ? c + 4 * h : 9 + h)) * 3840 + mt * 640;
#pragma unroll
        for (int q = 0; q < 3; ++q) {
          r0[8 + 2 * q + h] = BUF(q) + (rh * 2 + (c15 >> 3)) * 9216 + (c15 & 7) * 128 + (((4 * h + g4) ^ swz) * 16);
          const unsigned wa = BUF(q) + g4 * 9216 + q4 * 128 + ((tl ^ sw) * 32) + p4 * 8;
          if (q < 2) p2[12 + 2 * q + h] = wa;
          else p3[h] = wa;
        }
        p1[8 + h] = (rh * 48 + c15) * RS + 8 * c + 8 * (tl * 4 + g4);
      }
#pragma unroll
      for (int f = 0; f < 4; ++f) {
        p2[f] += ch ? 0 : 20480;
        p2[4 + f] = xa[f] + mt * 2048;
      }
      p2[8] = L_XT + (mt * 16 + kr) * 128 + (((lane & 7) ^ (kr >> 1)) * 16);
      p2[9] = ((tb + mt * 16 + kr) * 2 + par) * (DM * 2) + cg * 128 + (lane & 7) * 16;
      p2[10] = L_SMP + (mt * 16 + c15) * 8;
      p2[11] = (p2[8] + 1024) ^ 64;
      const bool tor = c < 4 && g4 >= 3 - c, dn = rh == 1 && c15 >= 8 && gi < 10;
      const int hidx = (g4 * 8 + c15 - 8) * 16;
      p3[2] = (wave == 0 && g4 < 5 - c && c > 0) ? (gb - 1) * INB + lane * 16 : (wave == 3 && tor) ? (gb + 1) * INB + lane * 16 : OOB;
      p3[5] = dn ? (gb + 10) * INB + 6144 + ch * 1536 + hidx : OOB;
      p3[4] = (wave == 1 && tor && c15 >= 8) ? (gb + 1) * INB + 9216 + hidx : p3[5];
      p3[3] = p3[2] != OOB ? p3[2] + 4096 : p3[4];
      p3[12] = p3[13] = p3[14] = p3[15] = 0x3F803F80u;
      PIN(p0); PIN(p1); PIN(p2); PIN(p3); PIN(r0);
      sc[2] = 0;
    }
    int ko = wave == 3 ? C_H + gb * 128 + c15 * 8 : c15 > 1 ? OOB : C_K + gb * 256 + wave * 64 + c15 * 32 + g4 * 8;
    PIN(ko);
    auto rows4 = [&](float (&p)[5]) AI {
      const auto a = __builtin_amdgcn_permlane32_swap(__float_as_uint(p[0]), __float_as_uint(p[2]), false, false);
      const auto c_ = __builtin_amdgcn_permlane32_swap(__float_as_uint(p[1]), __float_as_uint(p[3]), false, false);
      const auto e = __builtin_amdgcn_permlane16_swap(__float_as_uint(__uint_as_float(a[0]) + __uint_as_float(a[1])),
                                                      __float_as_uint(__uint_as_float(c_[0]) + __uint_as_float(c_[1])), false, false);
      return row_sum(__uint_as_float(e[0]) + __uint_as_float(e[1]) + p[4]);
    };
    SB();
    get_tag();
    if (wave == 3) __builtin_amdgcn_raw_buffer_store_b64((u32x2){0u, ~tag}, rc_, ko, 0, 16);
    int rowi = tj < 1024 ? 2047 - (2 * (tj0 + g4) + par) : -1;
    PIN(rowi);
    SB();
    if (wjob) {
      WAITV(0);
#pragma unroll
      for (int i = 0; i < 5; ++i) PIN(xs[i]);
#pragma unroll
      for (int i = 0; i < 4; ++i) PIN(wv[i]);
      float p[5], pg[5];
#pragma unroll
      for (int i = 0; i < 5; ++i) {
        p[i] = pg[i] = 0.f;
#pragma unroll
        for (int q = 0; q < 4; ++q) {
          p[i] = dot2(xs[i][q], wv[i == 4][q], p[i]);
          pg[i] = dot2(xs[i][q], wv[2 + (i == 4)][q], pg[i]);
        }
      }
      const float r_ = rows4(p), g_ = rbf(rows4(pg));
      unsigned u = pk2(r_, r_) & 0xFFFFu;
      u ^= (u & 0x8000u) ? 0xFFFFu : 0x8000u;
      const unsigned key = rowi >= 0 ? (u << 16) | (unsigned)rowi : 0u;
      __builtin_amdgcn_raw_buffer_store_b64((u32x2){c15 ? __float_as_uint(rbf(__builtin_amdgcn_rcpf(1.0f + __expf(-g_)))) : key, tag}, rc_, ko, 0, 16);
      SB();
    }
    WAITL0();
  }
  sc[0] = tag;
  sc[1] = wave;
  sc[3] = hw * 9216;
  sc[6] = 0;
  sc[7] = rg * 122880 + (hw + 1) * 640 + (wave >= 4 && (wave & 1) ? 30720 : 0);
  sc[8] = rg * 122880 + mt * 640 + (wave < 4 ? ((wave & 1) ? 0 : 15360) : (wave & 1) * 30720);
  sc[9] = (gi > 0 ? 8 : 0) | ((0x7801Eu >> (hw * 5 + c)) & 1);
  sc[10] = cg * 128;
  sc[11] = hw * 1024;
  sc[12] = ((hw >> 1) * 6 + (hw & 1)) * 1024;
  sc[13] = par;
  sc[14] = tb;
  sc[15] = mt;
  u32x16 uc;
#pragma unroll
  for (int i = 0; i < 16; ++i) {
    PINS(sc[i]);
    uc[i] = sc[i];
  }
  asm volatile(
    "s_nop 4\n"
    "s_load_dwordx4 s[92:95], s[90:91], 0x0\n"
    "s_load_dwordx2 s[44:45], s[90:91], 0x10\n"
    "s_load_dwordx4 s[96:99], s[90:91], 0x40\n"
    "s_waitcnt lgkmcnt(0)\n"
    "s_cmp_ge_u32 s69, 4\n"
    "s_cbranch_scc1 LOADER%=\n"
    "s_barrier\n"
    "ds_read_b64_tr_b16 v[72:73], v236 offset:1024\n"
    "ds_read_b64_tr_b16 v[74:75], v236 offset:1536\n"
    "ds_read_b128 v[96:99], v184 offset:3072\n"
    "ds_read_b64_tr_b16 v[76:77], v236 offset:2048\n"
    "ds_read_b64_tr_b16 v[78:79], v236 offset:2560\n"
    "ds_read_b64_tr_b16 v[80:81], v236\n"
    "ds_read_b64_tr_b16 v[82:83], v236 offset:512\n"
    "ds_read_b64_tr_b16 v[84:85], v237 offset:1024\n"
    "ds_read_b64_tr_b16 v[86:87], v237 offset:1536\n"
    "ds_read_b64_tr_b16 v[88:89], v237 offset:2048\n"
    "ds_read_b64_tr_b16 v[90:91], v237 offset:2560\n"
    "ds_read_b64_tr_b16 v[92:93], v237\n"
    "ds_read_b64_tr_b16 v[94:95], v237 offset:512\n"
    "ds_read_b128 v[100:103], v184 offset:7168\n"
    "ds_read_b128 v[104:107], v184 offset:8192\n"
    "s_waitcnt lgkmcnt(12)\n"
    "v_mfma_f32_16x16x32_bf16 v[0:3], v[72:75], v[96:99], 0\n"
    "ds_read_b64_tr_b16 v[108:109], v236 offset:5120\n"
    "s_waitcnt lgkmcnt(11)\n"
    "v_mfma_f32_16x16x32_bf16 v[4:7], v[76:79], v[96:99], 0\n"
    "ds_read_b64_tr_b16 v[110:111], v236 offset:5632\n"
    "s_waitcnt lgkmcnt(10)\n"
    "v_mfma_f32_16x16x32_bf16 v[8:11], v[80:83], v[96:99], 0\n"
    "ds_read_b128 v[132:135], v185 offset:3072\n"
    "s_waitcnt lgkmcnt(9)\n"
    "v_mfma_f32_16x16x32_bf16 v[12:15], v[84:87], v[96:99], 0\n"
    "ds_read_b64_tr_b16 v[112:113], v236 offset:6144\n"
    "s_waitcnt lgkmcnt(8)\n"
    "v_mfma_f32_16x16x32_bf16 v[16:19], v[88:91], v[96:99], 0\n"
    "ds_read_b64_tr_b16 v[114:115], v236 offset:6656\n"
    "s_waitcnt lgkmcnt(7)\n"
    "v_mfma_f32_16x16x32_bf16 v[20:23], v[92:95], v[96:99], 0\n"
    "ds_read_b64_tr_b16 v[116:117], v236 offset:4096\n"
    "s_waitcnt lgkmcnt(7)\n"
    "v_mfma_f32_16x16x32_bf16 v[24:27], v[72:75], v[100:103], 0\n"
    "ds_read_b64_tr_b16 v[118:119], v236 offset:4608\n"
    "v_mfma_f32_16x16x32_bf16 v[28:31], v[76:79], v[100:103], 0\n"
    "ds_read_b64_tr_b16 v[120:121], v237 offset:5120\n"
    "v_mfma_f32_16x16x32_bf16 v[32:35], v[80:83], v[100:103], 0\n"
    "ds_read_b64_tr_b16 v[122:123], v237 offset:5632\n"
    "v_mfma_f32_16x16x32_bf16 v[36:39], v[84:87], v[100:103], 0\n"
    "ds_read_b64_tr_b16 v[124:125], v237 offset:6144\n"
    "v_mfma_f32_16x16x32_bf16 v[40:43], v[88:91], v[100:103], 0\n"
    "ds_read_b64_tr_b16 v[126:127], v237 offset:6656\n"
    "v_mfma_f32_16x16x32_bf16 v[44:47], v[92:95], v[100:103], 0\n"
    "ds_read_b64_tr_b16 v[128:129], v237 offset:4096\n"
    "s_waitcnt lgkmcnt(12)\n"
    "v_mfma_f32_16x16x32_bf16 v[48:51], v[72:75], v[104:107], 0\n"
    "ds_read_b64_tr_b16 v[130:131], v237 offset:4608\n"
    "v_mfma_f32_16x16x32_bf16 v[52:55], v[76:79], v[104:107], 0\n"
    "ds_read_b128 v[136:139], v185 offset:7168\n"
    "v_mfma_f32_16x16x32_bf16 v[56:59], v[80:83], v[104:107], 0\n"
    "ds_read_b128 v[140:143], v185 offset:8192\n"
    "v_mfma_f32_16x16x32_bf16 v[60:63], v[84:87], v[104:107], 0\n"
    "s_mov_b32 s48, s96\n"
    "s_and_b32 s49, s97, 65535\n"
    "v_mfma_f32_16x16x32_bf16 v[64:67], v[88:91], v[104:107], 0\n"
    "s_mov_b32 s50, 2703360\n"
    "s_mov_b32 s51, 131072\n"
    "v_mfma_f32_16x16x32_bf16 v[68:71], v[92:95], v[104:107], 0\n"
    "s_add_u32 s52, s96, 2703360\n"
    "s_addc_u32 s53, s97, 0\n"
    "s_waitcnt lgkmcnt(6)\n"
    "v_mfma_f32_16x16x32_bf16 v[0:3], v[108:111], v[132:135], v[0:3]\n"
    "s_and_b32 s53, s53, 65535\n"
    "s_mov_b32 s54, 2252800\n"
    "v_mfma_f32_16x16x32_bf16 v[4:7], v[112:115], v[132:135], v[4:7]\n"
    "s_mov_b32 s55, 131072\n"
    "s_mov_b32 s56, s98\n"
    "v_mfma_f32_16x16x32_bf16 v[8:11], v[116:119], v[132:135], v[8:11]\n"
    "s_and_b32 s57, s99, 65535\n"
    "s_mov_b32 s58, 2621440\n"
    "s_waitcnt lgkmcnt(0)\n"
    "s_barrier\n"
    "v_mfma_f32_16x16x32_bf16 v[12:15], v[120:123], v[132:135], v[12:15]\n"
    "ds_read_b64_tr_b16 v[72:73], v238 offset:1024\n"
    "ds_read_b64_tr_b16 v[74:75], v238 offset:1536\n"
    "v_mfma_f32_16x16x32_bf16 v[16:19], v[124:127], v[132:135], v[16:19]\n"
    "ds_read_b128 v[96:99], v186 offset:3072\n"
    "v_mfma_f32_16x16x32_bf16 v[20:23], v[128:131], v[132:135], v[20:23]\n"
    "ds_read_b64_tr_b16 v[76:77], v238 offset:2048\n"
    "v_mfma_f32_16x16x32_bf16 v[24:27], v[108:111], v[136:139], v[24:27]\n"
    "ds_read_b64_tr_b16 v[78:79], v238 offset:2560\n"
    "v_mfma_f32_16x16x32_bf16 v[28:31], v[112:115], v[136:139], v[28:31]\n"
    "ds_read_b64_tr_b16 v[80:81], v238\n"
    "v_mfma_f32_16x16x32_bf16 v[32:35], v[116:119], v[136:139], v[32:35]\n"
    "ds_read_b64_tr_b16 v[82:83], v238 offset:512\n"
    "v_mfma_f32_16x16x32_bf16 v[36:39], v[120:123], v[136:139], v[36:39]\n"
    "ds_read_b64_tr_b16 v[84:85], v239 offset:1024\n"
    "v_mfma_f32_16x16x32_bf16 v[40:43], v[124:127], v[136:139], v[40:43]\n"
    "ds_read_b64_tr_b16 v[86:87], v239 offset:1536\n"
    "v_mfma_f32_16x16x32_bf16 v[44:47], v[128:131], v[136:139], v[44:47]\n"
    "ds_read_b64_tr_b16 v[88:89], v239 offset:2048\n"
    "v_mfma_f32_16x16x32_bf16 v[48:51], v[108:111], v[140:143], v[48:51]\n"
    "ds_read_b64_tr_b16 v[90:91], v239 offset:2560\n"
    "v_mfma_f32_16x16x32_bf16 v[52:55], v[112:115], v[140:143], v[52:55]\n"
    "ds_read_b64_tr_b16 v[92:93], v239\n"
    "v_mfma_f32_16x16x32_bf16 v[56:59], v[116:119], v[140:143], v[56:59]\n"
    "ds_read_b64_tr_b16 v[94:95], v239 offset:512\n"
    "v_mfma_f32_16x16x32_bf16 v[60:63], v[120:123], v[140:143], v[60:63]\n"
    "ds_read_b128 v[100:103], v186 offset:7168\n"
    "v_mfma_f32_16x16x32_bf16 v[64:67], v[124:127], v[140:143], v[64:67]\n"
    "ds_read_b128 v[104:107], v186 offset:8192\n"
    "v_mfma_f32_16x16x32_bf16 v[68:71], v[128:131], v[140:143], v[68:71]\n"
    "s_mov_b32 s59, 131072\n"
    "s_add_u32 s64, s96, 5102592\n"
    "s_waitcnt lgkmcnt(9)\n"
    "v_mfma_f32_16x16x32_bf16 v[0:3], v[72:75], v[96:99], v[0:3]\n"
    "ds_read_b64_tr_b16 v[108:109], v238 offset:5120\n"
    "v_mfma_f32_16x16x32_bf16 v[4:7], v[76:79], v[96:99], v[4:7]\n"
    "ds_read_b64_tr_b16 v[110:111], v238 offset:5632\n"
    "s_waitcnt lgkmcnt(10)\n"
    "v_mfma_f32_16x16x32_bf16 v[8:11], v[80:83], v[96:99], v[8:11]\n"
    "ds_read_b128 v[132:135], v187 offset:3072\n"
    "s_waitcnt lgkmcnt(9)\n"
    "v_mfma_f32_16x16x32_bf16 v[12:15], v[84:87], v[96:99], v[12:15]\n"
    "ds_read_b64_tr_b16 v[112:113], v238 offset:6144\n"
    "s_waitcnt lgkmcnt(4)\n"
    "v_mfma_f32_16x16x32_bf16 v[16:19], v[88:91], v[96:99], v[16:19]\n"
    "ds_read_b64_tr_b16 v[114:115], v238 offset:6656\n"
    "v_mfma_f32_16x16x32_bf16 v[20:23], v[92:95], v[96:99], v[20:23]\n"
    "ds_read_b64_tr_b16 v[116:117], v238 offset:4096\n"
    "v_mfma_f32_16x16x32_bf16 v[24:27], v[72:75], v[100:103], v[24:27]\n"
    "ds_read_b64_tr_b16 v[118:119], v238 offset:4608\n"
    "v_mfma_f32_16x16x32_bf16 v[28:31], v[76:79], v[100:103], v[28:31]\n"
    "ds_read_b64_tr_b16 v[120:121], v239 offset:5120\n"
    "v_mfma_f32_16x16x32_bf16 v[32:35], v[80:83], v[100:103], v[32:35]\n"
    "ds_read_b64_tr_b16 v[122:123], v239 offset:5632\n"
    "v_mfma_f32_16x16x32_bf16 v[36:39], v[84:87], v[100:103], v[36:39]\n"
    "ds_read_b64_tr_b16 v[124:125], v239 offset:6144\n"
    "v_mfma_f32_16x16x32_bf16 v[40:43], v[88:91], v[100:103], v[40:43]\n"
    "ds_read_b64_tr_b16 v[126:127], v239 offset:6656\n"
    "v_mfma_f32_16x16x32_bf16 v[44:47], v[92:95], v[100:103], v[44:47]\n"
    "ds_read_b64_tr_b16 v[128:129], v239 offset:4096\n"
    "v_mfma_f32_16x16x32_bf16 v[48:51], v[72:75], v[104:107], v[48:51]\n"
    "ds_read_b64_tr_b16 v[130:131], v239 offset:4608\n"
    "v_mfma_f32_16x16x32_bf16 v[52:55], v[76:79], v[104:107], v[52:55]\n"
    "ds_read_b128 v[136:139], v187 offset:7168\n"
    "v_mfma_f32_16x16x32_bf16 v[56:59], v[80:83], v[104:107], v[56:59]\n"
    "ds_read_b128 v[140:143], v187 offset:8192\n"
    "v_mfma_f32_16x16x32_bf16 v[60:63], v[84:87], v[104:107], v[60:63]\n"
    "s_addc_u32 s65, s97, 0\n"
    "s_and_b32 s65, s65, 65535\n"
    "v_mfma_f32_16x16x32_bf16 v[64:67], v[88:91], v[104:107], v[64:67]\n"
    "s_mov_b32 s66, 276480\n"
    "s_mov_b32 s67, 131072\n"
    "v_mfma_f32_16x16x32_bf16 v[68:71], v[92:95], v[104:107], v[68:71]\n"
    "s_lshl_b32 s85, s85, 7\n"
    "s_add_u32 s85, s85, 113408\n"
    "s_waitcnt lgkmcnt(6)\n"
    "v_mfma_f32_16x16x32_bf16 v[0:3], v[108:111], v[132:135], v[0:3]\n"
    "v_mfma_f32_16x16x32_bf16 v[4:7], v[112:115], v[132:135], v[4:7]\n"
    "v_mfma_f32_16x16x32_bf16 v[8:11], v[116:119], v[132:135], v[8:11]\n"
    "s_waitcnt lgkmcnt(0)\n"
    "s_barrier\n"
    "v_mfma_f32_16x16x32_bf16 v[12:15], v[120:123], v[132:135], v[12:15]\n"
    "ds_read_b64_tr_b16 v[72:73], v240 offset:1024\n"
    "ds_read_b64_tr_b16 v[74:75], v240 offset:1536\n"
    "v_mfma_f32_16x16x32_bf16 v[16:19], v[124:127], v[132:135], v[16:19]\n"
    "ds_read_b128 v[96:99], v188 offset:3072\n"
    "v_mfma_f32_16x16x32_bf16 v[20:23], v[128:131], v[132:135], v[20:23]\n"
    "ds_read_b64_tr_b16 v[76:77], v240 offset:2048\n"
    "v_mfma_f32_16x16x32_bf16 v[24:27], v[108:111], v[136:139], v[24:27]\n"
    "ds_read_b64_tr_b16 v[78:79], v240 offset:2560\n"
    "v_mfma_f32_16x16x32_bf16 v[28:31], v[112:115], v[136:139], v[28:31]\n"
    "ds_read_b64_tr_b16 v[80:81], v240\n"
    "v_mfma_f32_16x16x32_bf16 v[32:35], v[116:119], v[136:139], v[32:35]\n"
    "ds_read_b64_tr_b16 v[82:83], v240 offset:512\n"
    "v_mfma_f32_16x16x32_bf16 v[36:39], v[120:123], v[136:139], v[36:39]\n"
    "ds_read_b64_tr_b16 v[84:85], v241 offset:1024\n"
    "v_mfma_f32_16x16x32_bf16 v[40:43], v[124:127], v[136:139], v[40:43]\n"
    "ds_read_b64_tr_b16 v[86:87], v241 offset:1536\n"
    "v_mfma_f32_16x16x32_bf16 v[44:47], v[128:131], v[136:139], v[44:47]\n"
    "ds_read_b64_tr_b16 v[88:89], v241 offset:2048\n"
    "v_mfma_f32_16x16x32_bf16 v[48:51], v[108:111], v[140:143], v[48:51]\n"
    "ds_read_b64_tr_b16 v[90:91], v241 offset:2560\n"
    "v_mfma_f32_16x16x32_bf16 v[52:55], v[112:115], v[140:143], v[52:55]\n"
    "ds_read_b64_tr_b16 v[92:93], v241\n"
    "v_mfma_f32_16x16x32_bf16 v[56:59], v[116:119], v[140:143], v[56:59]\n"
    "ds_read_b64_tr_b16 v[94:95], v241 offset:512\n"
    "v_mfma_f32_16x16x32_bf16 v[60:63], v[120:123], v[140:143], v[60:63]\n"
    "ds_read_b128 v[100:103], v188 offset:7168\n"
    "v_mfma_f32_16x16x32_bf16 v[64:67], v[124:127], v[140:143], v[64:67]\n"
    "ds_read_b128 v[104:107], v188 offset:8192\n"
    "v_mfma_f32_16x16x32_bf16 v[68:71], v[128:131], v[140:143], v[68:71]\n"
    "s_waitcnt lgkmcnt(9)\n"
    "v_mfma_f32_16x16x32_bf16 v[0:3], v[72:75], v[96:99], v[0:3]\n"
    "ds_read_b64_tr_b16 v[108:109], v240 offset:5120\n"
    "v_mfma_f32_16x16x32_bf16 v[4:7], v[76:79], v[96:99], v[4:7]\n"
    "ds_read_b64_tr_b16 v[110:111], v240 offset:5632\n"
    "s_waitcnt lgkmcnt(10)\n"
    "v_mfma_f32_16x16x32_bf16 v[8:11], v[80:83], v[96:99], v[8:11]\n"
    "ds_read_b128 v[132:135], v189 offset:3072\n"
    "s_waitcnt lgkmcnt(9)\n"
    "v_mfma_f32_16x16x32_bf16 v[12:15], v[84:87], v[96:99], v[12:15]\n"
    "ds_read_b64_tr_b16 v[112:113], v240 offset:6144\n"
    "s_waitcnt lgkmcnt(4)\n"
    "v_mfma_f32_16x16x32_bf16 v[16:19], v[88:91], v[96:99], v[16:19]\n"
    "ds_read_b64_tr_b16 v[114:115], v240 offset:6656\n"
    "v_mfma_f32_16x16x32_bf16 v[20:23], v[92:95], v[96:99], v[20:23]\n"
    "ds_read_b64_tr_b16 v[116:117], v240 offset:4096\n"
    "v_mfma_f32_16x16x32_bf16 v[24:27], v[72:75], v[100:103], v[24:27]\n"
    "ds_read_b64_tr_b16 v[118:119], v240 offset:4608\n"
    "v_mfma_f32_16x16x32_bf16 v[28:31], v[76:79], v[100:103], v[28:31]\n"
    "ds_read_b64_tr_b16 v[120:121], v241 offset:5120\n"
    "v_mfma_f32_16x16x32_bf16 v[32:35], v[80:83], v[100:103], v[32:35]\n"
    "ds_read_b64_tr_b16 v[122:123], v241 offset:5632\n"
    "v_mfma_f32_16x16x32_bf16 v[36:39], v[84:87], v[100:103], v[36:39]\n"
    "ds_read_b64_tr_b16 v[124:125], v241 offset:6144\n"
    "v_mfma_f32_16x16x32_bf16 v[40:43], v[88:91], v[100:103], v[40:43]\n"
    "ds_read_b64_tr_b16 v[126:127], v241 offset:6656\n"
    "v_mfma_f32_16x16x32_bf16 v[44:47], v[92:95], v[100:103], v[44:47]\n"
    "ds_read_b64_tr_b16 v[128:129], v241 offset:4096\n"
    "v_mfma_f32_16x16x32_bf16 v[48:51], v[72:75], v[104:107], v[48:51]\n"
    "ds_read_b64_tr_b16 v[130:131], v241 offset:4608\n"
    "v_mfma_f32_16x16x32_bf16 v[52:55], v[76:79], v[104:107], v[52:55]\n"
    "ds_read_b128 v[136:139], v189 offset:7168\n"
    "v_mfma_f32_16x16x32_bf16 v[56:59], v[80:83], v[104:107], v[56:59]\n"
    "ds_read_b128 v[140:143], v189 offset:8192\n"
    "v_mfma_f32_16x16x32_bf16 v[60:63], v[84:87], v[104:107], v[60:63]\n"
    "v_mfma_f32_16x16x32_bf16 v[64:67], v[88:91], v[104:107], v[64:67]\n"
    "v_mfma_f32_16x16x32_bf16 v[68:71], v[92:95], v[104:107], v[68:71]\n"
    "s_waitcnt lgkmcnt(6)\n"
    "v_mfma_f32_16x16x32_bf16 v[0:3], v[108:111], v[132:135], v[0:3]\n"
    "v_mfma_f32_16x16x32_bf16 v[4:7], v[112:115], v[132:135], v[4:7]\n"
    "v_mfma_f32_16x16x32_bf16 v[8:11], v[116:119], v[132:135], v[8:11]\n"
    "s_waitcnt lgkmcnt(0)\n"
    "s_barrier\n"
    "v_mfma_f32_16x16x32_bf16 v[12:15], v[120:123], v[132:135], v[12:15]\n"
    "ds_read_b64_tr_b16 v[72:73], v236 offset:1024\n"
    "ds_read_b64_tr_b16 v[74:75], v236 offset:1536\n"
    "v_mfma_f32_16x16x32_bf16 v[16:19], v[124:127], v[132:135], v[16:19]\n"
    "ds_read_b128 v[96:99], v184 offset:3072\n"
    "v_mfma_f32_16x16x32_bf16 v[20:23], v[128:131], v[132:135], v[20:23]\n"
    "ds_read_b64_tr_b16 v[76:77], v236 offset:2048\n"
    "v_mfma_f32_16x16x32_bf16 v[24:27], v[108:111], v[136:139], v[24:27]\n"
    "ds_read_b64_tr_b16 v[78:79], v236 offset:2560\n"
    "v_mfma_f32_16x16x32_bf16 v[28:31], v[112:115], v[136:139], v[28:31]\n"
    "ds_read_b64_tr_b16 v[80:81], v236\n"
    "v_mfma_f32_16x16x32_bf16 v[32:35], v[116:119], v[136:139], v[32:35]\n"
    "ds_read_b64_tr_b16 v[82:83], v236 offset:512\n"
    "v_mfma_f32_16x16x32_bf16 v[36:39], v[120:123], v[136:139], v[36:39]\n"
    "ds_read_b64_tr_b16 v[84:85], v237 offset:1024\n"
    "v_mfma_f32_16x16x32_bf16 v[40:43], v[124:127], v[136:139], v[40:43]\n"
    "ds_read_b64_tr_b16 v[86:87], v237 offset:1536\n"
    "v_mfma_f32_16x16x32_bf16 v[44:47], v[128:131], v[136:139], v[44:47]\n"
    "ds_read_b64_tr_b16 v[88:89], v237 offset:2048\n"
    "v_mfma_f32_16x16x32_bf16 v[48:51], v[108:111], v[140:143], v[48:51]\n"
    "ds_read_b64_tr_b16 v[90:91], v237 offset:2560\n"
    "v_mfma_f32_16x16x32_bf16 v[52:55], v[112:115], v[140:143], v[52:55]\n"
    "ds_read_b64_tr_b16 v[92:93], v237\n"
    "v_mfma_f32_16x16x32_bf16 v[56:59], v[116:119], v[140:143], v[56:59]\n"
    "ds_read_b64_tr_b16 v[94:95], v237 offset:512\n"
    "v_mfma_f32_16x16x32_bf16 v[60:63], v[120:123], v[140:143], v[60:63]\n"
    "ds_read_b128 v[100:103], v184 offset:7168\n"
    "v_mfma_f32_16x16x32_bf16 v[64:67], v[124:127], v[140:143], v[64:67]\n"
    "ds_read_b128 v[104:107], v184 offset:8192\n"
    "v_mfma_f32_16x16x32_bf16 v[68:71], v[128:131], v[140:143], v[68:71]\n"
    "s_waitcnt lgkmcnt(9)\n"
    "v_mfma_f32_16x16x32_bf16 v[0:3], v[72:75], v[96:99], v[0:3]\n"
    "ds_read_b64_tr_b16 v[108:109], v236 offset:5120\n"
    "v_mfma_f32_16x16x32_bf16 v[4:7], v[76:79], v[96:99], v[4:7]\n"
    "ds_read_b64_tr_b16 v[110:111], v236 offset:5632\n"
    "s_waitcnt lgkmcnt(10)\n"
    "v_mfma_f32_16x16x32_bf16 v[8:11], v[80:83], v[96:99], v[8:11]\n"
    "ds_read_b128 v[132:135], v185 offset:3072\n"
    "s_waitcnt lgkmcnt(9)\n"
    "v_mfma_f32_16x16x32_bf16 v[12:15], v[84:87], v[96:99], v[12:15]\n"
    "ds_read_b64_tr_b16 v[112:113], v236 offset:6144\n"
    "s_waitcnt lgkmcnt(4)\n"
    "v_mfma_f32_16x16x32_bf16 v[16:19], v[88:91], v[96:99], v[16:19]\n"
    "ds_read_b64_tr_b16 v[114:115], v236 offset:6656\n"
    "v_mfma_f32_16x16x32_bf16 v[20:23], v[92:95], v[96:99], v[20:23]\n"
    "ds_read_b64_tr_b16 v[116:117], v236 offset:4096\n"
    "v_mfma_f32_16x16x32_bf16 v[24:27], v[72:75], v[100:103], v[24:27]\n"
    "ds_read_b64_tr_b16 v[118:119], v236 offset:4608\n"
    "v_mfma_f32_16x16x32_bf16 v[28:31], v[76:79], v[100:103], v[28:31]\n"
    "ds_read_b64_tr_b16 v[120:121], v237 offset:5120\n"
    "v_mfma_f32_16x16x32_bf16 v[32:35], v[80:83], v[100:103], v[32:35]\n"
    "ds_read_b64_tr_b16 v[122:123], v237 offset:5632\n"
    "v_mfma_f32_16x16x32_bf16 v[36:39], v[84:87], v[100:103], v[36:39]\n"
    "ds_read_b64_tr_b16 v[124:125], v237 offset:6144\n"
    "v_mfma_f32_16x16x32_bf16 v[40:43], v[88:91], v[100:103], v[40:43]\n"
    "ds_read_b64_tr_b16 v[126:127], v237 offset:6656\n"
    "v_mfma_f32_16x16x32_bf16 v[44:47], v[92:95], v[100:103], v[44:47]\n"
    "ds_read_b64_tr_b16 v[128:129], v237 offset:4096\n"
    "v_mfma_f32_16x16x32_bf16 v[48:51], v[72:75], v[104:107], v[48:51]\n"
    "ds_read_b64_tr_b16 v[130:131], v237 offset:4608\n"
    "v_mfma_f32_16x16x32_bf16 v[52:55], v[76:79], v[104:107], v[52:55]\n"
    "ds_read_b128 v[136:139], v185 offset:7168\n"
    "v_mfma_f32_16x16x32_bf16 v[56:59], v[80:83], v[104:107], v[56:59]\n"
    "ds_read_b128 v[140:143], v185 offset:8192\n"
    "v_mfma_f32_16x16x32_bf16 v[60:63], v[84:87], v[104:107], v[60:63]\n"
    "v_mfma_f32_16x16x32_bf16 v[64:67], v[88:91], v[104:107], v[64:67]\n"
    "v_mfma_f32_16x16x32_bf16 v[68:71], v[92:95], v[104:107], v[68:71]\n"
    "s_waitcnt lgkmcnt(6)\n"
    "v_mfma_f32_16x16x32_bf16 v[0:3], v[108:111], v[132:135], v[0:3]\n"
    "v_mfma_f32_16x16x32_bf16 v[4:7], v[112:115], v[132:135], v[4:7]\n"
    "v_mfma_f32_16x16x32_bf16 v[8:11], v[116:119], v[132:135], v[8:11]\n"
    "s_waitcnt lgkmcnt(0)\n"
    "s_barrier\n"
    "v_mfma_f32_16x16x32_bf16 v[12:15], v[120:123], v[132:135], v[12:15]\n"
    "ds_read_b64_tr_b16 v[72:73], v238 offset:1024\n"
    "ds_read_b64_tr_b16 v[74:75], v238 offset:1536\n"
    "v_mfma_f32_16x16x32_bf16 v[16:19], v[124:127], v[132:135], v[16:19]\n"
    "ds_read_b128 v[96:99], v186 offset:3072\n"
    "v_mfma_f32_16x16x32_bf16 v[20:23], v[128:131], v[132:135], v[20:23]\n"
    "ds_read_b64_tr_b16 v[76:77], v238 offset:2048\n"
    "v_mfma_f32_16x16x32_bf16 v[24:27], v[108:111], v[136:139], v[24:27]\n"
    "ds_read_b64_tr_b16 v[78:79], v238 offset:2560\n"
    "v_mfma_f32_16x16x32_bf16 v[28:31], v[112:115], v[136:139], v[28:31]\n"
    "ds_read_b64_tr_b16 v[80:81], v238\n"
    "v_mfma_f32_16x16x32_bf16 v[32:35], v[116:119], v[136:139], v[32:35]\n"
    "ds_read_b64_tr_b16 v[82:83], v238 offset:512\n"
    "v_mfma_f32_16x16x32_bf16 v[36:39], v[120:123], v[136:139], v[36:39]\n"
    "ds_read_b64_tr_b16 v[84:85], v239 offset:1024\n"
    "v_mfma_f32_16x16x32_bf16 v[40:43], v[124:127], v[136:139], v[40:43]\n"
    "ds_read_b64_tr_b16 v[86:87], v239 offset:1536\n"
    "v_mfma_f32_16x16x32_bf16 v[44:47], v[128:131], v[136:139], v[44:47]\n"
    "ds_read_b64_tr_b16 v[88:89], v239 offset:2048\n"
    "v_mfma_f32_16x16x32_bf16 v[48:51], v[108:111], v[140:143], v[48:51]\n"
    "ds_read_b64_tr_b16 v[90:91], v239 offset:2560\n"
    "v_mfma_f32_16x16x32_bf16 v[52:55], v[112:115], v[140:143], v[52:55]\n"
    "ds_read_b64_tr_b16 v[92:93], v239\n"
    "v_mfma_f32_16x16x32_bf16 v[56:59], v[116:119], v[140:143], v[56:59]\n"
    "ds_read_b64_tr_b16 v[94:95], v239 offset:512\n"
    "v_mfma_f32_16x16x32_bf16 v[60:63], v[120:123], v[140:143], v[60:63]\n"
    "ds_read_b128 v[100:103], v186 offset:7168\n"
    "v_mfma_f32_16x16x32_bf16 v[64:67], v[124:127], v[140:143], v[64:67]\n"
    "ds_read_b128 v[104:107], v186 offset:8192\n"
    "v_mfma_f32_16x16x32_bf16 v[68:71], v[128:131], v[140:143], v[68:71]\n"
    "s_waitcnt lgkmcnt(9)\n"
    "v_mfma_f32_16x16x32_bf16 v[0:3], v[72:75], v[96:99], v[0:3]\n"
    "ds_read_b64_tr_b16 v[108:109], v238 offset:5120\n"
    "v_mfma_f32_16x16x32_bf16 v[4:7], v[76:79], v[96:99], v[4:7]\n"
    "ds_read_b64_tr_b16 v[110:111], v238 offset:5632\n"
    "s_waitcnt lgkmcnt(10)\n"
    "v_mfma_f32_16x16x32_bf16 v[8:11], v[80:83], v[96:99], v[8:11]\n"
    "ds_read_b128 v[132:135], v187 offset:3072\n"
    "s_waitcnt lgkmcnt(9)\n"
    "v_mfma_f32_16x16x32_bf16 v[12:15], v[84:87], v[96:99], v[12:15]\n"
    "ds_read_b64_tr_b16 v[112:113], v238 offset:6144\n"
    "s_waitcnt lgkmcnt(4)\n"
    "v_mfma_f32_16x16x32_bf16 v[16:19], v[88:91], v[96:99], v[16:19]\n"
    "ds_read_b64_tr_b16 v[114:115], v238 offset:6656\n"
    "v_mfma_f32_16x16x32_bf16 v[20:23], v[92:95], v[96:99], v[20:23]\n"
    "ds_read_b64_tr_b16 v[116:117], v238 offset:4096\n"
    "v_mfma_f32_16x16x32_bf16 v[24:27], v[72:75], v[100:103], v[24:27]\n"
    "ds_read_b64_tr_b16 v[118:119], v238 offset:4608\n"
    "v_mfma_f32_16x16x32_bf16 v[28:31], v[76:79], v[100:103], v[28:31]\n"
    "ds_read_b64_tr_b16 v[120:121], v239 offset:5120\n"
    "v_mfma_f32_16x16x32_bf16 v[32:35], v[80:83], v[100:103], v[32:35]\n"
    "ds_read_b64_tr_b16 v[122:123], v239 offset:5632\n"
    "v_mfma_f32_16x16x32_bf16 v[36:39], v[84:87], v[100:103], v[36:39]\n"
    "ds_read_b64_tr_b16 v[124:125], v239 offset:6144\n"
    "v_mfma_f32_16x16x32_bf16 v[40:43], v[88:91], v[100:103], v[40:43]\n"
    "ds_read_b64_tr_b16 v[126:127], v239 offset:6656\n"
    "v_mfma_f32_16x16x32_bf16 v[44:47], v[92:95], v[100:103], v[44:47]\n"
    "ds_read_b64_tr_b16 v[128:129], v239 offset:4096\n"
    "v_mfma_f32_16x16x32_bf16 v[48:51], v[72:75], v[104:107], v[48:51]\n"
    "ds_read_b64_tr_b16 v[130:131], v239 offset:4608\n"
    "v_mfma_f32_16x16x32_bf16 v[52:55], v[76:79], v[104:107], v[52:55]\n"
    "ds_read_b128 v[136:139], v187 offset:7168\n"
    "v_mfma_f32_16x16x32_bf16 v[56:59], v[80:83], v[104:107], v[56:59]\n"
    "ds_read_b128 v[140:143], v187 offset:8192\n"
    "v_mfma_f32_16x16x32_bf16 v[60:63], v[84:87], v[104:107], v[60:63]\n"
    "v_mfma_f32_16x16x32_bf16 v[64:67], v[88:91], v[104:107], v[64:67]\n"
    "v_mfma_f32_16x16x32_bf16 v[68:71], v[92:95], v[104:107], v[68:71]\n"
    "s_waitcnt lgkmcnt(6)\n"
    "v_mfma_f32_16x16x32_bf16 v[0:3], v[108:111], v[132:135], v[0:3]\n"
    "v_mfma_f32_16x16x32_bf16 v[4:7], v[112:115], v[132:135], v[4:7]\n"
    "v_mfma_f32_16x16x32_bf16 v[8:11], v[116:119], v[132:135], v[8:11]\n"
    "s_waitcnt lgkmcnt(0)\n"
    "s_barrier\n"
    "v_mfma_f32_16x16x32_bf16 v[12:15], v[120:123], v[132:135], v[12:15]\n"
    "ds_read_b64_tr_b16 v[72:73], v240 offset:1024\n"
    "ds_read_b64_tr_b16 v[74:75], v240 offset:1536\n"
    "v_mfma_f32_16x16x32_bf16 v[16:19], v[124:127], v[132:135], v[16:19]\n"
    "ds_read_b128 v[96:99], v188 offset:3072\n"
    "v_mfma_f32_16x16x32_bf16 v[20:23], v[128:131], v[132:135], v[20:23]\n"
    "ds_read_b64_tr_b16 v[76:77], v240 offset:2048\n"
    "v_mfma_f32_16x16x32_bf16 v[24:27], v[108:111], v[136:139], v[24:27]\n"
    "ds_read_b64_tr_b16 v[78:79], v240 offset:2560\n"
    "v_mfma_f32_16x16x32_bf16 v[28:31], v[112:115], v[136:139], v[28:31]\n"
    "ds_read_b64_tr_b16 v[80:81], v240\n"
    "v_mfma_f32_16x16x32_bf16 v[32:35], v[116:119], v[136:139], v[32:35]\n"
    "ds_read_b64_tr_b16 v[82:83], v240 offset:512\n"
    "v_mfma_f32_16x16x32_bf16 v[36:39], v[120:123], v[136:139], v[36:39]\n"
    "ds_read_b64_tr_b16 v[84:85], v241 offset:1024\n"
    "v_mfma_f32_16x16x32_bf16 v[40:43], v[124:127], v[136:139], v[40:43]\n"
    "ds_read_b64_tr_b16 v[86:87], v241 offset:1536\n"
    "v_mfma_f32_16x16x32_bf16 v[44:47], v[128:131], v[136:139], v[44:47]\n"
    "ds_read_b64_tr_b16 v[88:89], v241 offset:2048\n"
    "v_mfma_f32_16x16x32_bf16 v[48:51], v[108:111], v[140:143], v[48:51]\n"
    "ds_read_b64_tr_b16 v[90:91], v241 offset:2560\n"
    "v_mfma_f32_16x16x32_bf16 v[52:55], v[112:115], v[140:143], v[52:55]\n"
    "ds_read_b64_tr_b16 v[92:93], v241\n"
    "v_mfma_f32_16x16x32_bf16 v[56:59], v[116:119], v[140:143], v[56:59]\n"
    "ds_read_b64_tr_b16 v[94:95], v241 offset:512\n"
    "v_mfma_f32_16x16x32_bf16 v[60:63], v[120:123], v[140:143], v[60:63]\n"
    "ds_read_b128 v[100:103], v188 offset:7168\n"
    "v_mfma_f32_16x16x32_bf16 v[64:67], v[124:127], v[140:143], v[64:67]\n"
    "ds_read_b128 v[104:107], v188 offset:8192\n"
    "v_mfma_f32_16x16x32_bf16 v[68:71], v[128:131], v[140:143], v[68:71]\n"
    "s_waitcnt lgkmcnt(9)\n"
    "v_mfma_f32_16x16x32_bf16 v[0:3], v[72:75], v[96:99], v[0:3]\n"
    "ds_read_b64_tr_b16 v[108:109], v240 offset:5120\n"
    "v_mfma_f32_16x16x32_bf16 v[4:7], v[76:79], v[96:99], v[4:7]\n"
    "ds_read_b64_tr_b16 v[110:111], v240 offset:5632\n"
    "s_waitcnt lgkmcnt(10)\n"
    "v_mfma_f32_16x16x32_bf16 v[8:11], v[80:83], v[96:99], v[8:11]\n"
    "ds_read_b128 v[132:135], v189 offset:3072\n"
    "s_waitcnt lgkmcnt(9)\n"
    "v_mfma_f32_16x16x32_bf16 v[12:15], v[84:87], v[96:99], v[12:15]\n"
    "ds_read_b64_tr_b16 v[112:113], v240 offset:6144\n"
    "s_waitcnt lgkmcnt(4)\n"
    "v_mfma_f32_16x16x32_bf16 v[16:19], v[88:91], v[96:99], v[16:19]\n"
    "ds_read_b64_tr_b16 v[114:115], v240 offset:6656\n"
    "v_mfma_f32_16x16x32_bf16 v[20:23], v[92:95], v[96:99], v[20:23]\n"
    "ds_read_b64_tr_b16 v[116:117], v240 offset:4096\n"
    "v_mfma_f32_16x16x32_bf16 v[24:27], v[72:75], v[100:103], v[24:27]\n"
    "ds_read_b64_tr_b16 v[118:119], v240 offset:4608\n"
    "v_mfma_f32_16x16x32_bf16 v[28:31], v[76:79], v[100:103], v[28:31]\n"
    "ds_read_b64_tr_b16 v[120:121], v241 offset:5120\n"
    "v_mfma_f32_16x16x32_bf16 v[32:35], v[80:83], v[100:103], v[32:35]\n"
    "ds_read_b64_tr_b16 v[122:123], v241 offset:5632\n"
    "v_mfma_f32_16x16x32_bf16 v[36:39], v[84:87], v[100:103], v[36:39]\n"
    "ds_read_b64_tr_b16 v[124:125], v241 offset:6144\n"
    "v_mfma_f32_16x16x32_bf16 v[40:43], v[88:91], v[100:103], v[40:43]\n"
    "ds_read_b64_tr_b16 v[126:127], v241 offset:6656\n"
    "v_mfma_f32_16x16x32_bf16 v[44:47], v[92:95], v[100:103], v[44:47]\n"
    "ds_read_b64_tr_b16 v[128:129], v241 offset:4096\n"
    "v_mfma_f32_16x16x32_bf16 v[48:51], v[72:75], v[104:107], v[48:51]\n"
    "ds_read_b64_tr_b16 v[130:131], v241 offset:4608\n"
    "v_mfma_f32_16x16x32_bf16 v[52:55], v[76:79], v[104:107], v[52:55]\n"
    "ds_read_b128 v[136:139], v189 offset:7168\n"
    "v_mfma_f32_16x16x32_bf16 v[56:59], v[80:83], v[104:107], v[56:59]\n"
    "ds_read_b128 v[140:143], v189 offset:8192\n"
    "v_mfma_f32_16x16x32_bf16 v[60:63], v[84:87], v[104:107], v[60:63]\n"
    "v_mfma_f32_16x16x32_bf16 v[64:67], v[88:91], v[104:107], v[64:67]\n"
    "v_mfma_f32_16x16x32_bf16 v[68:71], v[92:95], v[104:107], v[68:71]\n"
    "s_waitcnt lgkmcnt(6)\n"
    "v_mfma_f32_16x16x32_bf16 v[0:3], v[108:111], v[132:135], v[0:3]\n"
    "v_mfma_f32_16x16x32_bf16 v[4:7], v[112:115], v[132:135], v[4:7]\n"
    "v_mfma_f32_16x16x32_bf16 v[8:11], v[116:119], v[132:135], v[8:11]\n"
    "s_waitcnt lgkmcnt(0)\n"
    "s_barrier\n"
    "v_mfma_f32_16x16x32_bf16 v[12:15], v[120:123], v[132:135], v[12:15]\n"
    "ds_read_b64_tr_b16 v[72:73], v236 offset:1024\n"
    "ds_read_b64_tr_b16 v[74:75], v236 offset:1536\n"
    "v_mfma_f32_16x16x32_bf16 v[16:19], v[124:127], v[132:135], v[16:19]\n"
    "ds_read_b128 v[96:99], v184 offset:3072\n"
    "v_mfma_f32_16x16x32_bf16 v[20:23], v[128:131], v[132:135], v[20:23]\n"
    "ds_read_b64_tr_b16 v[76:77], v236 offset:2048\n"
    "v_mfma_f32_16x16x32_bf16 v[24:27], v[108:111], v[136:139], v[24:27]\n"
    "ds_read_b64_tr_b16 v[78:79], v236 offset:2560\n"
    "v_mfma_f32_16x16x32_bf16 v[28:31], v[112:115], v[136:139], v[28:31]\n"
    "ds_read_b64_tr_b16 v[80:81], v236\n"
    "v_mfma_f32_16x16x32_bf16 v[32:35], v[116:119], v[136:139], v[32:35]\n"
    "ds_read_b64_tr_b16 v[82:83], v236 offset:512\n"
    "v_mfma_f32_16x16x32_bf16 v[36:39], v[120:123], v[136:139], v[36:39]\n"
    "ds_read_b64_tr_b16 v[84:85], v237 offset:1024\n"
    "v_mfma_f32_16x16x32_bf16 v[40:43], v[124:127], v[136:139], v[40:43]\n"
    "ds_read_b64_tr_b16 v[86:87], v237 offset:1536\n"
    "v_mfma_f32_16x16x32_bf16 v[44:47], v[128:131], v[136:139], v[44:47]\n"
    "ds_read_b64_tr_b16 v[88:89], v237 offset:2048\n"
    "v_mfma_f32_16x16x32_bf16 v[48:51], v[108:111], v[140:143], v[48:51]\n"
    "ds_read_b64_tr_b16 v[90:91], v237 offset:2560\n"
    "v_mfma_f32_16x16x32_bf16 v[52:55], v[112:115], v[140:143], v[52:55]\n"
    "ds_read_b64_tr_b16 v[92:93], v237\n"
    "v_mfma_f32_16x16x32_bf16 v[56:59], v[116:119], v[140:143], v[56:59]\n"
    "ds_read_b64_tr_b16 v[94:95], v237 offset:512\n"
    "v_mfma_f32_16x16x32_bf16 v[60:63], v[120:123], v[140:143], v[60:63]\n"
    "ds_read_b128 v[100:103], v184 offset:7168\n"
    "v_mfma_f32_16x16x32_bf16 v[64:67], v[124:127], v[140:143], v[64:67]\n"
    "ds_read_b128 v[104:107], v184 offset:8192\n"
    "v_mfma_f32_16x16x32_bf16 v[68:71], v[128:131], v[140:143], v[68:71]\n"
    "s_waitcnt lgkmcnt(9)\n"
    "v_mfma_f32_16x16x32_bf16 v[0:3], v[72:75], v[96:99], v[0:3]\n"
    "ds_read_b64_tr_b16 v[108:109], v236 offset:5120\n"
    "v_mfma_f32_16x16x32_bf16 v[4:7], v[76:79], v[96:99], v[4:7]\n"
    "ds_read_b64_tr_b16 v[110:111], v236 offset:5632\n"
    "s_waitcnt lgkmcnt(10)\n"
    "v_mfma_f32_16x16x32_bf16 v[8:11], v[80:83], v[96:99], v[8:11]\n"
    "ds_read_b128 v[132:135], v185 offset:3072\n"
    "s_waitcnt lgkmcnt(9)\n"
    "v_mfma_f32_16x16x32_bf16 v[12:15], v[84:87], v[96:99], v[12:15]\n"
    "ds_read_b64_tr_b16 v[112:113], v236 offset:6144\n"
    "s_waitcnt lgkmcnt(4)\n"
    "v_mfma_f32_16x16x32_bf16 v[16:19], v[88:91], v[96:99], v[16:19]\n"
    "ds_read_b64_tr_b16 v[114:115], v236 offset:6656\n"
    "v_mfma_f32_16x16x32_bf16 v[20:23], v[92:95], v[96:99], v[20:23]\n"
    "ds_read_b64_tr_b16 v[116:117], v236 offset:4096\n"
    "v_mfma_f32_16x16x32_bf16 v[24:27], v[72:75], v[100:103], v[24:27]\n"
    "ds_read_b64_tr_b16 v[118:119], v236 offset:4608\n"
    "v_mfma_f32_16x16x32_bf16 v[28:31], v[76:79], v[100:103], v[28:31]\n"
    "ds_read_b64_tr_b16 v[120:121], v237 offset:5120\n"
    "v_mfma_f32_16x16x32_bf16 v[32:35], v[80:83], v[100:103], v[32:35]\n"
    "ds_read_b64_tr_b16 v[122:123], v237 offset:5632\n"
    "v_mfma_f32_16x16x32_bf16 v[36:39], v[84:87], v[100:103], v[36:39]\n"
    "ds_read_b64_tr_b16 v[124:125], v237 offset:6144\n"
    "v_mfma_f32_16x16x32_bf16 v[40:43], v[88:91], v[100:103], v[40:43]\n"
    "ds_read_b64_tr_b16 v[126:127], v237 offset:6656\n"
    "v_mfma_f32_16x16x32_bf16 v[44:47], v[92:95], v[100:103], v[44:47]\n"
    "ds_read_b64_tr_b16 v[128:129], v237 offset:4096\n"
    "v_mfma_f32_16x16x32_bf16 v[48:51], v[72:75], v[104:107], v[48:51]\n"
    "ds_read_b64_tr_b16 v[130:131], v237 offset:4608\n"
    "v_mfma_f32_16x16x32_bf16 v[52:55], v[76:79], v[104:107], v[52:55]\n"
    "ds_read_b128 v[136:139], v185 offset:7168\n"
    "v_mfma_f32_16x16x32_bf16 v[56:59], v[80:83], v[104:107], v[56:59]\n"
    "ds_read_b128 v[140:143], v185 offset:8192\n"
    "v_mfma_f32_16x16x32_bf16 v[60:63], v[84:87], v[104:107], v[60:63]\n"
    "v_mfma_f32_16x16x32_bf16 v[64:67], v[88:91], v[104:107], v[64:67]\n"
    "v_mfma_f32_16x16x32_bf16 v[68:71], v[92:95], v[104:107], v[68:71]\n"
    "s_waitcnt lgkmcnt(6)\n"
    "v_mfma_f32_16x16x32_bf16 v[0:3], v[108:111], v[132:135], v[0:3]\n"
    "v_mfma_f32_16x16x32_bf16 v[4:7], v[112:115], v[132:135], v[4:7]\n"
    "v_mfma_f32_16x16x32_bf16 v[8:11], v[116:119], v[132:135], v[8:11]\n"
    "s_waitcnt lgkmcnt(0)\n"
    "s_barrier\n"
    "v_mfma_f32_16x16x32_bf16 v[12:15], v[120:123], v[132:135], v[12:15]\n"
    "ds_read_b64_tr_b16 v[72:73], v238 offset:1024\n"
    "ds_read_b64_tr_b16 v[74:75], v238 offset:1536\n"
    "v_mfma_f32_16x16x32_bf16 v[16:19], v[124:127], v[132:135], v[16:19]\n"
    "ds_read_b128 v[96:99], v186 offset:3072\n"
    "v_mfma_f32_16x16x32_bf16 v[20:23], v[128:131], v[132:135], v[20:23]\n"
    "ds_read_b64_tr_b16 v[76:77], v238 offset:2048\n"
    "v_mfma_f32_16x16x32_bf16 v[24:27], v[108:111], v[136:139], v[24:27]\n"
    "ds_read_b64_tr_b16 v[78:79], v238 offset:2560\n"
    "v_mfma_f32_16x16x32_bf16 v[28:31], v[112:115], v[136:139], v[28:31]\n"
    "ds_read_b64_tr_b16 v[80:81], v238\n"
    "v_mfma_f32_16x16x32_bf16 v[32:35], v[116:119], v[136:139], v[32:35]\n"
    "ds_read_b64_tr_b16 v[82:83], v238 offset:512\n"
    "v_mfma_f32_16x16x32_bf16 v[36:39], v[120:123], v[136:139], v[36:39]\n"
    "ds_read_b64_tr_b16 v[84:85], v239 offset:1024\n"
    "v_mfma_f32_16x16x32_bf16 v[40:43], v[124:127], v[136:139], v[40:43]\n"
    "ds_read_b64_tr_b16 v[86:87], v239 offset:1536\n"
    "v_mfma_f32_16x16x32_bf16 v[44:47], v[128:131], v[136:139], v[44:47]\n"
    "ds_read_b64_tr_b16 v[88:89], v239 offset:2048\n"
    "v_mfma_f32_16x16x32_bf16 v[48:51], v[108:111], v[140:143], v[48:51]\n"
    "ds_read_b64_tr_b16 v[90:91], v239 offset:2560\n"
    "v_mfma_f32_16x16x32_bf16 v[52:55], v[112:115], v[140:143], v[52:55]\n"
    "ds_read_b64_tr_b16 v[92:93], v239\n"
    "v_mfma_f32_16x16x32_bf16 v[56:59], v[116:119], v[140:143], v[56:59]\n"
    "ds_read_b64_tr_b16 v[94:95], v239 offset:512\n"
    "v_mfma_f32_16x16x32_bf16 v[60:63], v[120:123], v[140:143], v[60:63]\n"
    "ds_read_b128 v[100:103], v186 offset:7168\n"
    "v_mfma_f32_16x16x32_bf16 v[64:67], v[124:127], v[140:143], v[64:67]\n"
    "ds_read_b128 v[104:107], v186 offset:8192\n"
    "v_mfma_f32_16x16x32_bf16 v[68:71], v[128:131], v[140:143], v[68:71]\n"
    "s_waitcnt lgkmcnt(9)\n"
    "v_mfma_f32_16x16x32_bf16 v[0:3], v[72:75], v[96:99], v[0:3]\n"
    "ds_read_b64_tr_b16 v[108:109], v238 offset:5120\n"
    "v_mfma_f32_16x16x32_bf16 v[4:7], v[76:79], v[96:99], v[4:7]\n"
    "ds_read_b64_tr_b16 v[110:111], v238 offset:5632\n"
    "s_waitcnt lgkmcnt(10)\n"
    "v_mfma_f32_16x16x32_bf16 v[8:11], v[80:83], v[96:99], v[8:11]\n"
    "ds_read_b128 v[132:135], v187 offset:3072\n"
    "s_waitcnt lgkmcnt(9)\n"
    "v_mfma_f32_16x16x32_bf16 v[12:15], v[84:87], v[96:99], v[12:15]\n"
    "ds_read_b64_tr_b16 v[112:113], v238 offset:6144\n"
    "s_waitcnt lgkmcnt(4)\n"
    "v_mfma_f32_16x16x32_bf16 v[16:19], v[88:91], v[96:99], v[16:19]\n"
    "ds_read_b64_tr_b16 v[114:115], v238 offset:6656\n"
    "v_mfma_f32_16x16x32_bf16 v[20:23], v[92:95], v[96:99], v[20:23]\n"
    "ds_read_b64_tr_b16 v[116:117], v238 offset:4096\n"
    "v_mfma_f32_16x16x32_bf16 v[24:27], v[72:75], v[100:103], v[24:27]\n"
    "ds_read_b64_tr_b16 v[118:119], v238 offset:4608\n"
    "v_mfma_f32_16x16x32_bf16 v[28:31], v[76:79], v[100:103], v[28:31]\n"
    "ds_read_b64_tr_b16 v[120:121], v239 offset:5120\n"
    "v_mfma_f32_16x16x32_bf16 v[32:35], v[80:83], v[100:103], v[32:35]\n"
    "ds_read_b64_tr_b16 v[122:123], v239 offset:5632\n"
    "v_mfma_f32_16x16x32_bf16 v[36:39], v[84:87], v[100:103], v[36:39]\n"
    "ds_read_b64_tr_b16 v[124:125], v239 offset:6144\n"
    "v_mfma_f32_16x16x32_bf16 v[40:43], v[88:91], v[100:103], v[40:43]\n"
    "ds_read_b64_tr_b16 v[126:127], v239 offset:6656\n"
    "v_mfma_f32_16x16x32_bf16 v[44:47], v[92:95], v[100:103], v[44:47]\n"
    "ds_read_b64_tr_b16 v[128:129], v239 offset:4096\n"
    "v_mfma_f32_16x16x32_bf16 v[48:51], v[72:75], v[104:107], v[48:51]\n"
    "ds_read_b64_tr_b16 v[130:131], v239 offset:4608\n"
    "v_mfma_f32_16x16x32_bf16 v[52:55], v[76:79], v[104:107], v[52:55]\n"
    "ds_read_b128 v[136:139], v187 offset:7168\n"
    "v_mfma_f32_16x16x32_bf16 v[56:59], v[80:83], v[104:107], v[56:59]\n"
    "ds_read_b128 v[140:143], v187 offset:8192\n"
    "v_mfma_f32_16x16x32_bf16 v[60:63], v[84:87], v[104:107], v[60:63]\n"
    "v_mfma_f32_16x16x32_bf16 v[64:67], v[88:91], v[104:107], v[64:67]\n"
    "v_mfma_f32_16x16x32_bf16 v[68:71], v[92:95], v[104:107], v[68:71]\n"
    "s_waitcnt lgkmcnt(6)\n"
    "v_mfma_f32_16x16x32_bf16 v[0:3], v[108:111], v[132:135], v[0:3]\n"
    "v_mfma_f32_16x16x32_bf16 v[4:7], v[112:115], v[132:135], v[4:7]\n"
    "v_mfma_f32_16x16x32_bf16 v[8:11], v[116:119], v[132:135], v[8:11]\n"
    "s_cmp_lg_u32 s69, 3\n"
    "s_cbranch_scc1 NVM%=\n"
    "s_waitcnt vmcnt(0)\n"
    "NVM%=:\n"
    "s_waitcnt lgkmcnt(0)\n"
    "s_barrier\n"
    "v_mfma_f32_16x16x32_bf16 v[12:15], v[120:123], v[132:135], v[12:15]\n"
    "ds_read_b64_tr_b16 v[72:73], v240 offset:1024\n"
    "ds_read_b64_tr_b16 v[74:75], v240 offset:1536\n"
    "v_mfma_f32_16x16x32_bf16 v[16:19], v[124:127], v[132:135], v[16:19]\n"
    "ds_read_b128 v[96:99], v188 offset:3072\n"
    "v_mfma_f32_16x16x32_bf16 v[20:23], v[128:131], v[132:135], v[20:23]\n"
    "ds_read_b64_tr_b16 v[76:77], v240 offset:2048\n"
    "v_mfma_f32_16x16x32_bf16 v[24:27], v[108:111], v[136:139], v[24:27]\n"
    "ds_read_b64_tr_b16 v[78:79], v240 offset:2560\n"
    "v_mfma_f32_16x16x32_bf16 v[28:31], v[112:115], v[136:139], v[28:31]\n"
    "ds_read_b64_tr_b16 v[80:81], v240\n"
    "v_mfma_f32_16x16x32_bf16 v[32:35], v[116:119], v[136:139], v[32:35]\n"
    "ds_read_b64_tr_b16 v[82:83], v240 offset:512\n"
    "v_mfma_f32_16x16x32_bf16 v[36:39], v[120:123], v[136:139], v[36:39]\n"
    "ds_read_b64_tr_b16 v[84:85], v241 offset:1024\n"
    "v_mfma_f32_16x16x32_bf16 v[40:43], v[124:127], v[136:139], v[40:43]\n"
    "ds_read_b64_tr_b16 v[86:87], v241 offset:1536\n"
    "v_mfma_f32_16x16x32_bf16 v[44:47], v[128:131], v[136:139], v[44:47]\n"
    "ds_read_b64_tr_b16 v[88:89], v241 offset:2048\n"
    "v_mfma_f32_16x16x32_bf16 v[48:51], v[108:111], v[140:143], v[48:51]\n"
    "ds_read_b64_tr_b16 v[90:91], v241 offset:2560\n"
    "v_mfma_f32_16x16x32_bf16 v[52:55], v[112:115], v[140:143], v[52:55]\n"
    "ds_read_b64_tr_b16 v[92:93], v241\n"
    "v_mfma_f32_16x16x32_bf16 v[56:59], v[116:119], v[140:143], v[56:59]\n"
    "ds_read_b64_tr_b16 v[94:95], v241 offset:512\n"
    "v_mfma_f32_16x16x32_bf16 v[60:63], v[120:123], v[140:143], v[60:63]\n"
    "ds_read_b128 v[100:103], v188 offset:7168\n"
    "v_mfma_f32_16x16x32_bf16 v[64:67], v[124:127], v[140:143], v[64:67]\n"
    "ds_read_b128 v[104:107], v188 offset:8192\n"
    "v_mfma_f32_16x16x32_bf16 v[68:71], v[128:131], v[140:143], v[68:71]\n"
    "s_waitcnt lgkmcnt(9)\n"
    "v_mfma_f32_16x16x32_bf16 v[0:3], v[72:75], v[96:99], v[0:3]\n"
    "ds_read_b64_tr_b16 v[108:109], v240 offset:5120\n"
    "v_mfma_f32_16x16x32_bf16 v[4:7], v[76:79], v[96:99], v[4:7]\n"
    "ds_read_b64_tr_b16 v[110:111], v240 offset:5632\n"
    "s_waitcnt lgkmcnt(10)\n"
    "v_mfma_f32_16x16x32_bf16 v[8:11], v[80:83], v[96:99], v[8:11]\n"
    "ds_read_b128 v[132:135], v189 offset:3072\n"
    "s_waitcnt lgkmcnt(9)\n"
    "v_mfma_f32_16x16x32_bf16 v[12:15], v[84:87], v[96:99], v[12:15]\n"
    "ds_read_b64_tr_b16 v[112:113], v240 offset:6144\n"
    "s_waitcnt lgkmcnt(4)\n"
    "v_mfma_f32_16x16x32_bf16 v[16:19], v[88:91], v[96:99], v[16:19]\n"
    "ds_read_b64_tr_b16 v[114:115], v240 offset:6656\n"
    "v_mfma_f32_16x16x32_bf16 v[20:23], v[92:95], v[96:99], v[20:23]\n"
    "ds_read_b64_tr_b16 v[116:117], v240 offset:4096\n"
    "v_mfma_f32_16x16x32_bf16 v[24:27], v[72:75], v[100:103], v[24:27]\n"
    "ds_read_b64_tr_b16 v[118:119], v240 offset:4608\n"
    "v_mfma_f32_16x16x32_bf16 v[28:31], v[76:79], v[100:103], v[28:31]\n"
    "ds_read_b64_tr_b16 v[120:121], v241 offset:5120\n"
    "v_mfma_f32_16x16x32_bf16 v[32:35], v[80:83], v[100:103], v[32:35]\n"
    "ds_read_b64_tr_b16 v[122:123], v241 offset:5632\n"
    "v_mfma_f32_16x16x32_bf16 v[36:39], v[84:87], v[100:103], v[36:39]\n"
    "ds_read_b64_tr_b16 v[124:125], v241 offset:6144\n"
    "v_mfma_f32_16x16x32_bf16 v[40:43], v[88:91], v[100:103], v[40:43]\n"
    "ds_read_b64_tr_b16 v[126:127], v241 offset:6656\n"
    "v_mfma_f32_16x16x32_bf16 v[44:47], v[92:95], v[100:103], v[44:47]\n"
    "ds_read_b64_tr_b16 v[128:129], v241 offset:4096\n"
    "v_mfma_f32_16x16x32_bf16 v[48:51], v[72:75], v[104:107], v[48:51]\n"
    "ds_read_b64_tr_b16 v[130:131], v241 offset:4608\n"
    "v_mfma_f32_16x16x32_bf16 v[52:55], v[76:79], v[104:107], v[52:55]\n"
    "ds_read_b128 v[136:139], v189 offset:7168\n"
    "v_mfma_f32_16x16x32_bf16 v[56:59], v[80:83], v[104:107], v[56:59]\n"
    "ds_read_b128 v[140:143], v189 offset:8192\n"
    "v_mfma_f32_16x16x32_bf16 v[60:63], v[84:87], v[104:107], v[60:63]\n"
    "v_mfma_f32_16x16x32_bf16 v[64:67], v[88:91], v[104:107], v[64:67]\n"
    "v_mfma_f32_16x16x32_bf16 v[68:71], v[92:95], v[104:107], v[68:71]\n"
    "s_waitcnt lgkmcnt(0)\n"
    "s_barrier\n"
    "v_mfma_f32_16x16x32_bf16 v[48:51], v[108:111], v[140:143], v[48:51]\n"
    "ds_read_b128 v[104:107], v184 offset:8192\n"
    "ds_read_b64_tr_b16 v[72:73], v236 offset:1024\n"
    "v_mfma_f32_16x16x32_bf16 v[52:55], v[112:115], v[140:143], v[52:55]\n"
    "ds_read_b64_tr_b16 v[74:75], v236 offset:1536\n"
    "ds_read_b64_tr_b16 v[76:77], v236 offset:2048\n"
    "v_mfma_f32_16x16x32_bf16 v[0:3], v[108:111], v[132:135], v[0:3]\n"
    "ds_read_b64_tr_b16 v[78:79], v236 offset:2560\n"
    "s_movk_i32 s0, 4096\n"
    "v_mfma_f32_16x16x32_bf16 v[4:7], v[112:115], v[132:135], v[4:7]\n"
    "ds_read_b128 v[176:179], v185 offset:8192\n"
    "s_bitcmp1_b32 s77, 0\n"
    "v_mfma_f32_16x16x32_bf16 v[24:27], v[108:111], v[136:139], v[24:27]\n"
    "ds_read_b64_tr_b16 v[144:145], v236 offset:5120\n"
    "s_cselect_b64 vcc, -1, 0\n"
    "v_mfma_f32_16x16x32_bf16 v[28:31], v[112:115], v[136:139], v[28:31]\n"
    "ds_read_b64_tr_b16 v[146:147], v236 offset:5632\n"
    "s_cmp_ge_u32 s69, 2\n"
    "v_mfma_f32_16x16x32_bf16 v[56:59], v[116:119], v[140:143], v[56:59]\n"
    "ds_read_b64_tr_b16 v[148:149], v236 offset:6144\n"
    "v_mov_b32 v111, s68\n"
    "v_mfma_f32_16x16x32_bf16 v[8:11], v[116:119], v[132:135], v[8:11]\n"
    "ds_read_b64_tr_b16 v[150:151], v236 offset:6656\n"
    "v_mov_b32 v115, s68\n"
    "v_mfma_f32_16x16x32_bf16 v[32:35], v[116:119], v[136:139], v[32:35]\n"
    "ds_read_b128 v[96:99], v184 offset:3072\n"
    "v_mov_b32 v183, s68\n"
    "v_mfma_f32_16x16x32_bf16 v[60:63], v[120:123], v[140:143], v[60:63]\n"
    "ds_read_b128 v[168:171], v185 offset:3072\n"
    "v_mfma_f32_16x16x32_bf16 v[64:67], v[124:127], v[140:143], v[64:67]\n"
    "ds_read_b128 v[100:103], v184 offset:7168\n"
    "v_mov_b32 v119, s68\n"
    "v_mfma_f32_16x16x32_bf16 v[12:15], v[120:123], v[132:135], v[12:15]\n"
    "ds_read_b128 v[172:175], v185 offset:7168\n"
    "s_waitcnt lgkmcnt(11)\n"
    "v_mfma_f32_16x16x32_bf16 v[48:51], v[72:75], v[104:107], v[48:51]\n"
    "ds_read_b64_tr_b16 v[80:81], v236\n"
    "s_waitcnt lgkmcnt(10)\n"
    "v_mfma_f32_16x16x32_bf16 v[52:55], v[76:79], v[104:107], v[52:55]\n"
    "ds_read_b64_tr_b16 v[82:83], v236 offset:512\n"
    "s_waitcnt lgkmcnt(8)\n"
    "v_mfma_f32_16x16x32_bf16 v[48:51], v[144:147], v[176:179], v[48:51]\n"
    "ds_read_b64_tr_b16 v[152:153], v236 offset:4096\n"
    "s_waitcnt lgkmcnt(7)\n"
    "v_mfma_f32_16x16x32_bf16 v[52:55], v[148:151], v[176:179], v[52:55]\n"
    "ds_read_b64_tr_b16 v[154:155], v236 offset:4608\n"
    "s_waitcnt lgkmcnt(7)\n"
    "v_mfma_f32_16x16x32_bf16 v[0:3], v[72:75], v[96:99], v[0:3]\n"
    "ds_read_b64_tr_b16 v[84:85], v237 offset:1024\n"
    "v_mfma_f32_16x16x32_bf16 v[4:7], v[76:79], v[96:99], v[4:7]\n"
    "ds_read_b64_tr_b16 v[86:87], v237 offset:1536\n"
    "v_cvt_pk_bf16_f32 v108, v48, v49\n"
    "v_cvt_pk_bf16_f32 v109, v50, v51\n"
    "s_waitcnt lgkmcnt(8)\n"
    "v_mfma_f32_16x16x32_bf16 v[0:3], v[144:147], v[168:171], v[0:3]\n"
    "ds_read_b64_tr_b16 v[156:157], v237 offset:5120\n"
    "ds_write_b64 v216, v[108:109] offset:22848\n"
    "v_cvt_pk_bf16_f32 v110, v52, v53\n"
    "v_mfma_f32_16x16x32_bf16 v[4:7], v[148:151], v[168:171], v[4:7]\n"
    "ds_read_b64_tr_b16 v[158:159], v237 offset:5632\n"
    "buffer_store_dwordx4 v[108:111], v243, s[52:55], 0 offen sc1\n"
    "s_nop 1\n"
    "v_cvt_pk_bf16_f32 v111, v54, v55\n"
    "s_waitcnt lgkmcnt(10)\n"
    "v_mfma_f32_16x16x32_bf16 v[24:27], v[72:75], v[100:103], v[24:27]\n"
    "ds_read_b64_tr_b16 v[88:89], v237 offset:2048\n"
    "ds_write_b64 v216, v[110:111] offset:40320\n"
    "v_mfma_f32_16x16x32_bf16 v[28:31], v[76:79], v[100:103], v[28:31]\n"
    "ds_read_b64_tr_b16 v[90:91], v237 offset:2560\n"
    "v_cvt_pk_bf16_f32 v112, v0, v1\n"
    "v_cvt_pk_bf16_f32 v113, v2, v3\n"
    "s_waitcnt lgkmcnt(12)\n"
    "v_mfma_f32_16x16x32_bf16 v[24:27], v[144:147], v[172:175], v[24:27]\n"
    "ds_read_b64_tr_b16 v[160:161], v237 offset:6144\n"
    "ds_write_b64 v216, v[112:113] offset:17472\n"
    "v_cvt_pk_bf16_f32 v114, v4, v5\n"
    "v_mfma_f32_16x16x32_bf16 v[28:31], v[148:151], v[172:175], v[28:31]\n"
    "ds_read_b64_tr_b16 v[162:163], v237 offset:6656\n"
    "s_cbranch_vccz SK1%=\n"
    "buffer_store_dwordx4 v[112:115], v242, s[52:55], 0 offen sc1\n"
    "SK1%=:\n"
    "v_mov_b32 v75, s68\n"
    "s_waitcnt lgkmcnt(13)\n"
    "v_mfma_f32_16x16x32_bf16 v[56:59], v[80:83], v[104:107], v[56:59]\n"
    "ds_read_b64_tr_b16 v[92:93], v237\n"
    "v_mov_b32 v79, s68\n"
    "v_cvt_pk_bf16_f32 v182, v54, v55\n"
    "v_mfma_f32_16x16x32_bf16 v[8:11], v[80:83], v[96:99], v[8:11]\n"
    "ds_read_b64_tr_b16 v[94:95], v237 offset:512\n"
    "v_cvt_pk_bf16_f32 v74, v6, v7\n"
    "v_mov_b32 v147, s68\n"
    "v_mfma_f32_16x16x32_bf16 v[32:35], v[80:83], v[100:103], v[32:35]\n"
    "ds_read_b64_tr_b16 v[164:165], v237 offset:4096\n"
    "v_mov_b32 v151, s68\n"
    "v_cvt_pk_bf16_f32 v146, v54, v55\n"
    "s_waitcnt lgkmcnt(14)\n"
    "v_mfma_f32_16x16x32_bf16 v[56:59], v[152:155], v[176:179], v[56:59]\n"
    "ds_read_b64_tr_b16 v[166:167], v237 offset:4608\n"
    "v_cvt_pk_bf16_f32 v116, v24, v25\n"
    "v_mfma_f32_16x16x32_bf16 v[8:11], v[152:155], v[168:171], v[8:11]\n"
    "v_cvt_pk_bf16_f32 v117, v26, v27\n"
    "v_cvt_pk_bf16_f32 v118, v28, v29\n"
    "v_mfma_f32_16x16x32_bf16 v[32:35], v[152:155], v[172:175], v[32:35]\n"
    "ds_write_b64 v216, v[116:117] offset:20160\n"
    "s_cbranch_vccz SK2%=\n"
    "buffer_store_dwordx4 v[116:119], v242, s[52:55], 0 offen offset:2048 sc1\n"
    "SK2%=:\n"
    "s_waitcnt lgkmcnt(14)\n"
    "v_mfma_f32_16x16x32_bf16 v[60:63], v[84:87], v[104:107], v[60:63]\n"
    "v_cvt_pk_bf16_f32 v78, v30, v31\n"
    "v_cvt_pk_bf16_f32 v180, v56, v57\n"
    "s_waitcnt lgkmcnt(8)\n"
    "v_mfma_f32_16x16x32_bf16 v[64:67], v[88:91], v[104:107], v[64:67]\n"
    "s_waitcnt lgkmcnt(0)\n"
    "s_barrier\n"
    "v_cvt_pk_bf16_f32 v181, v58, v59\n"
    "v_mfma_f32_16x16x32_bf16 v[12:15], v[84:87], v[96:99], v[12:15]\n"
    "s_cbranch_vccz SK3%=\n"
    "buffer_store_dwordx4 v[180:183], v242, s[52:55], s0 offen offset:1024 sc1\n"
    "SK3%=:\n"
    "v_cvt_pk_bf16_f32 v72, v8, v9\n"
    "v_mfma_f32_16x16x32_bf16 v[60:63], v[156:159], v[176:179], v[60:63]\n"
    "ds_write_b64 v216, v[180:181] offset:5376\n"
    "v_cvt_pk_bf16_f32 v73, v10, v11\n"
    "v_mfma_f32_16x16x32_bf16 v[64:67], v[160:163], v[176:179], v[64:67]\n"
    "s_cbranch_vccz SK4%=\n"
    "buffer_store_dwordx4 v[72:75], v242, s[52:55], 0 offen offset:1024 sc1\n"
    "SK4%=:\n"
    "v_cvt_pk_bf16_f32 v76, v32, v33\n"
    "v_mfma_f32_16x16x32_bf16 v[12:15], v[156:159], v[168:171], v[12:15]\n"
    "ds_write_b64 v216, v[72:73]\n"
    "v_cvt_pk_bf16_f32 v77, v34, v35\n"
    "v_mfma_f32_16x16x32_bf16 v[16:19], v[124:127], v[132:135], v[16:19]\n"
    "s_cbranch_vccz SK5%=\n"
    "buffer_store_dwordx4 v[76:79], v242, s[52:55], 0 offen offset:3072 sc1\n"
    "SK5%=:\n"
    "v_cvt_pk_bf16_f32 v144, v60, v61\n"
    "v_mfma_f32_16x16x32_bf16 v[16:19], v[88:91], v[96:99], v[16:19]\n"
    "ds_write_b64 v216, v[76:77] offset:2688\n"
    "v_cvt_pk_bf16_f32 v145, v62, v63\n"
    "v_mfma_f32_16x16x32_bf16 v[16:19], v[160:163], v[168:171], v[16:19]\n"
    "buffer_store_dwordx4 v[144:147], v244, s[52:55], 0 offen offset:512 sc1\n"
    "v_cvt_pk_bf16_f32 v148, v64, v65\n"
    "v_mfma_f32_16x16x32_bf16 v[40:43], v[124:127], v[136:139], v[40:43]\n"
    "ds_write_b64 v217, v[144:145] offset:22848\n"
    "v_cvt_pk_bf16_f32 v149, v66, v67\n"
    "v_mfma_f32_16x16x32_bf16 v[40:43], v[88:91], v[100:103], v[40:43]\n"
    "s_cbranch_scc0 SK6%=\n"
    "buffer_store_dwordx4 v[148:151], v245, s[52:55], 0 offen offset:1024 sc1\n"
    "SK6%=:\n"
    "v_cvt_pk_bf16_f32 v115, v6, v7\n"
    "v_mfma_f32_16x16x32_bf16 v[40:43], v[160:163], v[172:175], v[40:43]\n"
    "ds_write_b64 v216, v[114:115] offset:34944\n"
    "v_cvt_pk_bf16_f32 v119, v30, v31\n"
    "v_mfma_f32_16x16x32_bf16 v[36:39], v[120:123], v[136:139], v[36:39]\n"
    "v_cvt_pk_bf16_f32 v12, v12, v13\n"
    "v_cvt_pk_bf16_f32 v13, v14, v15\n"
    "v_mfma_f32_16x16x32_bf16 v[36:39], v[84:87], v[100:103], v[36:39]\n"
    "ds_write_b64 v216, v[118:119] offset:37632\n"
    "v_cvt_pk_bf16_f32 v16, v16, v17\n"
    "v_mfma_f32_16x16x32_bf16 v[36:39], v[156:159], v[172:175], v[36:39]\n"
    "v_cvt_pk_bf16_f32 v17, v18, v19\n"
    "v_cvt_pk_bf16_f32 v40, v40, v41\n"
    "v_mfma_f32_16x16x32_bf16 v[20:23], v[128:131], v[132:135], v[20:23]\n"
    "ds_write_b64 v217, v[148:149] offset:40320\n"
    "v_cvt_pk_bf16_f32 v41, v42, v43\n"
    "v_mfma_f32_16x16x32_bf16 v[20:23], v[92:95], v[96:99], v[20:23]\n"
    "v_mfma_f32_16x16x32_bf16 v[20:23], v[164:167], v[168:171], v[20:23]\n"
    "ds_write_b64 v217, v[12:13] offset:17472\n"
    "v_cvt_pk_bf16_f32 v36, v36, v37\n"
    "v_mfma_f32_16x16x32_bf16 v[44:47], v[128:131], v[136:139], v[44:47]\n"
    "v_cvt_pk_bf16_f32 v37, v38, v39\n"
    "v_mfma_f32_16x16x32_bf16 v[44:47], v[92:95], v[100:103], v[44:47]\n"
    "ds_write_b64 v217, v[16:17] offset:34944\n"
    "v_mfma_f32_16x16x32_bf16 v[44:47], v[164:167], v[172:175], v[44:47]\n"
    "s_nop 0\n"
    "v_cvt_pk_bf16_f32 v20, v20, v21\n"
    "v_cvt_pk_bf16_f32 v21, v22, v23\n"
    "v_mfma_f32_16x16x32_bf16 v[68:71], v[128:131], v[140:143], v[68:71]\n"
    "ds_write_b64 v217, v[40:41] offset:37632\n"
    "v_mfma_f32_16x16x32_bf16 v[68:71], v[92:95], v[104:107], v[68:71]\n"
    "ds_write_b64 v217, v[36:37] offset:20160\n"
    "v_mfma_f32_16x16x32_bf16 v[68:71], v[164:167], v[176:179], v[68:71]\n"
    "v_cvt_pk_bf16_f32 v44, v44, v45\n"
    "v_cvt_pk_bf16_f32 v45, v46, v47\n"
    "ds_write_b64 v217, v[20:21]\n"
    "ds_write_b64 v217, v[44:45] offset:2688\n"
    "s_nop 3\n"
    "v_cvt_pk_bf16_f32 v68, v68, v69\n"
    "v_cvt_pk_bf16_f32 v69, v70, v71\n"
    "ds_write_b64 v217, v[68:69] offset:5376\n"
    "s_waitcnt lgkmcnt(0)\n"
    "s_barrier\n"
    "s_branch TAIL%=\n"
    "LOADER%=:\n"
    "s_mov_b32 s36, s94\n"
    "s_and_b32 s37, s95, 65535\n"
    "s_mov_b32 s38, 2457600\n"
    "s_mov_b32 s39, 131072\n"
    "s_add_u32 s40, s92, 4294964224\n"
    "s_addc_u32 s41, s93, -1\n"
    "s_and_b32 s41, s41, 65535\n"
    "s_mov_b32 s42, 2624512\n"
    "s_mov_b32 s43, 131072\n"
    "s_add_u32 s60, s96, 4956160\n"
    "s_addc_u32 s61, s97, 0\n"
    "s_and_b32 s61, s61, 65535\n"
    "s_mov_b32 s62, 146432\n"
    "s_mov_b32 s63, 131072\n"
    "s_and_b32 s45, s45, 65535\n"
    "s_mov_b32 s46, 819200\n"
    "s_mov_b32 s47, 131072\n"
    "s_mul_i32 s0, s70, 245760\n"
    "s_lshl_b32 s1, s70, 7\n"
    "s_load_dword s68, s[60:61], 146176\n"
    "s_add_u32 m0, s71, 116736\n"
    "s_nop 0\n"
    "buffer_load_dwordx4 v182, s[36:39], s0 offen lds\n"
    "buffer_load_dwordx4 v183, s[36:39], s0 offen offset:1024 lds\n"
    "buffer_load_dwordx4 v184, s[36:39], s0 offen offset:2048 lds\n"
    "buffer_load_dwordx4 v177, s[40:43], s1 offen offset:3072 lds\n"
    "s_add_u32 s70, s70, 1\n"
    "s_sub_u32 s2, s70, 10\n"
    "s_cselect_b32 s70, s70, s2\n"
    "s_mul_i32 s0, s70, 245760\n"
    "s_lshl_b32 s1, s70, 7\n"
    "s_add_u32 m0, s71, 51200\n"
    "s_nop 0\n"
    "buffer_load_dwordx4 v179, s[36:39], s0 offen lds\n"
    "buffer_load_dwordx4 v180, s[36:39], s0 offen offset:1024 lds\n"
    "buffer_load_dwordx4 v181, s[36:39], s0 offen offset:2048 lds\n"
    "buffer_load_dwordx4 v176, s[40:43], s1 offen offset:3072 lds\n"
    "s_add_u32 m0, s71, 59392\n"
    "s_nop 0\n"
    "buffer_load_dwordx4 v178, s[40:43], s1 offen lds\n"
    "s_add_u32 m0, s71, 55296\n"
    "s_nop 0\n"
    "buffer_load_dwordx4 v182, s[36:39], s0 offen lds\n"
    "buffer_load_dwordx4 v183, s[36:39], s0 offen offset:1024 lds\n"
    "buffer_load_dwordx4 v184, s[36:39], s0 offen offset:2048 lds\n"
    "buffer_load_dwordx4 v177, s[40:43], s1 offen offset:3072 lds\n"
    "s_add_u32 s70, s70, 1\n"
    "s_sub_u32 s2, s70, 10\n"
    "s_cselect_b32 s70, s70, s2\n"
    "s_mul_i32 s0, s70, 245760\n"
    "s_lshl_b32 s1, s70, 7\n"
    "s_mov_b32 m0, 158720\n"
    "s_nop 0\n"
    "buffer_load_dword v185, s[44:47], 0 offen lds\n"
    "v_mbcnt_lo_u32_b32 v4, -1, 0\n"
    "v_mbcnt_hi_u32_b32 v4, -1, v4\n"
    "v_lshlrev_b32 v4, 4, v4\n"
    "v_add_u32 v4, 88064, v4\n"
    "s_mov_b32 s48, s96\n"
    "s_and_b32 s49, s97, 65535\n"
    "s_mov_b32 s50, 2703360\n"
    "s_mov_b32 s51, 131072\n"
    "s_add_u32 s52, s96, 2703360\n"
    "s_addc_u32 s53, s97, 0\n"
    "s_and_b32 s53, s53, 65535\n"
    "s_mov_b32 s54, 2252800\n"
    "s_mov_b32 s55, 131072\n"
    "s_mov_b32 s56, s98\n"
    "s_and_b32 s57, s99, 65535\n"
    "s_mov_b32 s58, 2621440\n"
    "s_mov_b32 s59, 131072\n"
    "s_add_u32 s64, s96, 5102592\n"
    "s_addc_u32 s65, s97, 0\n"
    "s_and_b32 s65, s65, 65535\n"
    "s_mov_b32 s66, 276480\n"
    "s_mov_b32 s67, 131072\n"
    "s_lshl_b32 s85, s85, 7\n"
    "s_add_u32 s85, s85, 113408\n"
    "s_waitcnt vmcnt(10)\n"
    "s_barrier\n"
    "ds_read_b128 v[192:195], v4\n"
    "ds_read_b128 v[196:199], v4 offset:1024\n"
    "ds_read_b128 v[200:203], v4 offset:2048\n"
    "ds_read_b128 v[204:207], v4 offset:3072\n"
    "ds_read_b128 v[16:19], v4 offset:4096\n"
    "ds_read_b128 v[20:23], v4 offset:5120\n"
    "ds_read_b128 v[224:227], v4 offset:6144\n"
    "ds_read_b128 v[228:231], v4 offset:7168\n"
    "ds_read_b128 v[232:235], v4 offset:8192\n"
    "ds_read_b128 v[236:239], v4 offset:9216\n"
    "ds_read_b128 v[240:243], v4 offset:10240\n"
    "ds_read_b128 v[244:247], v4 offset:11264\n"
    "ds_read_b128 v[248:251], v4 offset:12288\n"
    "s_add_u32 m0, s71, 0\n"
    "s_nop 0\n"
    "buffer_load_dwordx4 v179, s[36:39], s0 offen lds\n"
    "buffer_load_dwordx4 v180, s[36:39], s0 offen offset:1024 lds\n"
    "buffer_load_dwordx4 v181, s[36:39], s0 offen offset:2048 lds\n"
    "buffer_load_dwordx4 v176, s[40:43], s1 offen offset:3072 lds\n"
    "s_waitcnt lgkmcnt(0)\n"
    "s_add_u32 s68, s68, 0x9E3779B9\n"
    "s_lshl_b32 s20, s69, 2\n"
    "s_and_b32 s23, s69, 1\n"
    "s_lshl_b32 s22, s23, 6\n"
    "v_mov_b32 v6, 0x7fff0000\n"
    "v_mov_b32 v7, 0xff800000\n"
    "s_bfe_u32 s21, s69, 65537\n"
    "s_mul_i32 s21, s21, 80\n"
    "s_add_u32 s21, s21, s23\n"
    "s_mul_i32 s21, s21, 5120\n"
    "v_mov_b32 v8, s21\n"
    "v_xad_u32 v236, v236, s22, v8\n"
    "v_xor_b32 v237, s22, v237\n"
    "s_mul_i32 s21, s79, 25\n"
    "s_add_u32 s24, s21, 307200\n"
    "s_cmp_eq_u32 s79, 0\n"
    "s_cselect_b32 s24, 0, s24\n"
    "v_add_u32 v238, s24, v237\n"
    "s_mul_i32 s21, s21, 3\n"
    "v_add_u32 v237, s21, v237\n"
    "v_mov_b32 v252, 0x3f803f80\n"
    "v_mov_b32 v253, 0x3f803f80\n"
    "v_mov_b32 v254, 0x3f803f80\n"
    "v_mov_b32 v255, 0x3f803f80\n"
    "v_mov_b32 v85, s68\n"
    "v_and_b32 v86, 240, v4\n"
    "v_lshrrev_b32 v86, 1, v86\n"
    "v_add_u32 v86, s84, v86\n"
    "v_mov_b32 v87, s85\n"
    "v_readlane_b32 s21, v21, s20\n"
    "v_readlane_b32 s72, v22, s20\n"
    "s_add_u32 s20, s20, 1\n"
    "v_add_u32 v208, s21, v16\n"
    "v_add_u32 v209, s21, v17\n"
    "v_mad_u32_u24 v210, s21, v20, v18\n"
    "v_add_u32 v211, s21, v19\n"
    "v_readlane_b32 s21, v21, s20\n"
    "v_readlane_b32 s73, v22, s20\n"
    "s_add_u32 s20, s20, 1\n"
    "v_add_u32 v212, s21, v16\n"
    "v_add_u32 v213, s21, v17\n"
    "v_mad_u32_u24 v214, s21, v20, v18\n"
    "v_add_u32 v215, s21, v19\n"
    "v_readlane_b32 s21, v21, s20\n"
    "v_readlane_b32 s74, v22, s20\n"
    "s_add_u32 s20, s20, 1\n"
    "v_add_u32 v216, s21, v16\n"
    "v_add_u32 v217, s21, v17\n"
    "v_mad_u32_u24 v218, s21, v20, v18\n"
    "v_add_u32 v219, s21, v19\n"
    "s_bitcmp1_b32 s77, 3\n"
    "s_cselect_b32 s21, 2688, s21\n"
    "s_cmp_lt_u32 s21, 2688\n"
    "s_cselect_b64 vcc, -1, 0\n"
    "v_cndmask_b32 v168, v192, v7, vcc\n"
    "v_cndmask_b32 v169, v193, v7, vcc\n"
    "v_cndmask_b32 v170, v194, v7, vcc\n"
    "v_cndmask_b32 v171, v195, v7, vcc\n"
    "s_cmp_lt_i32 s74, 0\n"
    "s_cselect_b64 vcc, -1, 0\n"
    "s_max_i32 s74, s74, 0\n"
    "v_cndmask_b32 v186, v206, v6, vcc\n"
    "v_cndmask_b32 v187, v207, v6, vcc\n"
    "v_readlane_b32 s21, v21, s20\n"
    "v_readlane_b32 s88, v22, s20\n"
    "s_add_u32 s20, s20, 1\n"
    "v_add_u32 v220, s21, v16\n"
    "v_add_u32 v221, s21, v17\n"
    "v_mad_u32_u24 v222, s21, v20, v18\n"
    "v_add_u32 v223, s21, v19\n"
    "s_bitcmp1_b32 s77, 3\n"
    "s_cselect_b32 s21, 2688, s21\n"
    "s_cmp_lt_u32 s21, 2688\n"
    "s_cselect_b64 vcc, -1, 0\n"
    "v_cndmask_b32 v172, v192, v7, vcc\n"
    "v_cndmask_b32 v173, v193, v7, vcc\n"
    "v_cndmask_b32 v174, v194, v7, vcc\n"
    "v_cndmask_b32 v175, v195, v7, vcc\n"
    "s_cmp_lt_i32 s88, 0\n"
    "s_cselect_b64 vcc, -1, 0\n"
    "s_max_i32 s88, s88, 0\n"
    "v_cndmask_b32 v188, v206, v6, vcc\n"
    "v_cndmask_b32 v189, v207, v6, vcc\n"
    "s_mul_i32 s21, s23, 40960\n"
    "v_mov_b32 v8, s21\n"
    "v_xad_u32 v224, v224, s22, v8\n"
    "v_xad_u32 v225, v225, s22, v8\n"
    "v_xad_u32 v226, v226, s22, v8\n"
    "v_xad_u32 v227, v227, s22, v8\n"
    "s_lshl_b32 s21, s83, 11\n"
    "v_mov_b32 v8, s21\n"
    "v_xad_u32 v228, v228, s22, v8\n"
    "v_xad_u32 v229, v229, s22, v8\n"
    "v_xad_u32 v230, v230, s22, v8\n"
    "v_xad_u32 v231, v231, s22, v8\n"
    "v_xad_u32 v232, v232, s22, v8\n"
    "s_mul_i32 s21, s83, 40960\n"
    "s_add_u32 s21, s21, s22\n"
    "v_add_u32 v233, s21, v233\n"
    "s_lshl_b32 s21, s83, 7\n"
    "v_add_u32 v234, s21, v234\n"
    "s_lshl_b32 s21, s79, 1\n"
    "v_add_u32 v235, s21, v235\n"
    "s_add_u32 m0, s71, 8192\n"
    "s_nop 0\n"
    "buffer_load_dwordx4 v178, s[40:43], s1 offen lds\n"
    "s_add_u32 m0, s71, 4096\n"
    "s_nop 0\n"
    "buffer_load_dwordx4 v182, s[36:39], s0 offen lds\n"
    "buffer_load_dwordx4 v183, s[36:39], s0 offen offset:1024 lds\n"
    "buffer_load_dwordx4 v184, s[36:39], s0 offen offset:2048 lds\n"
    "buffer_load_dwordx4 v177, s[40:43], s1 offen offset:3072 lds\n"
    "s_add_u32 s70, s70, 1\n"
    "s_sub_u32 s2, s70, 10\n"
    "s_cselect_b32 s70, s70, s2\n"
    "s_mul_i32 s0, s70, 245760\n"
    "s_lshl_b32 s1, s70, 7\n"
    "s_waitcnt vmcnt(10)\n"
    "s_barrier\n"
    "s_add_u32 m0, s71, 112640\n"
    "s_nop 0\n"
    "buffer_load_dwordx4 v179, s[36:39], s0 offen lds\n"
    "buffer_load_dwordx4 v180, s[36:39], s0 offen offset:1024 lds\n"
    "buffer_load_dwordx4 v181, s[36:39], s0 offen offset:2048 lds\n"
    "buffer_load_dwordx4 v176, s[40:43], s1 offen offset:3072 lds\n"
    "s_add_u32 m0, s71, 120832\n"
    "s_nop 0\n"
    "buffer_load_dwordx4 v178, s[40:43], s1 offen lds\n"
    "s_add_u32 m0, s71, 116736\n"
    "s_nop 0\n"
    "buffer_load_dwordx4 v182, s[36:39], s0 offen lds\n"
    "buffer_load_dwordx4 v183, s[36:39], s0 offen offset:1024 lds\n"
    "buffer_load_dwordx4 v184, s[36:39], s0 offen offset:2048 lds\n"
    "buffer_load_dwordx4 v177, s[40:43], s1 offen offset:3072 lds\n"
    "s_add_u32 s70, s70, 1\n"
    "s_sub_u32 s2, s70, 10\n"
    "s_cselect_b32 s70, s70, s2\n"
    "s_mul_i32 s0, s70, 245760\n"
    "s_lshl_b32 s1, s70, 7\n"
    "s_waitcnt vmcnt(9)\n"
    "s_barrier\n"
    "s_add_u32 m0, s71, 51200\n"
    "s_nop 0\n"
    "buffer_load_dwordx4 v179, s[36:39], s0 offen lds\n"
    "buffer_load_dwordx4 v180, s[36:39], s0 offen offset:1024 lds\n"
    "buffer_load_dwordx4 v181, s[36:39], s0 offen offset:2048 lds\n"
    "buffer_load_dwordx4 v176, s[40:43], s1 offen offset:3072 lds\n"
    "s_add_u32 m0, s71, 59392\n"
    "s_nop 0\n"
    "buffer_load_dwordx4 v178, s[40:43], s1 offen lds\n"
    "s_add_u32 m0, s71, 55296\n"
    "s_nop 0\n"
    "buffer_load_dwordx4 v182, s[36:39], s0 offen lds\n"
    "buffer_load_dwordx4 v183, s[36:39], s0 offen offset:1024 lds\n"
    "buffer_load_dwordx4 v184, s[36:39], s0 offen offset:2048 lds\n"
    "buffer_load_dwordx4 v177, s[40:43], s1 offen offset:3072 lds\n"
    "s_add_u32 s70, s70, 1\n"
    "s_sub_u32 s2, s70, 10\n"
    "s_cselect_b32 s70, s70, s2\n"
    "s_mul_i32 s0, s70, 245760\n"
    "s_lshl_b32 s1, s70, 7\n"
    "s_waitcnt vmcnt(9)\n"
    "s_barrier\n"
    "s_add_u32 m0, s71, 0\n"
    "s_nop 0\n"
    "buffer_load_dwordx4 v179, s[36:39], s0 offen lds\n"
    "buffer_load_dwordx4 v180, s[36:39], s0 offen offset:1024 lds\n"
    "buffer_load_dwordx4 v181, s[36:39], s0 offen offset:2048 lds\n"
    "buffer_load_dwordx4 v176, s[40:43], s1 offen offset:3072 lds\n"
    "s_add_u32 m0, s71, 8192\n"
    "s_nop 0\n"
    "buffer_load_dwordx4 v178, s[40:43], s1 offen lds\n"
    "s_add_u32 m0, s71, 4096\n"
    "s_nop 0\n"
    "buffer_load_dwordx4 v182, s[36:39], s0 offen lds\n"
    "buffer_load_dwordx4 v183, s[36:39], s0 offen offset:1024 lds\n"
    "buffer_load_dwordx4 v184, s[36:39], s0 offen offset:2048 lds\n"
    "buffer_load_dwordx4 v177, s[40:43], s1 offen offset:3072 lds\n"
    "s_add_u32 s70, s70, 1\n"
    "s_sub_u32 s2, s70, 10\n"
    "s_cselect_b32 s70, s70, s2\n"
    "s_mul_i32 s0, s70, 245760\n"
    "s_lshl_b32 s1, s70, 7\n"
    "s_waitcnt vmcnt(9)\n"
    "s_barrier\n"
    "s_add_u32 m0, s71, 112640\n"
    "s_nop 0\n"
    "buffer_load_dwordx4 v179, s[36:39], s0 offen lds\n"
    "buffer_load_dwordx4 v180, s[36:39], s0 offen offset:1024 lds\n"
    "buffer_load_dwordx4 v181, s[36:39], s0 offen offset:2048 lds\n"
    "buffer_load_dwordx4 v176, s[40:43], s1 offen offset:3072 lds\n"
    "s_add_u32 m0, s71, 120832\n"
    "s_nop 0\n"
    "buffer_load_dwordx4 v178, s[40:43], s1 offen lds\n"
    "s_add_u32 m0, s71, 116736\n"
    "s_nop 0\n"
    "buffer_load_dwordx4 v182, s[36:39], s0 offen lds\n"
    "buffer_load_dwordx4 v183, s[36:39], s0 offen offset:1024 lds\n"
    "buffer_load_dwordx4 v184, s[36:39], s0 offen offset:2048 lds\n"
    "buffer_load_dwordx4 v177, s[40:43], s1 offen offset:3072 lds\n"
    "s_add_u32 s70, s70, 1\n"
    "s_sub_u32 s2, s70, 10\n"
    "s_cselect_b32 s70, s70, s2\n"
    "s_mul_i32 s0, s70, 245760\n"
    "s_lshl_b32 s1, s70, 7\n"
    "s_waitcnt vmcnt(9)\n"
    "s_barrier\n"
    "s_add_u32 m0, s71, 51200\n"
    "s_nop 0\n"
    "buffer_load_dwordx4 v179, s[36:39], s0 offen lds\n"
    "buffer_load_dwordx4 v180, s[36:39], s0 offen offset:1024 lds\n"
    "buffer_load_dwordx4 v181, s[36:39], s0 offen offset:2048 lds\n"
    "buffer_load_dwordx4 v176, s[40:43], s1 offen offset:3072 lds\n"
    "s_add_u32 m0, s71, 59392\n"
    "s_nop 0\n"
    "buffer_load_dwordx4 v178, s[40:43], s1 offen lds\n"
    "s_add_u32 m0, s71, 55296\n"
    "s_nop 0\n"
    "buffer_load_dwordx4 v182, s[36:39], s0 offen lds\n"
    "buffer_load_dwordx4 v183, s[36:39], s0 offen offset:1024 lds\n"
    "buffer_load_dwordx4 v184, s[36:39], s0 offen offset:2048 lds\n"
    "buffer_load_dwordx4 v177, s[40:43], s1 offen offset:3072 lds\n"
    "s_add_u32 s70, s70, 1\n"
    "s_sub_u32 s2, s70, 10\n"
    "s_cselect_b32 s70, s70, s2\n"
    "s_mul_i32 s0, s70, 245760\n"
    "s_lshl_b32 s1, s70, 7\n"
    "s_waitcnt vmcnt(9)\n"
    "s_barrier\n"
    "s_add_u32 m0, s71, 0\n"
    "s_nop 0\n"
    "buffer_load_dwordx4 v179, s[36:39], s0 offen lds\n"
    "buffer_load_dwordx4 v180, s[36:39], s0 offen offset:1024 lds\n"
    "buffer_load_dwordx4 v181, s[36:39], s0 offen offset:2048 lds\n"
    "buffer_load_dwordx4 v176, s[40:43], s1 offen offset:3072 lds\n"
    "s_add_u32 m0, s71, 8192\n"
    "s_nop 0\n"
    "buffer_load_dwordx4 v178, s[40:43], s1 offen lds\n"
    "s_add_u32 m0, s71, 4096\n"
    "s_nop 0\n"
    "buffer_load_dwordx4 v182, s[36:39], s0 offen lds\n"
    "buffer_load_dwordx4 v183, s[36:39], s0 offen offset:1024 lds\n"
    "buffer_load_dwordx4 v184, s[36:39], s0 offen offset:2048 lds\n"
    "buffer_load_dwordx4 v177, s[40:43], s1 offen offset:3072 lds\n"
    "s_add_u32 s70, s70, 1\n"
    "s_sub_u32 s2, s70, 10\n"
    "s_cselect_b32 s70, s70, s2\n"
    "s_mul_i32 s0, s70, 245760\n"
    "s_lshl_b32 s1, s70, 7\n"
    "s_waitcnt vmcnt(9)\n"
    "s_barrier\n"
    "s_add_u32 m0, s71, 112640\n"
    "s_nop 0\n"
    "buffer_load_dwordx4 v179, s[36:39], s0 offen lds\n"
    "buffer_load_dwordx4 v180, s[36:39], s0 offen offset:1024 lds\n"
    "buffer_load_dwordx4 v181, s[36:39], s0 offen offset:2048 lds\n"
    "buffer_load_dwordx4 v176, s[40:43], s1 offen offset:3072 lds\n"
    "s_add_u32 m0, s71, 120832\n"
    "s_nop 0\n"
    "buffer_load_dwordx4 v178, s[40:43], s1 offen lds\n"
    "s_add_u32 m0, s71, 116736\n"
    "s_nop 0\n"
    "buffer_load_dwordx4 v182, s[36:39], s0 offen lds\n"
    "buffer_load_dwordx4 v183, s[36:39], s0 offen offset:1024 lds\n"
    "buffer_load_dwordx4 v184, s[36:39], s0 offen offset:2048 lds\n"
    "buffer_load_dwordx4 v177, s[40:43], s1 offen offset:3072 lds\n"
    "s_waitcnt vmcnt(9)\n"
    "s_barrier\n"
    "s_memrealtime s[28:29]\n"
    "s_cmp_lg_u32 s69, 6\n"
    "s_cbranch_scc1 NH1%=\n"
    "s_waitcnt lgkmcnt(0)\n"
    "v_mov_b32 v84, s28\n"
    "buffer_store_dwordx2 v[84:85], v86, s[60:63], 0 offen sc1\n"
    "s_add_u32 m0, s79, 51200\n"
    "s_mov_b32 s2, 25600\n"
    "buffer_load_dwordx4 v236, s[44:47], s2 offen lds\n"
    "s_add_u32 m0, s79, 55296\n"
    "s_mov_b32 s2, 102400\n"
    "buffer_load_dwordx4 v236, s[44:47], s2 offen lds\n"
    "s_add_u32 m0, s79, 59392\n"
    "s_mov_b32 s2, 179200\n"
    "buffer_load_dwordx4 v236, s[44:47], s2 offen lds\n"
    "s_add_u32 m0, s79, 63488\n"
    "s_mov_b32 s2, 256000\n"
    "buffer_load_dwordx4 v236, s[44:47], s2 offen lds\n"
    "s_add_u32 m0, s79, 67584\n"
    "s_mov_b32 s2, 25600\n"
    "buffer_load_dwordx4 v237, s[44:47], s2 offen lds\n"
    "s_add_u32 m0, s79, 71680\n"
    "s_mov_b32 s2, 51200\n"
    "buffer_load_dwordx4 v236, s[44:47], s2 offen lds\n"
    "s_add_u32 m0, s79, 75776\n"
    "s_mov_b32 s2, 128000\n"
    "buffer_load_dwordx4 v236, s[44:47], s2 offen lds\n"
    "s_add_u32 m0, s79, 79872\n"
    "s_mov_b32 s2, 204800\n"
    "buffer_load_dwordx4 v236, s[44:47], s2 offen lds\n"
    "s_add_u32 m0, s79, 83968\n"
    "s_mov_b32 s2, 281600\n"
    "buffer_load_dwordx4 v236, s[44:47], s2 offen lds\n"
    "s_add_u32 m0, s79, 88064\n"
    "s_mov_b32 s2, 51200\n"
    "buffer_load_dwordx4 v237, s[44:47], s2 offen lds\n"
    "s_waitcnt vmcnt(11)\n"
    "s_branch NH2%=\n"
    "NH1%=:\n"
    "s_add_u32 m0, s79, 51200\n"
    "s_mov_b32 s2, 25600\n"
    "buffer_load_dwordx4 v236, s[44:47], s2 offen lds\n"
    "s_add_u32 m0, s79, 55296\n"
    "s_mov_b32 s2, 102400\n"
    "buffer_load_dwordx4 v236, s[44:47], s2 offen lds\n"
    "s_add_u32 m0, s79, 59392\n"
    "s_mov_b32 s2, 179200\n"
    "buffer_load_dwordx4 v236, s[44:47], s2 offen lds\n"
    "s_add_u32 m0, s79, 63488\n"
    "s_mov_b32 s2, 256000\n"
    "buffer_load_dwordx4 v236, s[44:47], s2 offen lds\n"
    "s_add_u32 m0, s79, 67584\n"
    "s_mov_b32 s2, 25600\n"
    "buffer_load_dwordx4 v237, s[44:47], s2 offen lds\n"
    "s_add_u32 m0, s79, 71680\n"
    "s_mov_b32 s2, 51200\n"
    "buffer_load_dwordx4 v236, s[44:47], s2 offen lds\n"
    "s_add_u32 m0, s79, 75776\n"
    "s_mov_b32 s2, 128000\n"
    "buffer_load_dwordx4 v236, s[44:47], s2 offen lds\n"
    "s_add_u32 m0, s79, 79872\n"
    "s_mov_b32 s2, 204800\n"
    "buffer_load_dwordx4 v236, s[44:47], s2 offen lds\n"
    "s_add_u32 m0, s79, 83968\n"
    "s_mov_b32 s2, 281600\n"
    "buffer_load_dwordx4 v236, s[44:47], s2 offen lds\n"
    "s_add_u32 m0, s79, 88064\n"
    "s_mov_b32 s2, 51200\n"
    "buffer_load_dwordx4 v237, s[44:47], s2 offen lds\n"
    "s_waitcnt vmcnt(10)\n"
    "NH2%=:\n"
    "s_barrier\n"
    "s_add_u32 m0, s79, 92160\n"
    "s_mov_b32 s2, 0\n"
    "buffer_load_dwordx4 v236, s[44:47], s2 offen lds\n"
    "s_add_u32 m0, s79, 96256\n"
    "s_mov_b32 s2, 332800\n"
    "buffer_load_dwordx4 v236, s[44:47], s2 offen lds\n"
    "s_add_u32 m0, s79, 100352\n"
    "s_mov_b32 s2, 358400\n"
    "buffer_load_dwordx4 v236, s[44:47], s2 offen lds\n"
    "s_add_u32 m0, s79, 104448\n"
    "s_mov_b32 s2, 384000\n"
    "buffer_load_dwordx4 v236, s[44:47], s2 offen lds\n"
    "s_add_u32 m0, s79, 108544\n"
    "s_mov_b32 s2, 0\n"
    "buffer_load_dwordx4 v238, s[44:47], s2 offen lds\n"
    "s_cmp_eq_u32 s69, 7\n"
    "s_cbranch_scc1 RX%=\n"
    "s_cmp_lg_u32 s69, 5\n"
    "s_cbranch_scc1 NS2%=\n"
    "s_mov_b32 m0, 155648\n"
    "s_nop 0\n"
    "buffer_load_dwordx4 v239, s[60:63], 0 offen lds\n"
    "NS2%=:\n"
    "s_barrier\n"
    "s_waitcnt vmcnt(0)\n"
    "s_cmp_lg_u32 s69, 5\n"
    "s_cbranch_scc1 NV2%=\n"
    "s_mov_b32 s3, 262144\n"
    "v_mbcnt_lo_u32_b32 v4, -1, 0\n"
    "v_mbcnt_hi_u32_b32 v4, -1, v4\n"
    "v_lshlrev_b32 v4, 4, v4\n"
    "v_add_u32 v4, 155648, v4\n"
    "VF2%=:\n"
    "ds_read_b128 v[0:3], v4\n"
    "s_waitcnt lgkmcnt(0)\n"
    "v_xor_b32 v1, s68, v1\n"
    "v_xor_b32 v3, s68, v3\n"
    "v_or_b32 v1, v1, v3\n"
    "v_cmp_ne_u32 vcc, 0, v1\n"
    "s_cbranch_vccz VF2D%=\n"
    "s_sleep 8\n"
    "s_mov_b32 m0, 155648\n"
    "s_sub_u32 s3, s3, 1\n"
    "buffer_load_dwordx4 v239, s[60:63], 0 offen sc1 lds\n"
    "s_waitcnt vmcnt(0)\n"
    "s_cbranch_scc0 VF2%=\n"
    "VF2D%=:\n"
    "NV2%=:\n"
    "s_barrier\n"
    "s_add_u32 m0, s79, 112640\n"
    "s_mov_b32 s2, 76800\n"
    "buffer_load_dwordx4 v236, s[44:47], s2 offen lds\n"
    "s_add_u32 m0, s79, 116736\n"
    "s_mov_b32 s2, 153600\n"
    "buffer_load_dwordx4 v236, s[44:47], s2 offen lds\n"
    "s_add_u32 m0, s79, 120832\n"
    "s_mov_b32 s2, 230400\n"
    "buffer_load_dwordx4 v236, s[44:47], s2 offen lds\n"
    "s_add_u32 m0, s79, 124928\n"
    "s_mov_b32 s2, 307200\n"
    "buffer_load_dwordx4 v236, s[44:47], s2 offen lds\n"
    "s_add_u32 m0, s79, 129024\n"
    "s_mov_b32 s2, 76800\n"
    "buffer_load_dwordx4 v237, s[44:47], s2 offen lds\n"
    "s_add_u32 m0, s80, 130048\n"
    "s_nop 0\n"
    "buffer_load_dwordx4 v176, s[40:43], s78 offen offset:3072 lds\n"
    "s_add_u32 m0, s80, 132096\n"
    "s_nop 0\n"
    "buffer_load_dwordx4 v177, s[40:43], s78 offen offset:3072 lds\n"
    "s_add_u32 m0, s80, 137216\n"
    "s_nop 0\n"
    "buffer_load_dwordx4 v178, s[40:43], s78 offen lds\n"
    "s_cmp_lg_u32 s69, 6\n"
    "s_cbranch_scc1 TAIL%=\n"
    "buffer_store_dword v85, v87, s[60:63], 0 offen sc1\n"
    "TAIL%=:\n"
    "s_mov_b32 s4, 0x3ea52b5b\n"
    "s_cmp_lt_i32 s72, 0\n"
    "s_cbranch_scc1 PRE%=\n"
    "ds_read_b64 v[100:101], v208\n"
    "ds_read_b64 v[102:103], v210\n"
    "ds_read_b64 v[108:109], v208 offset:17472\n"
    "ds_read_b64 v[110:111], v209 offset:17472\n"
    "ds_read_b64 v[104:105], v208 offset:14784\n"
    "ds_read_b64 v[106:107], v209 offset:14784\n"
    "ds_read_b64 v[132:133], v212\n"
    "ds_read_b64 v[134:135], v214\n"
    "ds_read_b64 v[140:141], v212 offset:17472\n"
    "ds_read_b64 v[142:143], v213 offset:17472\n"
    "ds_read_b64 v[136:137], v212 offset:14784\n"
    "ds_read_b64 v[138:139], v213 offset:14784\n"
    "ds_read_b64_tr_b16 v[112:113], v211\n"
    "ds_read_b64_tr_b16 v[114:115], v211 offset:2688\n"
    "ds_read_b64_tr_b16 v[116:117], v211 offset:16\n"
    "ds_read_b64_tr_b16 v[118:119], v211 offset:2704\n"
    "ds_read_b64_tr_b16 v[144:145], v215\n"
    "ds_read_b64_tr_b16 v[146:147], v215 offset:2688\n"
    "ds_read_b64_tr_b16 v[148:149], v215 offset:16\n"
    "ds_read_b64_tr_b16 v[150:151], v215 offset:2704\n"
    "s_waitcnt lgkmcnt(15)\n"
    "v_mfma_f32_16x16x32_bf16 v[120:123], v[108:111], v[100:103], v[196:199]\n"
    "s_waitcnt lgkmcnt(14)\n"
    "v_mfma_f32_16x16x32_bf16 v[124:127], v[104:107], v[100:103], v[192:195]\n"
    "s_waitcnt lgkmcnt(10)\n"
    "v_mfma_f32_16x16x32_bf16 v[152:155], v[140:143], v[132:135], v[196:199]\n"
    "s_waitcnt lgkmcnt(8)\n"
    "v_mfma_f32_16x16x32_bf16 v[156:159], v[136:139], v[132:135], v[192:195]\n"
    "s_nop 3\n"
    "v_maximum3_f32 v120, v120, v124, v124\n"
    "v_maximum3_f32 v121, v121, v125, v125\n"
    "v_maximum3_f32 v122, v122, v126, v126\n"
    "v_maximum3_f32 v128, v120, v121, v122\n"
    "v_maximum3_f32 v123, v123, v127, v127\n"
    "v_maximum3_f32 v129, v128, v123, v123\n"
    "v_maximum3_f32 v128, v128, v123, v123\n"
    "v_maximum3_f32 v152, v152, v156, v156\n"
    "v_maximum3_f32 v153, v153, v157, v157\n"
    "v_permlane32_swap_b32 v128, v129\n"
    "v_maximum3_f32 v124, v128, v129, v129\n"
    "v_maximum3_f32 v129, v128, v129, v129\n"
    "v_maximum3_f32 v154, v154, v158, v158\n"
    "v_maximum3_f32 v155, v155, v159, v159\n"
    "v_permlane16_swap_b32 v124, v129\n"
    "v_maximum3_f32 v128, v124, v129, v129\n"
    "v_mul_f32 v128, 0xbea52b5b, v128\n"
    "v_pk_fma_f32 v[120:121], v[120:121], s[4:5], v[128:129] op_sel_hi:[1,0,0]\n"
    "v_pk_fma_f32 v[122:123], v[122:123], s[4:5], v[128:129] op_sel_hi:[1,0,0]\n"
    "v_exp_f32 v120, v120\n"
    "v_exp_f32 v121, v121\n"
    "v_exp_f32 v122, v122\n"
    "v_exp_f32 v123, v123\n"
    "v_cvt_pk_bf16_f32 v120, v120, v121\n"
    "v_cvt_pk_bf16_f32 v122, v122, v123\n"
    "v_and_b32 v100, v200, v120\n"
    "v_and_b32 v102, v202, v120\n"
    "v_and_b32 v101, v201, v122\n"
    "v_and_b32 v103, v203, v122\n"
    "v_maximum3_f32 v160, v152, v153, v154\n"
    "v_maximum3_f32 v161, v160, v155, v155\n"
    "v_mfma_f32_16x16x32_bf16 v[104:107], v[252:255], v[100:103], 0\n"
    "v_maximum3_f32 v160, v160, v155, v155\n"
    "s_waitcnt lgkmcnt(6)\n"
    "v_mfma_f32_16x16x32_bf16 v[108:111], v[112:115], v[100:103], 0\n"
    "v_permlane32_swap_b32 v160, v161\n"
    "s_waitcnt lgkmcnt(4)\n"
    "v_mfma_f32_16x16x32_bf16 v[124:127], v[116:119], v[100:103], 0\n"
    "v_maximum3_f32 v156, v160, v161, v161\n"
    "v_maximum3_f32 v161, v160, v161, v161\n"
    "s_nop 1\n"
    "v_permlane16_swap_b32 v156, v161\n"
    "v_maximum3_f32 v160, v156, v161, v161\n"
    "v_rcp_f32 v128, v104\n"
    "v_mul_f32 v160, 0xbea52b5b, v160\n"
    "v_pk_mul_f32 v[108:109], v[108:109], v[128:129] op_sel_hi:[1,0]\n"
    "v_pk_mul_f32 v[110:111], v[110:111], v[128:129] op_sel_hi:[1,0]\n"
    "v_pk_mul_f32 v[124:125], v[124:125], v[128:129] op_sel_hi:[1,0]\n"
    "v_pk_mul_f32 v[126:127], v[126:127], v[128:129] op_sel_hi:[1,0]\n"
    "v_pk_fma_f32 v[152:153], v[152:153], s[4:5], v[160:161] op_sel_hi:[1,0,0]\n"
    "v_pk_fma_f32 v[154:155], v[154:155], s[4:5], v[160:161] op_sel_hi:[1,0,0]\n"
    "v_cvt_pk_bf16_f32 v120, v108, v109\n"
    "v_cvt_pk_bf16_f32 v121, v110, v111\n"
    "v_cvt_pk_bf16_f32 v122, v124, v125\n"
    "v_cvt_pk_bf16_f32 v123, v126, v127\n"
    "v_exp_f32 v152, v152\n"
    "v_exp_f32 v153, v153\n"
    "v_exp_f32 v154, v154\n"
    "v_exp_f32 v155, v155\n"
    "buffer_store_dwordx2 v[120:121], v207, s[48:51], s72 offen sc1\n"
    "buffer_store_dwordx4 v[120:123], v206, s[48:51], s72 offen sc1\n"
    "v_cvt_pk_bf16_f32 v152, v152, v153\n"
    "v_cvt_pk_bf16_f32 v154, v154, v155\n"
    "v_and_b32 v132, v200, v152\n"
    "v_and_b32 v134, v202, v152\n"
    "v_and_b32 v133, v201, v154\n"
    "v_and_b32 v135, v203, v154\n"
    "s_nop 1\n"
    "v_mfma_f32_16x16x32_bf16 v[136:139], v[252:255], v[132:135], 0\n"
    "s_waitcnt lgkmcnt(2)\n"
    "v_mfma_f32_16x16x32_bf16 v[140:143], v[144:147], v[132:135], 0\n"
    "s_waitcnt lgkmcnt(0)\n"
    "v_mfma_f32_16x16x32_bf16 v[156:159], v[148:151], v[132:135], 0\n"
    "s_nop 3\n"
    "v_rcp_f32 v160, v136\n"
    "s_nop 0\n"
    "v_pk_mul_f32 v[140:141], v[140:141], v[160:161] op_sel_hi:[1,0]\n"
    "v_cvt_pk_bf16_f32 v152, v140, v141\n"
    "v_pk_mul_f32 v[142:143], v[142:143], v[160:161] op_sel_hi:[1,0]\n"
    "v_cvt_pk_bf16_f32 v153, v142, v143\n"
    "buffer_store_dwordx2 v[152:153], v207, s[48:51], s73 offen sc1\n"
    "v_pk_mul_f32 v[156:157], v[156:157], v[160:161] op_sel_hi:[1,0]\n"
    "v_cvt_pk_bf16_f32 v154, v156, v157\n"
    "v_pk_mul_f32 v[158:159], v[158:159], v[160:161] op_sel_hi:[1,0]\n"
    "v_cvt_pk_bf16_f32 v155, v158, v159\n"
    "buffer_store_dwordx4 v[152:155], v206, s[48:51], s73 offen sc1\n"
    "s_branch PRE%=\n"
    "RX%=:\n"
    "buffer_load_dwordx2 v[102:103], v240, s[60:63], 0 offen sc1\n"
    "s_barrier\n"
    "s_add_u32 m0, s79, 112640\n"
    "s_mov_b32 s2, 76800\n"
    "buffer_load_dwordx4 v236, s[44:47], s2 offen lds\n"
    "s_add_u32 m0, s79, 116736\n"
    "s_mov_b32 s2, 153600\n"
    "buffer_load_dwordx4 v236, s[44:47], s2 offen lds\n"
    "s_add_u32 m0, s79, 120832\n"
    "s_mov_b32 s2, 230400\n"
    "buffer_load_dwordx4 v236, s[44:47], s2 offen lds\n"
    "s_add_u32 m0, s79, 124928\n"
    "s_mov_b32 s2, 307200\n"
    "buffer_load_dwordx4 v236, s[44:47], s2 offen lds\n"
    "s_add_u32 m0, s79, 129024\n"
    "s_mov_b32 s2, 76800\n"
    "buffer_load_dwordx4 v237, s[44:47], s2 offen lds\n"
    "s_add_u32 m0, s80, 130048\n"
    "s_nop 0\n"
    "buffer_load_dwordx4 v176, s[40:43], s78 offen offset:3072 lds\n"
    "s_add_u32 m0, s80, 132096\n"
    "s_nop 0\n"
    "buffer_load_dwordx4 v177, s[40:43], s78 offen offset:3072 lds\n"
    "s_add_u32 m0, s80, 137216\n"
    "s_nop 0\n"
    "buffer_load_dwordx4 v178, s[40:43], s78 offen lds\n"
    "s_add_u32 s8, s75, 0\n"
    "s_add_u32 s9, s75, 7680\n"
    "s_add_u32 s10, s75, 61440\n"
    "s_add_u32 s14, s76, 0\n"
    "s_add_u32 s15, s76, 7680\n"
    "s_add_u32 s16, s76, 61440\n"
    "s_add_u32 s17, s76, 15360\n"
    "s_add_u32 s18, s76, 23040\n"
    "s_add_u32 s19, s76, 76800\n"
    "s_movk_i32 s6, 4096\n"
    "s_mov_b32 s3, 262144\n"
    "s_mov_b32 s4, 0x3ea52b5b\n"
    "s_bitcmp1_b32 s77, 3\n"
    "s_cselect_b32 s7, s68, 0\n"
    "v_and_b32 v251, s68, v251\n"
    "s_waitcnt vmcnt(8)\n"
    "s_waitcnt lgkmcnt(0)\n"
    "v_subrev_u32 v102, s28, v102\n"
    "s_nop 1\n"
    "v_max_i32_dpp v102, v102, v102 quad_perm:[1,0,3,2] row_mask:0xf bank_mask:0xf\n"
    "s_nop 1\n"
    "v_max_i32_dpp v102, v102, v102 quad_perm:[2,3,0,1] row_mask:0xf bank_mask:0xf\n"
    "s_nop 1\n"
    "v_max_i32_dpp v102, v102, v102 row_half_mirror row_mask:0xf bank_mask:0xf\n"
    "s_nop 1\n"
    "v_max_i32_dpp v102, v102, v102 row_mirror row_mask:0xf bank_mask:0xf\n"
    "s_nop 0\n"
    "v_readlane_b32 s1, v102, 0\n"
    "v_readlane_b32 s2, v102, 16\n"
    "v_readlane_b32 s35, v102, 32\n"
    "s_max_i32 s1, s1, s2\n"
    "s_min_i32 s1, s1, 200\n"
    "s_mul_i32 s1, s1, 6\n"
    "s_ashr_i32 s34, s1, 2\n"
    "s_min_i32 s35, s35, 200\n"
    "s_mul_i32 s35, s35, 5\n"
    "s_ashr_i32 s35, s35, 2\n"
    "v_cmp_ne_u32 vcc, s68, v103\n"
    "s_barrier\n"
    "s_memrealtime s[26:27]\n"
    "s_setprio 3\n"
    "s_cbranch_vccz HPD%=\n"
    "HPL%=:\n"
    "buffer_load_dwordx2 v[102:103], v240, s[60:63], 0 offen sc1\n"
    "s_waitcnt vmcnt(0)\n"
    "v_cmp_ne_u32 vcc, s68, v103\n"
    "s_cbranch_vccz HPE%=\n"
    "s_sleep 2\n"
    "s_sub_u32 s3, s3, 1\n"
    "s_cbranch_scc0 HPL%=\n"
    "HPE%=:\n"
    "s_waitcnt lgkmcnt(0)\n"
    "v_subrev_u32 v102, s28, v102\n"
    "s_nop 1\n"
    "v_max_i32_dpp v102, v102, v102 quad_perm:[1,0,3,2] row_mask:0xf bank_mask:0xf\n"
    "s_nop 1\n"
    "v_max_i32_dpp v102, v102, v102 quad_perm:[2,3,0,1] row_mask:0xf bank_mask:0xf\n"
    "s_nop 1\n"
    "v_max_i32_dpp v102, v102, v102 row_half_mirror row_mask:0xf bank_mask:0xf\n"
    "s_nop 1\n"
    "v_max_i32_dpp v102, v102, v102 row_mirror row_mask:0xf bank_mask:0xf\n"
    "s_nop 0\n"
    "v_readlane_b32 s1, v102, 0\n"
    "v_readlane_b32 s2, v102, 16\n"
    "v_readlane_b32 s35, v102, 32\n"
    "s_max_i32 s1, s1, s2\n"
    "s_min_i32 s1, s1, 200\n"
    "s_mul_i32 s1, s1, 6\n"
    "s_ashr_i32 s34, s1, 2\n"
    "s_min_i32 s35, s35, 200\n"
    "s_mul_i32 s35, s35, 5\n"
    "s_ashr_i32 s35, s35, 2\n"
    "HPD%=:\n"
    "s_waitcnt lgkmcnt(0)\n"
    "s_add_u32 s34, s34, s26\n"
    "s_add_u32 s35, s35, s26\n"
    "s_add_u32 s2, s35, 8\n"
    "URX%=:\n"
    "s_memrealtime s[0:1]\n"
    "s_waitcnt lgkmcnt(0)\n"
    "s_sub_u32 s0, s0, s2\n"
    "s_cmp_lt_i32 s0, 0\n"
    "s_cbranch_scc1 URX%=\n"
    "buffer_load_dword v144, v242, s[60:63], 0 offen\n"
    "buffer_load_dwordx4 v[100:103], v244, s[52:55], 0 offen sc1\n"
    "buffer_load_dwordx4 v[104:107], v245, s[52:55], 0 offen offset:512 sc1\n"
    "buffer_load_dwordx4 v[108:111], v245, s[52:55], 0 offen offset:1024 sc1\n"
    "buffer_load_dwordx4 v[112:115], v246, s[52:55], 0 offen sc1\n"
    "buffer_load_dwordx4 v[116:119], v246, s[52:55], 0 offen offset:512 sc1\n"
    "buffer_load_dwordx4 v[120:123], v243, s[52:55], s6 offen sc1\n"
    "buffer_load_dwordx4 v[124:127], v243, s[52:55], s6 offen offset:1024 sc1\n"
    "buffer_load_dwordx4 v[128:131], v243, s[52:55], 0 offen sc1\n"
    "buffer_load_dwordx4 v[132:135], v243, s[52:55], 0 offen offset:1024 sc1\n"
    "buffer_load_dwordx4 v[136:139], v243, s[52:55], 0 offen offset:2048 sc1\n"
    "buffer_load_dwordx4 v[140:143], v243, s[52:55], 0 offen offset:3072 sc1\n"
    "v_mov_b32 v152, 156544\n"
    "ds_read_b128 v[156:159], v152\n"
    "s_waitcnt lgkmcnt(0)\n"
    "v_readfirstlane_b32 s20, v156\n"
    "v_readfirstlane_b32 s21, v158\n"
    "s_andn2_b32 s24, 2047, s20\n"
    "s_lshr_b32 s22, s24, 1\n"
    "s_sub_u32 s22, s22, s82\n"
    "s_and_b32 s24, s24, 1\n"
    "s_cmp_eq_u32 s24, s81\n"
    "s_cselect_b32 s22, s22, -1\n"
    "s_andn2_b32 s24, 2047, s21\n"
    "s_lshr_b32 s23, s24, 1\n"
    "s_sub_u32 s23, s23, s82\n"
    "s_and_b32 s24, s24, 1\n"
    "s_cmp_eq_u32 s24, s81\n"
    "s_cselect_b32 s23, s23, -1\n"
    "s_min_u32 s25, s22, s23\n"
    "s_or_b32 s34, s34, 1\n"
    "v_mov_b32 v154, 156708\n"
    "v_mov_b32 v155, s34\n"
    "RXP%=:\n"
    "s_waitcnt vmcnt(8)\n"
    "ds_write_b64 v248, v[100:101]\n"
    "v_xor_b32 v151, s7, v103\n"
    "v_xor_b32 v152, s7, v107\n"
    "v_mov_b32 v103, v106\n"
    "v_or_b32 v150, v151, v152\n"
    "ds_write_b64 v248, v[102:103] offset:17472\n"
    "ds_write_b64 v249, v[104:105]\n"
    "ds_write_b64 v249, v[108:109] offset:17472\n"
    "v_xor_b32 v151, s7, v111\n"
    "v_or_b32 v150, v150, v151\n"
    "v_xor_b32 v153, s68, v144\n"
    "s_waitcnt vmcnt(6)\n"
    "ds_write_b64 v250, v[112:113]\n"
    "v_xor_b32 v151, v251, v115\n"
    "v_xor_b32 v152, v251, v119\n"
    "v_mov_b32 v115, v118\n"
    "v_or3_b32 v150, v150, v151, v152\n"
    "ds_write_b64 v250, v[114:115] offset:17472\n"
    "s_waitcnt vmcnt(4)\n"
    "ds_write_b64 v247, v[120:121] offset:22848\n"
    "v_xor_b32 v151, s68, v123\n"
    "v_xor_b32 v152, s68, v127\n"
    "v_mov_b32 v123, v126\n"
    "v_or3_b32 v150, v150, v151, v152\n"
    "ds_write_b64 v247, v[122:123] offset:40320\n"
    "ds_write_b64 v247, v[124:125] offset:5376\n"
    "s_waitcnt vmcnt(2)\n"
    "ds_write_b64 v247, v[128:129] offset:17472\n"
    "v_xor_b32 v151, s68, v131\n"
    "v_xor_b32 v152, s68, v135\n"
    "v_mov_b32 v131, v134\n"
    "v_or3_b32 v150, v150, v151, v152\n"
    "ds_write_b64 v247, v[130:131] offset:34944\n"
    "ds_write_b64 v247, v[132:133]\n"
    "s_waitcnt vmcnt(0)\n"
    "ds_write_b64 v247, v[136:137] offset:20160\n"
    "v_xor_b32 v151, s68, v139\n"
    "v_xor_b32 v152, s68, v143\n"
    "v_mov_b32 v139, v142\n"
    "v_or3_b32 v150, v150, v151, v152\n"
    "ds_write_b64 v247, v[138:139] offset:37632\n"
    "ds_write_b64 v247, v[140:141] offset:2688\n"
    "v_or_b32 v153, v153, v150\n"
    "v_cmp_ne_u32 vcc, 0, v153\n"
    "s_cbranch_vccz RXD%=\n"
    "s_waitcnt lgkmcnt(0)\n"
    "s_sub_u32 s3, s3, 1\n"
    "s_cbranch_scc1 RXD%=\n"
    "v_cmp_ne_u32 vcc, s68, v144\n"
    "s_cbranch_vccz RXA%=\n"
    "M2L%=:\n"
    "s_sleep 6\n"
    "buffer_load_dword v144, v242, s[60:63], 0 offen sc1\n"
    "s_waitcnt vmcnt(0)\n"
    "v_cmp_ne_u32 vcc, s68, v144\n"
    "s_cbranch_vccz M2D%=\n"
    "s_sub_u32 s3, s3, 1\n"
    "s_cbranch_scc0 M2L%=\n"
    "M2D%=:\n"
    "v_cmp_ne_u32 vcc, 0, v150\n"
    "s_cbranch_vccz RXD%=\n"
    "RXA%=:\n"
    "buffer_load_dword v144, v242, s[60:63], 0 offen sc1\n"
    "buffer_load_dwordx4 v[100:103], v244, s[52:55], 0 offen sc1\n"
    "buffer_load_dwordx4 v[104:107], v245, s[52:55], 0 offen offset:512 sc1\n"
    "buffer_load_dwordx4 v[108:111], v245, s[52:55], 0 offen offset:1024 sc1\n"
    "buffer_load_dwordx4 v[112:115], v246, s[52:55], 0 offen sc1\n"
    "buffer_load_dwordx4 v[116:119], v246, s[52:55], 0 offen offset:512 sc1\n"
    "buffer_load_dwordx4 v[120:123], v243, s[52:55], s6 offen sc1\n"
    "buffer_load_dwordx4 v[124:127], v243, s[52:55], s6 offen offset:1024 sc1\n"
    "buffer_load_dwordx4 v[128:131], v243, s[52:55], 0 offen sc1\n"
    "buffer_load_dwordx4 v[132:135], v243, s[52:55], 0 offen offset:1024 sc1\n"
    "buffer_load_dwordx4 v[136:139], v243, s[52:55], 0 offen offset:2048 sc1\n"
    "buffer_load_dwordx4 v[140:143], v243, s[52:55], 0 offen offset:3072 sc1\n"
    "s_waitcnt vmcnt(0)\n"
    "s_branch RXP%=\n"
    "RXD%=:\n"
    "ds_write_b32 v154, v155\n"
    "s_branch LATE%=\n"
    "PRE%=:\n"
    "s_cmp_lt_u32 s69, 4\n"
    "s_cbranch_scc1 FULL%=\n"
    "s_setprio 3\n"
    "ds_read_b64_tr_b16 v[0:1], v224\n"
    "ds_read_b64_tr_b16 v[2:3], v224 offset:512\n"
    "ds_read_b64_tr_b16 v[4:5], v225\n"
    "ds_read_b64_tr_b16 v[6:7], v225 offset:512\n"
    "ds_read_b64_tr_b16 v[8:9], v226\n"
    "ds_read_b64_tr_b16 v[10:11], v226 offset:512\n"
    "ds_read_b64_tr_b16 v[12:13], v227\n"
    "ds_read_b64_tr_b16 v[14:15], v227 offset:512\n"
    "ds_read_b64_tr_b16 v[16:17], v224 offset:4096\n"
    "ds_read_b64_tr_b16 v[18:19], v224 offset:4608\n"
    "ds_read_b64_tr_b16 v[20:21], v225 offset:4096\n"
    "ds_read_b64_tr_b16 v[22:23], v225 offset:4608\n"
    "ds_read_b64_tr_b16 v[24:25], v226 offset:4096\n"
    "ds_read_b64_tr_b16 v[26:27], v226 offset:4608\n"
    "ds_read_b64_tr_b16 v[28:29], v227 offset:4096\n"
    "ds_read_b64_tr_b16 v[30:31], v227 offset:4608\n"
    "ds_read_b64_tr_b16 v[32:33], v224 offset:8192\n"
    "ds_read_b64_tr_b16 v[34:35], v224 offset:8704\n"
    "ds_read_b64_tr_b16 v[36:37], v225 offset:8192\n"
    "ds_read_b64_tr_b16 v[38:39], v225 offset:8704\n"
    "ds_read_b64_tr_b16 v[40:41], v226 offset:8192\n"
    "ds_read_b64_tr_b16 v[42:43], v226 offset:8704\n"
    "ds_read_b64_tr_b16 v[44:45], v227 offset:8192\n"
    "ds_read_b64_tr_b16 v[46:47], v227 offset:8704\n"
    "ds_read_b64_tr_b16 v[48:49], v224 offset:12288\n"
    "ds_read_b64_tr_b16 v[50:51], v224 offset:12800\n"
    "ds_read_b64_tr_b16 v[52:53], v225 offset:12288\n"
    "ds_read_b64_tr_b16 v[54:55], v225 offset:12800\n"
    "ds_read_b64_tr_b16 v[56:57], v226 offset:12288\n"
    "ds_read_b64_tr_b16 v[58:59], v226 offset:12800\n"
    "ds_read_b64_tr_b16 v[60:61], v227 offset:12288\n"
    "ds_read_b64_tr_b16 v[62:63], v227 offset:12800\n"
    "ds_read_b64_tr_b16 v[64:65], v224 offset:16384\n"
    "ds_read_b64_tr_b16 v[66:67], v224 offset:16896\n"
    "ds_read_b64_tr_b16 v[68:69], v225 offset:16384\n"
    "ds_read_b64_tr_b16 v[70:71], v225 offset:16896\n"
    "ds_read_b64_tr_b16 v[72:73], v226 offset:16384\n"
    "ds_read_b64_tr_b16 v[74:75], v226 offset:16896\n"
    "ds_read_b64_tr_b16 v[76:77], v227 offset:16384\n"
    "ds_read_b64_tr_b16 v[78:79], v227 offset:16896\n"
    "v_mov_b32 v140, 156544\n"
    "ds_read_b128 v[144:147], v140\n"
    "s_add_u32 s8, s75, 0\n"
    "s_add_u32 s9, s75, 7680\n"
    "s_add_u32 s10, s75, 61440\n"
    "s_add_u32 s14, s76, 0\n"
    "s_add_u32 s15, s76, 7680\n"
    "s_add_u32 s16, s76, 61440\n"
    "s_add_u32 s17, s76, 15360\n"
    "s_add_u32 s18, s76, 23040\n"
    "s_add_u32 s19, s76, 76800\n"
    "s_waitcnt lgkmcnt(0)\n"
    "v_readfirstlane_b32 s20, v144\n"
    "v_readfirstlane_b32 s21, v146\n"
    "s_andn2_b32 s24, 2047, s20\n"
    "s_lshr_b32 s22, s24, 1\n"
    "s_sub_u32 s22, s22, s82\n"
    "s_and_b32 s24, s24, 1\n"
    "s_cmp_eq_u32 s24, s81\n"
    "s_cselect_b32 s22, s22, -1\n"
    "s_andn2_b32 s24, 2047, s21\n"
    "s_lshr_b32 s23, s24, 1\n"
    "s_sub_u32 s23, s23, s82\n"
    "s_and_b32 s24, s24, 1\n"
    "s_cmp_eq_u32 s24, s81\n"
    "s_cselect_b32 s23, s23, -1\n"
    "s_min_u32 s25, s22, s23\n"
    "s_mov_b32 s3, 16777216\n"
    "v_mov_b32 v141, 156708\n"
    "RXW%=:\n"
    "ds_read_b32 v140, v141\n"
    "s_waitcnt lgkmcnt(0)\n"
    "v_cmp_ne_u32 vcc, 0, v140\n"
    "s_cbranch_vccnz RXWD%=\n"
    "s_sleep 1\n"
    "s_sub_u32 s3, s3, 1\n"
    "s_cbranch_scc0 RXW%=\n"
    "RXWD%=:\n"
    "v_readfirstlane_b32 s34, v140\n"
    "LATE%=:\n"
    "s_memrealtime s[0:1]\n"
    "s_add_u32 s2, s34, 65\n"
    "s_waitcnt lgkmcnt(0)\n"
    "s_sub_u32 s35, s0, s2\n"
    "ds_read_b64 v[100:101], v216\n"
    "ds_read_b64 v[102:103], v218\n"
    "ds_read_b64 v[108:109], v216 offset:17472\n"
    "ds_read_b64 v[110:111], v217 offset:17472\n"
    "ds_read_b64 v[104:105], v216 offset:14784\n"
    "ds_read_b64 v[106:107], v217 offset:14784\n"
    "ds_read_b64 v[132:133], v220\n"
    "ds_read_b64 v[134:135], v222\n"
    "ds_read_b64 v[140:141], v220 offset:17472\n"
    "ds_read_b64 v[142:143], v221 offset:17472\n"
    "ds_read_b64 v[136:137], v220 offset:14784\n"
    "ds_read_b64 v[138:139], v221 offset:14784\n"
    "ds_read_b64_tr_b16 v[112:113], v219\n"
    "ds_read_b64_tr_b16 v[114:115], v219 offset:2688\n"
    "ds_read_b64_tr_b16 v[144:145], v223\n"
    "ds_read_b64_tr_b16 v[146:147], v223 offset:2688\n"
    "ds_read_b64_tr_b16 v[116:117], v219 offset:16\n"
    "ds_read_b64_tr_b16 v[118:119], v219 offset:2704\n"
    "ds_read_b64_tr_b16 v[148:149], v223 offset:16\n"
    "ds_read_b64_tr_b16 v[150:151], v223 offset:2704\n"
    "s_cmp_lt_i32 s35, 0\n"
    "s_cbranch_scc1 NE%=\n"
    "buffer_load_dwordx4 v[80:83], v204, s[48:51], s8 offen sc1\n"
    "buffer_load_dwordx4 v[84:87], v204, s[48:51], s8 offen offset:3840 sc1\n"
    "buffer_load_dwordx4 v[88:91], v204, s[48:51], s9 offen sc1\n"
    "buffer_load_dwordx4 v[92:95], v204, s[48:51], s9 offen offset:3840 sc1\n"
    "buffer_load_dwordx2 v[96:97], v205, s[48:51], s8 offen sc1\n"
    "buffer_load_dwordx2 v[98:99], v205, s[48:51], s10 offen sc1\n"
    "NE%=:\n"
    "s_waitcnt lgkmcnt(15)\n"
    "v_mfma_f32_16x16x32_bf16 v[120:123], v[108:111], v[100:103], v[196:199]\n"
    "s_waitcnt lgkmcnt(14)\n"
    "v_mfma_f32_16x16x32_bf16 v[124:127], v[104:107], v[100:103], v[168:171]\n"
    "s_waitcnt lgkmcnt(10)\n"
    "v_mfma_f32_16x16x32_bf16 v[152:155], v[140:143], v[132:135], v[196:199]\n"
    "s_waitcnt lgkmcnt(8)\n"
    "v_mfma_f32_16x16x32_bf16 v[156:159], v[136:139], v[132:135], v[172:175]\n"
    "s_nop 3\n"
    "v_maximum3_f32 v120, v120, v124, v124\n"
    "v_maximum3_f32 v121, v121, v125, v125\n"
    "v_maximum3_f32 v122, v122, v126, v126\n"
    "v_maximum3_f32 v128, v120, v121, v122\n"
    "v_maximum3_f32 v123, v123, v127, v127\n"
    "v_maximum3_f32 v129, v128, v123, v123\n"
    "v_maximum3_f32 v128, v128, v123, v123\n"
    "v_maximum3_f32 v152, v152, v156, v156\n"
    "v_maximum3_f32 v153, v153, v157, v157\n"
    "v_maximum3_f32 v154, v154, v158, v158\n"
    "v_maximum3_f32 v155, v155, v159, v159\n"
    "v_maximum3_f32 v160, v152, v153, v154\n"
    "v_maximum3_f32 v161, v160, v155, v155\n"
    "v_maximum3_f32 v160, v160, v155, v155\n"
    "v_permlane32_swap_b32 v128, v129\n"
    "v_maximum3_f32 v124, v128, v129, v129\n"
    "v_permlane32_swap_b32 v160, v161\n"
    "v_maximum3_f32 v156, v160, v161, v161\n"
    "v_maximum3_f32 v129, v128, v129, v129\n"
    "v_maximum3_f32 v161, v160, v161, v161\n"
    "s_nop 0\n"
    "v_permlane16_swap_b32 v124, v129\n"
    "v_maximum3_f32 v128, v124, v129, v129\n"
    "v_permlane16_swap_b32 v156, v161\n"
    "v_maximum3_f32 v160, v156, v161, v161\n"
    "v_mul_f32 v128, 0xbea52b5b, v128\n"
    "v_mul_f32 v160, 0xbea52b5b, v160\n"
    "v_pk_fma_f32 v[120:121], v[120:121], s[4:5], v[128:129] op_sel_hi:[1,0,0]\n"
    "v_pk_fma_f32 v[122:123], v[122:123], s[4:5], v[128:129] op_sel_hi:[1,0,0]\n"
    "v_pk_fma_f32 v[152:153], v[152:153], s[4:5], v[160:161] op_sel_hi:[1,0,0]\n"
    "v_pk_fma_f32 v[154:155], v[154:155], s[4:5], v[160:161] op_sel_hi:[1,0,0]\n"
    "v_exp_f32 v120, v120\n"
    "v_exp_f32 v121, v121\n"
    "v_exp_f32 v122, v122\n"
    "v_exp_f32 v123, v123\n"
    "v_exp_f32 v152, v152\n"
    "v_exp_f32 v153, v153\n"
    "v_exp_f32 v154, v154\n"
    "v_exp_f32 v155, v155\n"
    "v_cvt_pk_bf16_f32 v120, v120, v121\n"
    "v_cvt_pk_bf16_f32 v122, v122, v123\n"
    "v_cvt_pk_bf16_f32 v152, v152, v153\n"
    "v_cvt_pk_bf16_f32 v154, v154, v155\n"
    "v_and_b32 v100, v200, v120\n"
    "v_and_b32 v102, v202, v120\n"
    "v_and_b32 v101, v201, v122\n"
    "v_and_b32 v103, v203, v122\n"
    "v_and_b32 v132, v200, v152\n"
    "v_and_b32 v134, v202, v152\n"
    "v_mfma_f32_16x16x32_bf16 v[104:107], v[252:255], v[100:103], 0\n"
    "v_and_b32 v133, v201, v154\n"
    "v_and_b32 v135, v203, v154\n"
    "s_waitcnt lgkmcnt(6)\n"
    "v_mfma_f32_16x16x32_bf16 v[108:111], v[112:115], v[100:103], 0\n"
    "v_mfma_f32_16x16x32_bf16 v[136:139], v[252:255], v[132:135], 0\n"
    "s_waitcnt lgkmcnt(4)\n"
    "v_mfma_f32_16x16x32_bf16 v[140:143], v[144:147], v[132:135], 0\n"
    "s_waitcnt lgkmcnt(2)\n"
    "v_mfma_f32_16x16x32_bf16 v[124:127], v[116:119], v[100:103], 0\n"
    "s_waitcnt lgkmcnt(0)\n"
    "v_mfma_f32_16x16x32_bf16 v[156:159], v[148:151], v[132:135], 0\n"
    "v_rcp_f32 v128, v104\n"
    "s_nop 0\n"
    "v_pk_mul_f32 v[108:109], v[108:109], v[128:129] op_sel_hi:[1,0]\n"
    "v_cvt_pk_bf16_f32 v120, v108, v109\n"
    "v_pk_mul_f32 v[110:111], v[110:111], v[128:129] op_sel_hi:[1,0]\n"
    "v_cvt_pk_bf16_f32 v121, v110, v111\n"
    "v_rcp_f32 v160, v136\n"
    "buffer_store_dwordx2 v[120:121], v187, s[48:51], s74 offen sc1\n"
    "v_pk_mul_f32 v[124:125], v[124:125], v[128:129] op_sel_hi:[1,0]\n"
    "v_pk_mul_f32 v[126:127], v[126:127], v[128:129] op_sel_hi:[1,0]\n"
    "v_pk_mul_f32 v[140:141], v[140:141], v[160:161] op_sel_hi:[1,0]\n"
    "v_pk_mul_f32 v[142:143], v[142:143], v[160:161] op_sel_hi:[1,0]\n"
    "v_pk_mul_f32 v[156:157], v[156:157], v[160:161] op_sel_hi:[1,0]\n"
    "v_pk_mul_f32 v[158:159], v[158:159], v[160:161] op_sel_hi:[1,0]\n"
    "v_cvt_pk_bf16_f32 v122, v124, v125\n"
    "v_cvt_pk_bf16_f32 v123, v126, v127\n"
    "v_cvt_pk_bf16_f32 v152, v140, v141\n"
    "v_cvt_pk_bf16_f32 v153, v142, v143\n"
    "v_cvt_pk_bf16_f32 v154, v156, v157\n"
    "v_cvt_pk_bf16_f32 v155, v158, v159\n"
    "buffer_store_dwordx4 v[120:123], v186, s[48:51], s74 offen sc1\n"
    "buffer_store_dwordx2 v[152:153], v189, s[48:51], s88 offen sc1\n"
    "buffer_store_dwordx4 v[152:155], v188, s[48:51], s88 offen sc1\n"
    "v_mov_b32 v181, 156700\n"
    "v_mov_b32 v180, 1\n"
    "s_cmp_lt_i32 s35, 0\n"
    "s_cbranch_scc1 ELATE%=\n"
    "s_waitcnt vmcnt(10)\n"
    "ds_add_u32 v181, v180\n"
    "EBACK%=:\n"
    "s_cmp_lg_u32 s69, 6\n"
    "s_cbranch_scc1 H1%=\n"
    "s_setprio 0\n"
    "buffer_load_dwordx4 v[100:103], v204, s[48:51], s14 offen sc1\n"
    "buffer_load_dwordx4 v[104:107], v204, s[48:51], s14 offen offset:3840 sc1\n"
    "buffer_load_dwordx4 v[108:111], v204, s[48:51], s15 offen sc1\n"
    "buffer_load_dwordx4 v[112:115], v204, s[48:51], s15 offen offset:3840 sc1\n"
    "buffer_load_dwordx2 v[116:117], v205, s[48:51], s14 offen sc1\n"
    "buffer_load_dwordx2 v[118:119], v205, s[48:51], s16 offen sc1\n"
    "buffer_load_dwordx4 v[140:143], v204, s[48:51], s17 offen sc1\n"
    "buffer_load_dwordx4 v[144:147], v204, s[48:51], s17 offen offset:3840 sc1\n"
    "buffer_load_dwordx4 v[148:151], v204, s[48:51], s18 offen sc1\n"
    "buffer_load_dwordx4 v[152:155], v204, s[48:51], s18 offen offset:3840 sc1\n"
    "buffer_load_dwordx2 v[156:157], v205, s[48:51], s17 offen sc1\n"
    "buffer_load_dwordx2 v[158:159], v205, s[48:51], s19 offen sc1\n"
    "s_waitcnt vmcnt(16)\n"
    "s_branch H3%=\n"
    "H1%=:\n"
    "s_cmp_lg_u32 s69, 7\n"
    "s_cbranch_scc1 H2%=\n"
    "ds_read_b64_tr_b16 v[0:1], v224\n"
    "ds_read_b64_tr_b16 v[2:3], v224 offset:512\n"
    "ds_read_b64_tr_b16 v[4:5], v225\n"
    "ds_read_b64_tr_b16 v[6:7], v225 offset:512\n"
    "ds_read_b64_tr_b16 v[8:9], v226\n"
    "ds_read_b64_tr_b16 v[10:11], v226 offset:512\n"
    "ds_read_b64_tr_b16 v[12:13], v227\n"
    "ds_read_b64_tr_b16 v[14:15], v227 offset:512\n"
    "ds_read_b64_tr_b16 v[16:17], v224 offset:4096\n"
    "ds_read_b64_tr_b16 v[18:19], v224 offset:4608\n"
    "ds_read_b64_tr_b16 v[20:21], v225 offset:4096\n"
    "ds_read_b64_tr_b16 v[22:23], v225 offset:4608\n"
    "ds_read_b64_tr_b16 v[24:25], v226 offset:4096\n"
    "ds_read_b64_tr_b16 v[26:27], v226 offset:4608\n"
    "ds_read_b64_tr_b16 v[28:29], v227 offset:4096\n"
    "ds_read_b64_tr_b16 v[30:31], v227 offset:4608\n"
    "ds_read_b64_tr_b16 v[32:33], v224 offset:8192\n"
    "ds_read_b64_tr_b16 v[34:35], v224 offset:8704\n"
    "ds_read_b64_tr_b16 v[36:37], v225 offset:8192\n"
    "ds_read_b64_tr_b16 v[38:39], v225 offset:8704\n"
    "ds_read_b64_tr_b16 v[40:41], v226 offset:8192\n"
    "ds_read_b64_tr_b16 v[42:43], v226 offset:8704\n"
    "ds_read_b64_tr_b16 v[44:45], v227 offset:8192\n"
    "ds_read_b64_tr_b16 v[46:47], v227 offset:8704\n"
    "ds_read_b64_tr_b16 v[48:49], v224 offset:12288\n"
    "ds_read_b64_tr_b16 v[50:51], v224 offset:12800\n"
    "ds_read_b64_tr_b16 v[52:53], v225 offset:12288\n"
    "ds_read_b64_tr_b16 v[54:55], v225 offset:12800\n"
    "ds_read_b64_tr_b16 v[56:57], v226 offset:12288\n"
    "ds_read_b64_tr_b16 v[58:59], v226 offset:12800\n"
    "ds_read_b64_tr_b16 v[60:61], v227 offset:12288\n"
    "ds_read_b64_tr_b16 v[62:63], v227 offset:12800\n"
    "ds_read_b64_tr_b16 v[64:65], v224 offset:16384\n"
    "ds_read_b64_tr_b16 v[66:67], v224 offset:16896\n"
    "ds_read_b64_tr_b16 v[68:69], v225 offset:16384\n"
    "ds_read_b64_tr_b16 v[70:71], v225 offset:16896\n"
    "ds_read_b64_tr_b16 v[72:73], v226 offset:16384\n"
    "ds_read_b64_tr_b16 v[74:75], v226 offset:16896\n"
    "ds_read_b64_tr_b16 v[76:77], v227 offset:16384\n"
    "ds_read_b64_tr_b16 v[78:79], v227 offset:16896\n"
    "H2%=:\n"
    "s_waitcnt vmcnt(4)\n"
    "H3%=:\n"
    "s_mov_b32 s3, 16777216\n"
    "v_mov_b32 v181, 156700\n"
    "HA%=:\n"
    "ds_read_b32 v180, v181\n"
    "s_waitcnt lgkmcnt(0)\n"
    "v_cmp_le_u32 vcc, 256, v180\n"
    "s_cbranch_vccnz HAD%=\n"
    "s_sleep 1\n"
    "s_sub_u32 s3, s3, 1\n"
    "s_cbranch_scc0 HA%=\n"
    "HAD%=:\n"
    "v_max3_u32 v180, v80, v81, v81\n"
    "v_max3_u32 v180, v82, v83, v180\n"
    "v_max3_u32 v180, v84, v85, v180\n"
    "v_max3_u32 v180, v86, v87, v180\n"
    "v_max3_u32 v180, v88, v89, v180\n"
    "v_max3_u32 v180, v90, v91, v180\n"
    "v_max3_u32 v180, v92, v93, v180\n"
    "v_max3_u32 v180, v94, v95, v180\n"
    "v_max3_u32 v180, v96, v97, v180\n"
    "v_max3_u32 v180, v98, v99, v180\n"
    "v_cmp_eq_u32 vcc, -1, v180\n"
    "s_cbranch_vccnz FIX0h%=\n"
    "OK0h%=:\n"
    "v_mfma_f32_16x16x32_bf16 v[160:163], v[0:3], v[80:83], 0\n"
    "ds_read_b64_tr_b16 v[120:121], v224 offset:20480\n"
    "ds_read_b64_tr_b16 v[122:123], v224 offset:20992\n"
    "v_mfma_f32_16x16x32_bf16 v[164:167], v[4:7], v[80:83], 0\n"
    "ds_read_b64_tr_b16 v[124:125], v225 offset:20480\n"
    "ds_read_b64_tr_b16 v[126:127], v225 offset:20992\n"
    "v_mfma_f32_16x16x32_bf16 v[168:171], v[8:11], v[80:83], 0\n"
    "ds_read_b64_tr_b16 v[128:129], v226 offset:20480\n"
    "ds_read_b64_tr_b16 v[130:131], v226 offset:20992\n"
    "v_mfma_f32_16x16x32_bf16 v[172:175], v[12:15], v[80:83], 0\n"
    "ds_read_b64_tr_b16 v[132:133], v227 offset:20480\n"
    "ds_read_b64_tr_b16 v[134:135], v227 offset:20992\n"
    "v_mfma_f32_16x16x32_bf16 v[160:163], v[16:19], v[84:87], v[160:163]\n"
    "ds_read_b64_tr_b16 v[136:137], v224 offset:24576\n"
    "ds_read_b64_tr_b16 v[138:139], v224 offset:25088\n"
    "v_mfma_f32_16x16x32_bf16 v[164:167], v[20:23], v[84:87], v[164:167]\n"
    "ds_read_b64_tr_b16 v[184:185], v225 offset:24576\n"
    "ds_read_b64_tr_b16 v[186:187], v225 offset:25088\n"
    "v_mfma_f32_16x16x32_bf16 v[168:171], v[24:27], v[84:87], v[168:171]\n"
    "ds_read_b64_tr_b16 v[192:193], v226 offset:24576\n"
    "ds_read_b64_tr_b16 v[194:195], v226 offset:25088\n"
    "v_mfma_f32_16x16x32_bf16 v[172:175], v[28:31], v[84:87], v[172:175]\n"
    "ds_read_b64_tr_b16 v[196:197], v227 offset:24576\n"
    "ds_read_b64_tr_b16 v[198:199], v227 offset:25088\n"
    "v_mfma_f32_16x16x32_bf16 v[160:163], v[32:35], v[88:91], v[160:163]\n"
    "ds_read_b64_tr_b16 v[200:201], v224 offset:28672\n"
    "ds_read_b64_tr_b16 v[202:203], v224 offset:29184\n"
    "v_mfma_f32_16x16x32_bf16 v[164:167], v[36:39], v[88:91], v[164:167]\n"
    "ds_read_b64_tr_b16 v[220:221], v225 offset:28672\n"
    "ds_read_b64_tr_b16 v[222:223], v225 offset:29184\n"
    "v_mfma_f32_16x16x32_bf16 v[168:171], v[40:43], v[88:91], v[168:171]\n"
    "ds_read_b64_tr_b16 v[236:237], v226 offset:28672\n"
    "ds_read_b64_tr_b16 v[238:239], v226 offset:29184\n"
    "v_mfma_f32_16x16x32_bf16 v[172:175], v[44:47], v[88:91], v[172:175]\n"
    "ds_read_b64_tr_b16 v[240:241], v227 offset:28672\n"
    "ds_read_b64_tr_b16 v[242:243], v227 offset:29184\n"
    "v_mfma_f32_16x16x32_bf16 v[160:163], v[48:51], v[92:95], v[160:163]\n"
    "ds_read_b64_tr_b16 v[244:245], v224 offset:32768\n"
    "ds_read_b64_tr_b16 v[246:247], v224 offset:33280\n"
    "v_mfma_f32_16x16x32_bf16 v[164:167], v[52:55], v[92:95], v[164:167]\n"
    "ds_read_b64_tr_b16 v[248:249], v225 offset:32768\n"
    "ds_read_b64_tr_b16 v[250:251], v225 offset:33280\n"
    "v_mfma_f32_16x16x32_bf16 v[168:171], v[56:59], v[92:95], v[168:171]\n"
    "ds_read_b64_tr_b16 v[252:253], v226 offset:32768\n"
    "ds_read_b64_tr_b16 v[254:255], v226 offset:33280\n"
    "v_mfma_f32_16x16x32_bf16 v[172:175], v[60:63], v[92:95], v[172:175]\n"
    "v_mfma_f32_16x16x32_bf16 v[160:163], v[64:67], v[96:99], v[160:163]\n"
    "v_mfma_f32_16x16x32_bf16 v[164:167], v[68:71], v[96:99], v[164:167]\n"
    "v_mfma_f32_16x16x32_bf16 v[168:171], v[72:75], v[96:99], v[168:171]\n"
    "v_mfma_f32_16x16x32_bf16 v[172:175], v[76:79], v[96:99], v[172:175]\n"
    "ds_read_b64_tr_b16 v[80:81], v227 offset:32768\n"
    "ds_read_b64_tr_b16 v[82:83], v227 offset:33280\n"
    "ds_read_b64_tr_b16 v[84:85], v224 offset:36864\n"
    "ds_read_b64_tr_b16 v[86:87], v224 offset:37376\n"
    "ds_read_b64_tr_b16 v[88:89], v225 offset:36864\n"
    "ds_read_b64_tr_b16 v[90:91], v225 offset:37376\n"
    "ds_read_b64_tr_b16 v[92:93], v226 offset:36864\n"
    "ds_read_b64_tr_b16 v[94:95], v226 offset:37376\n"
    "ds_read_b64_tr_b16 v[96:97], v227 offset:36864\n"
    "ds_read_b64_tr_b16 v[98:99], v227 offset:37376\n"
    "v_subrev_u32 v216, 147456, v235\n"
    "v_add_u32 v216, s79, v216\n"
    "v_add_u32 v216, s79, v216\n"
    "s_and_b32 s0, s69, 1\n"
    "s_lshl_b32 s0, s0, 11\n"
    "v_xor_b32 v216, s0, v216\n"
    "v_xor_b32 v217, 2048, v216\n"
    "s_lshl_b32 s0, s69, 2\n"
    "s_add_u32 s1, s0, 156696\n"
    "v_mov_b32 v218, s1\n"
    "ds_write_b128 v216, v[160:163]\n"
    "ds_write_b128 v216, v[164:167] offset:1024\n"
    "ds_write_b128 v217, v[168:171]\n"
    "ds_write_b128 v217, v[172:175] offset:1024\n"
    "ds_write_b32 v218, v218\n"
    "s_cmp_eq_u32 s69, 6\n"
    "s_cbranch_scc1 GL1%=\n"
    "s_add_u32 s2, s34, 160\n"
    "UL%=:\n"
    "s_memrealtime s[26:27]\n"
    "s_waitcnt lgkmcnt(0)\n"
    "s_sub_u32 s26, s26, s2\n"
    "s_cmp_lt_i32 s26, 0\n"
    "s_cbranch_scc1 UL%=\n"
    "buffer_load_dwordx4 v[100:103], v204, s[48:51], s14 offen sc1\n"
    "buffer_load_dwordx4 v[104:107], v204, s[48:51], s14 offen offset:3840 sc1\n"
    "buffer_load_dwordx4 v[108:111], v204, s[48:51], s15 offen sc1\n"
    "buffer_load_dwordx4 v[112:115], v204, s[48:51], s15 offen offset:3840 sc1\n"
    "buffer_load_dwordx2 v[116:117], v205, s[48:51], s14 offen sc1\n"
    "buffer_load_dwordx2 v[118:119], v205, s[48:51], s16 offen sc1\n"
    "buffer_load_dwordx4 v[140:143], v204, s[48:51], s17 offen sc1\n"
    "buffer_load_dwordx4 v[144:147], v204, s[48:51], s17 offen offset:3840 sc1\n"
    "buffer_load_dwordx4 v[148:151], v204, s[48:51], s18 offen sc1\n"
    "buffer_load_dwordx4 v[152:155], v204, s[48:51], s18 offen offset:3840 sc1\n"
    "buffer_load_dwordx2 v[156:157], v205, s[48:51], s17 offen sc1\n"
    "buffer_load_dwordx2 v[158:159], v205, s[48:51], s19 offen sc1\n"
    "v_mov_b32 v181, 156704\n"
    "ds_write_b32 v181, v181\n"
    "GL1%=:\n"
    "s_waitcnt lgkmcnt(0)\n"
    "ds_read_b32 v176, v234\n"
    "ds_read_b64 v[216:217], v228\n"
    "ds_read_b64 v[218:219], v229\n"
    "s_waitcnt lgkmcnt(2)\n"
    "v_sub_f32 v178, 1.0, v176\n"
    "s_waitcnt lgkmcnt(1)\n"
    "v_lshlrev_b32 v208, 16, v216\n"
    "v_and_b32 v209, 0xffff0000, v216\n"
    "v_pk_mul_f32 v[208:209], v[208:209], v[178:179] op_sel_hi:[1,0]\n"
    "v_lshlrev_b32 v210, 16, v217\n"
    "v_and_b32 v211, 0xffff0000, v217\n"
    "v_pk_mul_f32 v[210:211], v[210:211], v[178:179] op_sel_hi:[1,0]\n"
    "s_waitcnt lgkmcnt(0)\n"
    "v_lshlrev_b32 v212, 16, v218\n"
    "v_and_b32 v213, 0xffff0000, v218\n"
    "v_pk_mul_f32 v[212:213], v[212:213], v[178:179] op_sel_hi:[1,0]\n"
    "v_lshlrev_b32 v214, 16, v219\n"
    "v_and_b32 v215, 0xffff0000, v219\n"
    "v_pk_mul_f32 v[214:215], v[214:215], v[178:179] op_sel_hi:[1,0]\n"
    "s_add_u32 s0, s0, 156664\n"
    "v_mov_b32 v216, s0\n"
    "v_xor_b32 v217, 4, v216\n"
    "v_xor_b32 v218, 2048, v235\n"
    "s_waitcnt vmcnt(6)\n"
    "v_max3_u32 v180, v100, v101, v101\n"
    "v_max3_u32 v180, v102, v103, v180\n"
    "v_max3_u32 v180, v104, v105, v180\n"
    "v_max3_u32 v180, v106, v107, v180\n"
    "v_max3_u32 v180, v108, v109, v180\n"
    "v_max3_u32 v180, v110, v111, v180\n"
    "v_max3_u32 v180, v112, v113, v180\n"
    "v_max3_u32 v180, v114, v115, v180\n"
    "v_max3_u32 v180, v116, v117, v180\n"
    "v_max3_u32 v180, v118, v119, v180\n"
    "v_cmp_eq_u32 vcc, -1, v180\n"
    "s_cbranch_vccnz FIX2h%=\n"
    "OK2h%=:\n"
    "v_mfma_f32_16x16x32_bf16 v[160:163], v[0:3], v[100:103], 0\n"
    "v_mfma_f32_16x16x32_bf16 v[164:167], v[4:7], v[100:103], 0\n"
    "v_mfma_f32_16x16x32_bf16 v[168:171], v[8:11], v[100:103], 0\n"
    "v_mfma_f32_16x16x32_bf16 v[172:175], v[12:15], v[100:103], 0\n"
    "v_mfma_f32_16x16x32_bf16 v[160:163], v[16:19], v[104:107], v[160:163]\n"
    "v_mfma_f32_16x16x32_bf16 v[164:167], v[20:23], v[104:107], v[164:167]\n"
    "v_mfma_f32_16x16x32_bf16 v[168:171], v[24:27], v[104:107], v[168:171]\n"
    "v_mfma_f32_16x16x32_bf16 v[172:175], v[28:31], v[104:107], v[172:175]\n"
    "v_mfma_f32_16x16x32_bf16 v[160:163], v[32:35], v[108:111], v[160:163]\n"
    "v_mfma_f32_16x16x32_bf16 v[164:167], v[36:39], v[108:111], v[164:167]\n"
    "v_mfma_f32_16x16x32_bf16 v[168:171], v[40:43], v[108:111], v[168:171]\n"
    "v_mfma_f32_16x16x32_bf16 v[172:175], v[44:47], v[108:111], v[172:175]\n"
    "v_mfma_f32_16x16x32_bf16 v[160:163], v[48:51], v[112:115], v[160:163]\n"
    "v_mfma_f32_16x16x32_bf16 v[164:167], v[52:55], v[112:115], v[164:167]\n"
    "v_mfma_f32_16x16x32_bf16 v[168:171], v[56:59], v[112:115], v[168:171]\n"
    "v_mfma_f32_16x16x32_bf16 v[172:175], v[60:63], v[112:115], v[172:175]\n"
    "v_mfma_f32_16x16x32_bf16 v[160:163], v[64:67], v[116:119], v[160:163]\n"
    "v_mfma_f32_16x16x32_bf16 v[164:167], v[68:71], v[116:119], v[164:167]\n"
    "v_mfma_f32_16x16x32_bf16 v[168:171], v[72:75], v[116:119], v[168:171]\n"
    "v_mfma_f32_16x16x32_bf16 v[172:175], v[76:79], v[116:119], v[172:175]\n"
    "s_waitcnt lgkmcnt(0)\n"
    "s_waitcnt vmcnt(3)\n"
    "v_max3_u32 v180, v140, v141, v141\n"
    "v_max3_u32 v180, v142, v143, v180\n"
    "v_max3_u32 v180, v144, v145, v180\n"
    "v_max3_u32 v180, v146, v147, v180\n"
    "v_max3_u32 v180, v148, v149, v180\n"
    "v_max3_u32 v180, v150, v151, v180\n"
    "v_cmp_eq_u32 vcc, -1, v180\n"
    "s_cbranch_vccnz FIX3ha%=\n"
    "OK3ha%=:\n"
    "v_mfma_f32_16x16x32_bf16 v[160:163], v[120:123], v[140:143], v[160:163]\n"
    "v_mfma_f32_16x16x32_bf16 v[164:167], v[124:127], v[140:143], v[164:167]\n"
    "v_mfma_f32_16x16x32_bf16 v[168:171], v[128:131], v[140:143], v[168:171]\n"
    "v_mfma_f32_16x16x32_bf16 v[172:175], v[132:135], v[140:143], v[172:175]\n"
    "v_mfma_f32_16x16x32_bf16 v[160:163], v[136:139], v[144:147], v[160:163]\n"
    "v_mfma_f32_16x16x32_bf16 v[164:167], v[184:187], v[144:147], v[164:167]\n"
    "s_waitcnt vmcnt(0)\n"
    "v_mfma_f32_16x16x32_bf16 v[168:171], v[192:195], v[144:147], v[168:171]\n"
    "v_max3_u32 v180, v152, v153, v153\n"
    "v_mfma_f32_16x16x32_bf16 v[172:175], v[196:199], v[144:147], v[172:175]\n"
    "v_max3_u32 v180, v154, v155, v180\n"
    "v_mfma_f32_16x16x32_bf16 v[160:163], v[200:203], v[148:151], v[160:163]\n"
    "v_max3_u32 v180, v156, v157, v180\n"
    "v_mfma_f32_16x16x32_bf16 v[164:167], v[220:223], v[148:151], v[164:167]\n"
    "v_max3_u32 v180, v158, v159, v180\n"
    "v_mfma_f32_16x16x32_bf16 v[168:171], v[236:239], v[148:151], v[168:171]\n"
    "v_cmp_eq_u32 vcc, -1, v180\n"
    "v_mfma_f32_16x16x32_bf16 v[172:175], v[240:243], v[148:151], v[172:175]\n"
    "s_cbranch_vccnz FIX3hb%=\n"
    "OK3hb%=:\n"
    "v_mfma_f32_16x16x32_bf16 v[160:163], v[244:247], v[152:155], v[160:163]\n"
    "v_mfma_f32_16x16x32_bf16 v[164:167], v[248:251], v[152:155], v[164:167]\n"
    "v_mfma_f32_16x16x32_bf16 v[168:171], v[252:255], v[152:155], v[168:171]\n"
    "v_mfma_f32_16x16x32_bf16 v[172:175], v[80:83], v[152:155], v[172:175]\n"
    "v_mfma_f32_16x16x32_bf16 v[160:163], v[84:87], v[156:159], v[160:163]\n"
    "v_mfma_f32_16x16x32_bf16 v[164:167], v[88:91], v[156:159], v[164:167]\n"
    "v_mfma_f32_16x16x32_bf16 v[168:171], v[92:95], v[156:159], v[168:171]\n"
    "v_mfma_f32_16x16x32_bf16 v[172:175], v[96:99], v[156:159], v[172:175]\n"
    "s_nop 6\n"
    "ds_write_b128 v235, v[168:171]\n"
    "ds_write_b128 v235, v[172:175] offset:1024\n"
    "ds_write_b32 v216, v216\n"
    "s_cmp_lt_u32 s25, 96\n"
    "s_cbranch_scc1 YOWN%=\n"
    "YBH%=:\n"
    "s_mov_b32 s3, 16777216\n"
    "EXW%=:\n"
    "ds_read_b32 v4, v217\n"
    "ds_read_b128 v[8:11], v218\n"
    "ds_read_b128 v[12:15], v218 offset:1024\n"
    "s_waitcnt lgkmcnt(0)\n"
    "v_cmp_ne_u32 vcc, 0, v4\n"
    "s_cbranch_vccnz EXD%=\n"
    "s_sub_u32 s3, s3, 1\n"
    "s_cbranch_scc0 EXW%=\n"
    "EXD%=:\n"
    "v_pk_add_f32 v[160:161], v[160:161], v[8:9]\n"
    "v_pk_add_f32 v[162:163], v[162:163], v[10:11]\n"
    "v_pk_add_f32 v[164:165], v[164:165], v[12:13]\n"
    "v_pk_add_f32 v[166:167], v[166:167], v[14:15]\n"
    "v_pk_fma_f32 v[160:161], v[160:161], v[176:177], v[208:209] op_sel_hi:[1,0,1]\n"
    "v_pk_fma_f32 v[162:163], v[162:163], v[176:177], v[210:211] op_sel_hi:[1,0,1]\n"
    "v_cvt_pk_bf16_f32 v160, v160, v161\n"
    "v_cvt_pk_bf16_f32 v161, v162, v163\n"
    "ds_write_b64 v228, v[160:161]\n"
    "v_pk_fma_f32 v[164:165], v[164:165], v[176:177], v[212:213] op_sel_hi:[1,0,1]\n"
    "v_pk_fma_f32 v[166:167], v[166:167], v[176:177], v[214:215] op_sel_hi:[1,0,1]\n"
    "v_cvt_pk_bf16_f32 v164, v164, v165\n"
    "v_cvt_pk_bf16_f32 v165, v166, v167\n"
    "ds_write_b64 v229, v[164:165]\n"
    "ds_read_b128 v[4:7], v232\n"
    "s_waitcnt lgkmcnt(0)\n"
    "buffer_store_dwordx4 v[4:7], v233, s[56:59], 0 offen nt\n"
    "s_endpgm\n"
    "FULL%=:\n"
    "ds_read_b64_tr_b16 v[0:1], v224\n"
    "ds_read_b64_tr_b16 v[2:3], v224 offset:512\n"
    "ds_read_b64_tr_b16 v[4:5], v225\n"
    "ds_read_b64_tr_b16 v[6:7], v225 offset:512\n"
    "ds_read_b64_tr_b16 v[8:9], v226\n"
    "ds_read_b64_tr_b16 v[10:11], v226 offset:512\n"
    "ds_read_b64_tr_b16 v[12:13], v227\n"
    "ds_read_b64_tr_b16 v[14:15], v227 offset:512\n"
    "ds_read_b64_tr_b16 v[16:17], v224 offset:4096\n"
    "ds_read_b64_tr_b16 v[18:19], v224 offset:4608\n"
    "ds_read_b64_tr_b16 v[20:21], v225 offset:4096\n"
    "ds_read_b64_tr_b16 v[22:23], v225 offset:4608\n"
    "ds_read_b64_tr_b16 v[24:25], v226 offset:4096\n"
    "ds_read_b64_tr_b16 v[26:27], v226 offset:4608\n"
    "ds_read_b64_tr_b16 v[28:29], v227 offset:4096\n"
    "ds_read_b64_tr_b16 v[30:31], v227 offset:4608\n"
    "ds_read_b64_tr_b16 v[32:33], v224 offset:8192\n"
    "ds_read_b64_tr_b16 v[34:35], v224 offset:8704\n"
    "ds_read_b64_tr_b16 v[36:37], v225 offset:8192\n"
    "ds_read_b64_tr_b16 v[38:39], v225 offset:8704\n"
    "ds_read_b64_tr_b16 v[40:41], v226 offset:8192\n"
    "ds_read_b64_tr_b16 v[42:43], v226 offset:8704\n"
    "ds_read_b64_tr_b16 v[44:45], v227 offset:8192\n"
    "ds_read_b64_tr_b16 v[46:47], v227 offset:8704\n"
    "ds_read_b64_tr_b16 v[48:49], v224 offset:12288\n"
    "ds_read_b64_tr_b16 v[50:51], v224 offset:12800\n"
    "ds_read_b64_tr_b16 v[52:53], v225 offset:12288\n"
    "ds_read_b64_tr_b16 v[54:55], v225 offset:12800\n"
    "ds_read_b64_tr_b16 v[56:57], v226 offset:12288\n"
    "ds_read_b64_tr_b16 v[58:59], v226 offset:12800\n"
    "ds_read_b64_tr_b16 v[60:61], v227 offset:12288\n"
    "ds_read_b64_tr_b16 v[62:63], v227 offset:12800\n"
    "ds_read_b64_tr_b16 v[64:65], v224 offset:16384\n"
    "ds_read_b64_tr_b16 v[66:67], v224 offset:16896\n"
    "ds_read_b64_tr_b16 v[68:69], v225 offset:16384\n"
    "ds_read_b64_tr_b16 v[70:71], v225 offset:16896\n"
    "ds_read_b64_tr_b16 v[72:73], v226 offset:16384\n"
    "ds_read_b64_tr_b16 v[74:75], v226 offset:16896\n"
    "ds_read_b64_tr_b16 v[76:77], v227 offset:16384\n"
    "ds_read_b64_tr_b16 v[78:79], v227 offset:16896\n"
    "v_mov_b32 v140, 156544\n"
    "ds_read_b128 v[144:147], v140\n"
    "s_add_u32 s8, s76, 0\n"
    "s_add_u32 s9, s76, 7680\n"
    "s_add_u32 s10, s76, 61440\n"
    "s_add_u32 s11, s76, 15360\n"
    "s_add_u32 s12, s76, 23040\n"
    "s_add_u32 s13, s76, 76800\n"
    "s_add_u32 s17, s75, 46080\n"
    "s_add_u32 s18, s75, 53760\n"
    "s_add_u32 s19, s75, 107520\n"
    "s_waitcnt lgkmcnt(0)\n"
    "v_readfirstlane_b32 s20, v144\n"
    "v_readfirstlane_b32 s21, v146\n"
    "s_andn2_b32 s24, 2047, s20\n"
    "s_lshr_b32 s22, s24, 1\n"
    "s_sub_u32 s22, s22, s82\n"
    "s_and_b32 s24, s24, 1\n"
    "s_cmp_eq_u32 s24, s81\n"
    "s_cselect_b32 s22, s22, -1\n"
    "s_andn2_b32 s24, 2047, s21\n"
    "s_lshr_b32 s23, s24, 1\n"
    "s_sub_u32 s23, s23, s82\n"
    "s_and_b32 s24, s24, 1\n"
    "s_cmp_eq_u32 s24, s81\n"
    "s_cselect_b32 s23, s23, -1\n"
    "s_min_u32 s25, s22, s23\n"
    "s_mov_b32 s3, 16777216\n"
    "v_mov_b32 v141, 156708\n"
    "RXF%=:\n"
    "ds_read_b32 v140, v141\n"
    "s_waitcnt lgkmcnt(0)\n"
    "v_cmp_ne_u32 vcc, 0, v140\n"
    "s_cbranch_vccnz RXFD%=\n"
    "s_sleep 1\n"
    "s_sub_u32 s3, s3, 1\n"
    "s_cbranch_scc0 RXF%=\n"
    "RXFD%=:\n"
    "v_readfirstlane_b32 s34, v140\n"
    "s_nop 0\n"
    "s_add_u32 s2, s34, 65\n"
    "U0%=:\n"
    "s_memrealtime s[0:1]\n"
    "s_waitcnt lgkmcnt(0)\n"
    "s_sub_u32 s0, s0, s2\n"
    "s_cmp_lt_i32 s0, 0\n"
    "s_cbranch_scc1 U0%=\n"
    "buffer_load_dwordx4 v[80:83], v204, s[48:51], s8 offen sc1\n"
    "buffer_load_dwordx4 v[84:87], v204, s[48:51], s8 offen offset:3840 sc1\n"
    "buffer_load_dwordx4 v[88:91], v204, s[48:51], s9 offen sc1\n"
    "buffer_load_dwordx4 v[92:95], v204, s[48:51], s9 offen offset:3840 sc1\n"
    "buffer_load_dwordx2 v[96:97], v205, s[48:51], s8 offen sc1\n"
    "buffer_load_dwordx2 v[98:99], v205, s[48:51], s10 offen sc1\n"
    "buffer_load_dwordx4 v[120:123], v204, s[48:51], s11 offen sc1\n"
    "buffer_load_dwordx4 v[124:127], v204, s[48:51], s11 offen offset:3840 sc1\n"
    "buffer_load_dwordx4 v[128:131], v204, s[48:51], s12 offen sc1\n"
    "buffer_load_dwordx4 v[132:135], v204, s[48:51], s12 offen offset:3840 sc1\n"
    "buffer_load_dwordx2 v[136:137], v205, s[48:51], s11 offen sc1\n"
    "buffer_load_dwordx2 v[138:139], v205, s[48:51], s13 offen sc1\n"
    "v_mbcnt_lo_u32_b32 v100, -1, 0\n"
    "v_mbcnt_hi_u32_b32 v100, -1, v100\n"
    "v_lshlrev_b32 v100, 4, v100\n"
    "s_lshl_b32 s0, s69, 12\n"
    "v_add_u32 v100, s0, v100\n"
    "s_and_b32 s26, s69, 1\n"
    "s_mul_i32 s26, s26, 20480\n"
    "s_lshl_b32 s14, s69, 2\n"
    "s_add_u32 s14, s14, 156712\n"
    "s_cmp_lt_u32 s25, 96\n"
    "s_cbranch_scc1 YPRE%=\n"
    "YB0%=:\n"
    "s_waitcnt vmcnt(6)\n"
    "v_max3_u32 v180, v80, v81, v81\n"
    "v_max3_u32 v180, v82, v83, v180\n"
    "v_max3_u32 v180, v84, v85, v180\n"
    "v_max3_u32 v180, v86, v87, v180\n"
    "v_max3_u32 v180, v88, v89, v180\n"
    "v_max3_u32 v180, v90, v91, v180\n"
    "v_max3_u32 v180, v92, v93, v180\n"
    "v_max3_u32 v180, v94, v95, v180\n"
    "v_max3_u32 v180, v96, v97, v180\n"
    "v_max3_u32 v180, v98, v99, v180\n"
    "v_cmp_eq_u32 vcc, -1, v180\n"
    "s_cbranch_vccnz FIX0f%=\n"
    "OK0f%=:\n"
    "v_mfma_f32_16x16x32_bf16 v[160:163], v[0:3], v[80:83], 0\n"
    "ds_read_b64_tr_b16 v[0:1], v224 offset:20480\n"
    "ds_read_b64_tr_b16 v[2:3], v224 offset:20992\n"
    "v_mfma_f32_16x16x32_bf16 v[164:167], v[4:7], v[80:83], 0\n"
    "ds_read_b64_tr_b16 v[4:5], v225 offset:20480\n"
    "ds_read_b64_tr_b16 v[6:7], v225 offset:20992\n"
    "v_mfma_f32_16x16x32_bf16 v[168:171], v[8:11], v[80:83], 0\n"
    "ds_read_b64_tr_b16 v[8:9], v226 offset:20480\n"
    "ds_read_b64_tr_b16 v[10:11], v226 offset:20992\n"
    "v_mfma_f32_16x16x32_bf16 v[172:175], v[12:15], v[80:83], 0\n"
    "ds_read_b64_tr_b16 v[12:13], v227 offset:20480\n"
    "ds_read_b64_tr_b16 v[14:15], v227 offset:20992\n"
    "v_mfma_f32_16x16x32_bf16 v[160:163], v[16:19], v[84:87], v[160:163]\n"
    "ds_read_b64_tr_b16 v[16:17], v224 offset:24576\n"
    "ds_read_b64_tr_b16 v[18:19], v224 offset:25088\n"
    "v_mfma_f32_16x16x32_bf16 v[164:167], v[20:23], v[84:87], v[164:167]\n"
    "ds_read_b64_tr_b16 v[20:21], v225 offset:24576\n"
    "ds_read_b64_tr_b16 v[22:23], v225 offset:25088\n"
    "v_mfma_f32_16x16x32_bf16 v[168:171], v[24:27], v[84:87], v[168:171]\n"
    "ds_read_b64_tr_b16 v[24:25], v226 offset:24576\n"
    "ds_read_b64_tr_b16 v[26:27], v226 offset:25088\n"
    "v_mfma_f32_16x16x32_bf16 v[172:175], v[28:31], v[84:87], v[172:175]\n"
    "ds_read_b64_tr_b16 v[28:29], v227 offset:24576\n"
    "ds_read_b64_tr_b16 v[30:31], v227 offset:25088\n"
    "v_mfma_f32_16x16x32_bf16 v[160:163], v[32:35], v[88:91], v[160:163]\n"
    "ds_read_b64_tr_b16 v[32:33], v224 offset:28672\n"
    "ds_read_b64_tr_b16 v[34:35], v224 offset:29184\n"
    "v_mfma_f32_16x16x32_bf16 v[164:167], v[36:39], v[88:91], v[164:167]\n"
    "ds_read_b64_tr_b16 v[36:37], v225 offset:28672\n"
    "ds_read_b64_tr_b16 v[38:39], v225 offset:29184\n"
    "v_mfma_f32_16x16x32_bf16 v[168:171], v[40:43], v[88:91], v[168:171]\n"
    "ds_read_b64_tr_b16 v[40:41], v226 offset:28672\n"
    "ds_read_b64_tr_b16 v[42:43], v226 offset:29184\n"
    "v_mfma_f32_16x16x32_bf16 v[172:175], v[44:47], v[88:91], v[172:175]\n"
    "ds_read_b64_tr_b16 v[44:45], v227 offset:28672\n"
    "ds_read_b64_tr_b16 v[46:47], v227 offset:29184\n"
    "v_mfma_f32_16x16x32_bf16 v[160:163], v[48:51], v[92:95], v[160:163]\n"
    "ds_read_b64_tr_b16 v[48:49], v224 offset:32768\n"
    "ds_read_b64_tr_b16 v[50:51], v224 offset:33280\n"
    "v_mfma_f32_16x16x32_bf16 v[164:167], v[52:55], v[92:95], v[164:167]\n"
    "ds_read_b64_tr_b16 v[52:53], v225 offset:32768\n"
    "ds_read_b64_tr_b16 v[54:55], v225 offset:33280\n"
    "v_mfma_f32_16x16x32_bf16 v[168:171], v[56:59], v[92:95], v[168:171]\n"
    "ds_read_b64_tr_b16 v[56:57], v226 offset:32768\n"
    "ds_read_b64_tr_b16 v[58:59], v226 offset:33280\n"
    "v_mfma_f32_16x16x32_bf16 v[172:175], v[60:63], v[92:95], v[172:175]\n"
    "ds_read_b64_tr_b16 v[60:61], v227 offset:32768\n"
    "ds_read_b64_tr_b16 v[62:63], v227 offset:33280\n"
    "v_mfma_f32_16x16x32_bf16 v[160:163], v[64:67], v[96:99], v[160:163]\n"
    "ds_read_b64_tr_b16 v[64:65], v224 offset:36864\n"
    "ds_read_b64_tr_b16 v[66:67], v224 offset:37376\n"
    "v_mfma_f32_16x16x32_bf16 v[164:167], v[68:71], v[96:99], v[164:167]\n"
    "ds_read_b64_tr_b16 v[68:69], v225 offset:36864\n"
    "ds_read_b64_tr_b16 v[70:71], v225 offset:37376\n"
    "v_mfma_f32_16x16x32_bf16 v[168:171], v[72:75], v[96:99], v[168:171]\n"
    "ds_read_b64_tr_b16 v[72:73], v226 offset:36864\n"
    "ds_read_b64_tr_b16 v[74:75], v226 offset:37376\n"
    "v_mfma_f32_16x16x32_bf16 v[172:175], v[76:79], v[96:99], v[172:175]\n"
    "ds_read_b64_tr_b16 v[76:77], v227 offset:36864\n"
    "ds_read_b64_tr_b16 v[78:79], v227 offset:37376\n"
    "s_waitcnt vmcnt(0)\n"
    "v_max3_u32 v180, v120, v121, v121\n"
    "v_max3_u32 v180, v122, v123, v180\n"
    "v_max3_u32 v180, v124, v125, v180\n"
    "v_max3_u32 v180, v126, v127, v180\n"
    "v_max3_u32 v180, v128, v129, v180\n"
    "v_max3_u32 v180, v130, v131, v180\n"
    "v_max3_u32 v180, v132, v133, v180\n"
    "v_max3_u32 v180, v134, v135, v180\n"
    "v_max3_u32 v180, v136, v137, v180\n"
    "v_max3_u32 v180, v138, v139, v180\n"
    "v_cmp_eq_u32 vcc, -1, v180\n"
    "s_cbranch_vccnz FIX1f%=\n"
    "OK1f%=:\n"
    "v_add_u32 v224, s26, v224\n"
    "v_add_u32 v225, s26, v225\n"
    "v_add_u32 v226, s26, v226\n"
    "v_add_u32 v227, s26, v227\n"
    "s_mov_b32 s3, 16777216\n"
    "v_mov_b32 v181, 156700\n"
    "FA%=:\n"
    "ds_read_b32 v180, v181\n"
    "s_waitcnt lgkmcnt(0)\n"
    "v_cmp_le_u32 vcc, 256, v180\n"
    "s_cbranch_vccnz FAD%=\n"
    "s_sleep 1\n"
    "s_sub_u32 s3, s3, 1\n"
    "s_cbranch_scc0 FA%=\n"
    "FAD%=:\n"
    "v_mfma_f32_16x16x32_bf16 v[160:163], v[0:3], v[120:123], v[160:163]\n"
    "ds_read_b64_tr_b16 v[0:1], v224 offset:40960\n"
    "ds_read_b64_tr_b16 v[2:3], v224 offset:41472\n"
    "v_mfma_f32_16x16x32_bf16 v[164:167], v[4:7], v[120:123], v[164:167]\n"
    "ds_read_b64_tr_b16 v[4:5], v225 offset:40960\n"
    "ds_read_b64_tr_b16 v[6:7], v225 offset:41472\n"
    "v_mfma_f32_16x16x32_bf16 v[168:171], v[8:11], v[120:123], v[168:171]\n"
    "ds_read_b64_tr_b16 v[8:9], v226 offset:40960\n"
    "ds_read_b64_tr_b16 v[10:11], v226 offset:41472\n"
    "v_mfma_f32_16x16x32_bf16 v[172:175], v[12:15], v[120:123], v[172:175]\n"
    "ds_read_b64_tr_b16 v[12:13], v227 offset:40960\n"
    "ds_read_b64_tr_b16 v[14:15], v227 offset:41472\n"
    "v_mfma_f32_16x16x32_bf16 v[160:163], v[16:19], v[124:127], v[160:163]\n"
    "ds_read_b64_tr_b16 v[16:17], v224 offset:45056\n"
    "ds_read_b64_tr_b16 v[18:19], v224 offset:45568\n"
    "v_mfma_f32_16x16x32_bf16 v[164:167], v[20:23], v[124:127], v[164:167]\n"
    "ds_read_b64_tr_b16 v[20:21], v225 offset:45056\n"
    "ds_read_b64_tr_b16 v[22:23], v225 offset:45568\n"
    "v_mfma_f32_16x16x32_bf16 v[168:171], v[24:27], v[124:127], v[168:171]\n"
    "ds_read_b64_tr_b16 v[24:25], v226 offset:45056\n"
    "ds_read_b64_tr_b16 v[26:27], v226 offset:45568\n"
    "v_mfma_f32_16x16x32_bf16 v[172:175], v[28:31], v[124:127], v[172:175]\n"
    "ds_read_b64_tr_b16 v[28:29], v227 offset:45056\n"
    "ds_read_b64_tr_b16 v[30:31], v227 offset:45568\n"
    "v_mfma_f32_16x16x32_bf16 v[160:163], v[32:35], v[128:131], v[160:163]\n"
    "ds_read_b64_tr_b16 v[32:33], v224 offset:49152\n"
    "ds_read_b64_tr_b16 v[34:35], v224 offset:49664\n"
    "v_mfma_f32_16x16x32_bf16 v[164:167], v[36:39], v[128:131], v[164:167]\n"
    "ds_read_b64_tr_b16 v[36:37], v225 offset:49152\n"
    "ds_read_b64_tr_b16 v[38:39], v225 offset:49664\n"
    "v_mfma_f32_16x16x32_bf16 v[168:171], v[40:43], v[128:131], v[168:171]\n"
    "ds_read_b64_tr_b16 v[40:41], v226 offset:49152\n"
    "ds_read_b64_tr_b16 v[42:43], v226 offset:49664\n"
    "v_mfma_f32_16x16x32_bf16 v[172:175], v[44:47], v[128:131], v[172:175]\n"
    "ds_read_b64_tr_b16 v[44:45], v227 offset:49152\n"
    "ds_read_b64_tr_b16 v[46:47], v227 offset:49664\n"
    "v_mfma_f32_16x16x32_bf16 v[160:163], v[48:51], v[132:135], v[160:163]\n"
    "ds_read_b64_tr_b16 v[48:49], v224 offset:53248\n"
    "ds_read_b64_tr_b16 v[50:51], v224 offset:53760\n"
    "v_mfma_f32_16x16x32_bf16 v[164:167], v[52:55], v[132:135], v[164:167]\n"
    "ds_read_b64_tr_b16 v[52:53], v225 offset:53248\n"
    "ds_read_b64_tr_b16 v[54:55], v225 offset:53760\n"
    "v_mfma_f32_16x16x32_bf16 v[168:171], v[56:59], v[132:135], v[168:171]\n"
    "ds_read_b64_tr_b16 v[56:57], v226 offset:53248\n"
    "ds_read_b64_tr_b16 v[58:59], v226 offset:53760\n"
    "v_mfma_f32_16x16x32_bf16 v[172:175], v[60:63], v[132:135], v[172:175]\n"
    "ds_read_b64_tr_b16 v[60:61], v227 offset:53248\n"
    "ds_read_b64_tr_b16 v[62:63], v227 offset:53760\n"
    "v_mfma_f32_16x16x32_bf16 v[160:163], v[64:67], v[136:139], v[160:163]\n"
    "ds_read_b64_tr_b16 v[64:65], v224 offset:57344\n"
    "ds_read_b64_tr_b16 v[66:67], v224 offset:57856\n"
    "v_mfma_f32_16x16x32_bf16 v[164:167], v[68:71], v[136:139], v[164:167]\n"
    "ds_read_b64_tr_b16 v[68:69], v225 offset:57344\n"
    "ds_read_b64_tr_b16 v[70:71], v225 offset:57856\n"
    "v_mfma_f32_16x16x32_bf16 v[168:171], v[72:75], v[136:139], v[168:171]\n"
    "ds_read_b64_tr_b16 v[72:73], v226 offset:57344\n"
    "ds_read_b64_tr_b16 v[74:75], v226 offset:57856\n"
    "v_mfma_f32_16x16x32_bf16 v[172:175], v[76:79], v[136:139], v[172:175]\n"
    "ds_read_b64_tr_b16 v[76:77], v227 offset:57344\n"
    "ds_read_b64_tr_b16 v[78:79], v227 offset:57856\n"
    "s_cmp_lt_u32 s25, 96\n"
    "s_cbranch_scc1 YJOB%=\n"
    "YB1%=:\n"
    "s_mov_b32 s3, 16777216\n"
    "v_mov_b32 v181, 156704\n"
    "GOL%=:\n"
    "ds_read_b32 v180, v181\n"
    "s_waitcnt lgkmcnt(0)\n"
    "v_cmp_ne_u32 vcc, 0, v180\n"
    "s_cbranch_vccnz GOLD%=\n"
    "s_sleep 1\n"
    "s_sub_u32 s3, s3, 1\n"
    "s_cbranch_scc0 GOL%=\n"
    "GOLD%=:\n"
    "buffer_load_dwordx4 v[140:143], v204, s[48:51], s17 offen sc1\n"
    "buffer_load_dwordx4 v[144:147], v204, s[48:51], s17 offen offset:3840 sc1\n"
    "buffer_load_dwordx4 v[148:151], v204, s[48:51], s18 offen sc1\n"
    "buffer_load_dwordx4 v[152:155], v204, s[48:51], s18 offen offset:3840 sc1\n"
    "buffer_load_dwordx2 v[156:157], v205, s[48:51], s17 offen sc1\n"
    "buffer_load_dwordx2 v[158:159], v205, s[48:51], s19 offen sc1\n"
    "ds_read_b32 v176, v234\n"
    "ds_read_b64 v[104:105], v228\n"
    "ds_read_b64 v[106:107], v229\n"
    "ds_read_b64 v[108:109], v230\n"
    "ds_read_b64 v[110:111], v231\n"
    "s_waitcnt lgkmcnt(4)\n"
    "v_sub_f32 v178, 1.0, v176\n"
    "s_waitcnt lgkmcnt(3)\n"
    "v_lshlrev_b32 v208, 16, v104\n"
    "v_and_b32 v209, 0xffff0000, v104\n"
    "v_pk_mul_f32 v[208:209], v[208:209], v[178:179] op_sel_hi:[1,0]\n"
    "v_lshlrev_b32 v210, 16, v105\n"
    "v_and_b32 v211, 0xffff0000, v105\n"
    "v_pk_mul_f32 v[210:211], v[210:211], v[178:179] op_sel_hi:[1,0]\n"
    "s_waitcnt lgkmcnt(2)\n"
    "v_lshlrev_b32 v212, 16, v106\n"
    "v_and_b32 v213, 0xffff0000, v106\n"
    "v_pk_mul_f32 v[212:213], v[212:213], v[178:179] op_sel_hi:[1,0]\n"
    "v_lshlrev_b32 v214, 16, v107\n"
    "v_and_b32 v215, 0xffff0000, v107\n"
    "v_pk_mul_f32 v[214:215], v[214:215], v[178:179] op_sel_hi:[1,0]\n"
    "s_waitcnt lgkmcnt(1)\n"
    "v_lshlrev_b32 v216, 16, v108\n"
    "v_and_b32 v217, 0xffff0000, v108\n"
    "v_pk_mul_f32 v[216:217], v[216:217], v[178:179] op_sel_hi:[1,0]\n"
    "v_lshlrev_b32 v218, 16, v109\n"
    "v_and_b32 v219, 0xffff0000, v109\n"
    "v_pk_mul_f32 v[218:219], v[218:219], v[178:179] op_sel_hi:[1,0]\n"
    "s_waitcnt lgkmcnt(0)\n"
    "v_lshlrev_b32 v220, 16, v110\n"
    "v_and_b32 v221, 0xffff0000, v110\n"
    "v_pk_mul_f32 v[220:221], v[220:221], v[178:179] op_sel_hi:[1,0]\n"
    "v_lshlrev_b32 v222, 16, v111\n"
    "v_and_b32 v223, 0xffff0000, v111\n"
    "v_pk_mul_f32 v[222:223], v[222:223], v[178:179] op_sel_hi:[1,0]\n"
    "s_mov_b32 s3, 16777216\n"
    "v_mov_b32 v181, s14\n"
    "PW%=:\n"
    "ds_read_b32 v180, v181\n"
    "s_waitcnt lgkmcnt(0)\n"
    "v_cmp_ne_u32 vcc, 0, v180\n"
    "s_cbranch_vccnz PWD%=\n"
    "s_sleep 1\n"
    "s_sub_u32 s3, s3, 1\n"
    "s_cbranch_scc0 PW%=\n"
    "PWD%=:\n"
    "ds_read_b128 v[112:115], v100\n"
    "ds_read_b128 v[116:119], v100 offset:1024\n"
    "ds_read_b128 v[120:123], v100 offset:2048\n"
    "ds_read_b128 v[124:127], v100 offset:3072\n"
    "s_waitcnt lgkmcnt(3)\n"
    "v_pk_add_f32 v[160:161], v[160:161], v[112:113]\n"
    "v_pk_add_f32 v[162:163], v[162:163], v[114:115]\n"
    "s_waitcnt lgkmcnt(2)\n"
    "v_pk_add_f32 v[164:165], v[164:165], v[116:117]\n"
    "v_pk_add_f32 v[166:167], v[166:167], v[118:119]\n"
    "s_waitcnt lgkmcnt(1)\n"
    "v_pk_add_f32 v[168:169], v[168:169], v[120:121]\n"
    "v_pk_add_f32 v[170:171], v[170:171], v[122:123]\n"
    "s_waitcnt lgkmcnt(0)\n"
    "v_pk_add_f32 v[172:173], v[172:173], v[124:125]\n"
    "v_pk_add_f32 v[174:175], v[174:175], v[126:127]\n"
    "s_cmp_lt_u32 s25, 96\n"
    "s_cbranch_scc1 YW%=\n"
    "YB2%=:\n"
    "s_waitcnt vmcnt(3)\n"
    "v_max3_u32 v180, v140, v141, v141\n"
    "v_max3_u32 v180, v142, v143, v180\n"
    "v_max3_u32 v180, v144, v145, v180\n"
    "v_max3_u32 v180, v146, v147, v180\n"
    "v_max3_u32 v180, v148, v149, v180\n"
    "v_max3_u32 v180, v150, v151, v180\n"
    "v_cmp_eq_u32 vcc, -1, v180\n"
    "s_cbranch_vccnz FIX3fa%=\n"
    "OK3fa%=:\n"
    "v_mfma_f32_16x16x32_bf16 v[160:163], v[0:3], v[140:143], v[160:163]\n"
    "v_mfma_f32_16x16x32_bf16 v[164:167], v[4:7], v[140:143], v[164:167]\n"
    "v_mfma_f32_16x16x32_bf16 v[168:171], v[8:11], v[140:143], v[168:171]\n"
    "v_mfma_f32_16x16x32_bf16 v[172:175], v[12:15], v[140:143], v[172:175]\n"
    "v_mfma_f32_16x16x32_bf16 v[160:163], v[16:19], v[144:147], v[160:163]\n"
    "v_mfma_f32_16x16x32_bf16 v[164:167], v[20:23], v[144:147], v[164:167]\n"
    "s_waitcnt vmcnt(0)\n"
    "v_mfma_f32_16x16x32_bf16 v[168:171], v[24:27], v[144:147], v[168:171]\n"
    "v_max3_u32 v180, v152, v153, v153\n"
    "v_mfma_f32_16x16x32_bf16 v[172:175], v[28:31], v[144:147], v[172:175]\n"
    "v_max3_u32 v180, v154, v155, v180\n"
    "v_mfma_f32_16x16x32_bf16 v[160:163], v[32:35], v[148:151], v[160:163]\n"
    "v_max3_u32 v180, v156, v157, v180\n"
    "v_mfma_f32_16x16x32_bf16 v[164:167], v[36:39], v[148:151], v[164:167]\n"
    "v_max3_u32 v180, v158, v159, v180\n"
    "v_mfma_f32_16x16x32_bf16 v[168:171], v[40:43], v[148:151], v[168:171]\n"
    "v_cmp_eq_u32 vcc, -1, v180\n"
    "v_mfma_f32_16x16x32_bf16 v[172:175], v[44:47], v[148:151], v[172:175]\n"
    "s_cbranch_vccnz FIX3fb%=\n"
    "OK3fb%=:\n"
    "v_mfma_f32_16x16x32_bf16 v[160:163], v[48:51], v[152:155], v[160:163]\n"
    "v_mfma_f32_16x16x32_bf16 v[164:167], v[52:55], v[152:155], v[164:167]\n"
    "v_mfma_f32_16x16x32_bf16 v[168:171], v[56:59], v[152:155], v[168:171]\n"
    "v_mfma_f32_16x16x32_bf16 v[172:175], v[60:63], v[152:155], v[172:175]\n"
    "v_mfma_f32_16x16x32_bf16 v[160:163], v[64:67], v[156:159], v[160:163]\n"
    "v_mfma_f32_16x16x32_bf16 v[164:167], v[68:71], v[156:159], v[164:167]\n"
    "v_mfma_f32_16x16x32_bf16 v[168:171], v[72:75], v[156:159], v[168:171]\n"
    "v_mfma_f32_16x16x32_bf16 v[172:175], v[76:79], v[156:159], v[172:175]\n"
    "s_cmp_lt_u32 s25, 96\n"
    "s_cbranch_scc1 YOWN%=\n"
    "YBF%=:\n"
    "s_nop 2\n"
    "v_pk_fma_f32 v[160:161], v[160:161], v[176:177], v[208:209] op_sel_hi:[1,0,1]\n"
    "v_pk_fma_f32 v[162:163], v[162:163], v[176:177], v[210:211] op_sel_hi:[1,0,1]\n"
    "v_cvt_pk_bf16_f32 v160, v160, v161\n"
    "v_cvt_pk_bf16_f32 v161, v162, v163\n"
    "ds_write_b64 v228, v[160:161]\n"
    "v_pk_fma_f32 v[164:165], v[164:165], v[176:177], v[212:213] op_sel_hi:[1,0,1]\n"
    "v_pk_fma_f32 v[166:167], v[166:167], v[176:177], v[214:215] op_sel_hi:[1,0,1]\n"
    "v_cvt_pk_bf16_f32 v164, v164, v165\n"
    "v_cvt_pk_bf16_f32 v165, v166, v167\n"
    "ds_write_b64 v229, v[164:165]\n"
    "v_pk_fma_f32 v[168:169], v[168:169], v[176:177], v[216:217] op_sel_hi:[1,0,1]\n"
    "v_pk_fma_f32 v[170:171], v[170:171], v[176:177], v[218:219] op_sel_hi:[1,0,1]\n"
    "v_cvt_pk_bf16_f32 v168, v168, v169\n"
    "v_cvt_pk_bf16_f32 v169, v170, v171\n"
    "ds_write_b64 v230, v[168:169]\n"
    "v_pk_fma_f32 v[172:173], v[172:173], v[176:177], v[220:221] op_sel_hi:[1,0,1]\n"
    "v_pk_fma_f32 v[174:175], v[174:175], v[176:177], v[222:223] op_sel_hi:[1,0,1]\n"
    "v_cvt_pk_bf16_f32 v172, v172, v173\n"
    "v_cvt_pk_bf16_f32 v173, v174, v175\n"
    "ds_write_b64 v231, v[172:173]\n"
    "ds_read_b128 v[0:3], v232\n"
    "ds_read_b128 v[4:7], v235\n"
    "s_movk_i32 s0, 20480\n"
    "s_waitcnt lgkmcnt(1)\n"
    "buffer_store_dwordx4 v[0:3], v233, s[56:59], 0 offen nt\n"
    "s_waitcnt lgkmcnt(0)\n"
    "buffer_store_dwordx4 v[4:7], v233, s[56:59], s0 offen nt\n"
    "s_endpgm\n"
    "FIX0h%=:\n"
    "s_mov_b32 s3, 262144\n"
    "s_nop 7\n"
    "FL0h%=:\n"
    "buffer_load_dwordx4 v[80:83], v204, s[48:51], s8 offen sc1\n"
    "buffer_load_dwordx4 v[84:87], v204, s[48:51], s8 offen offset:3840 sc1\n"
    "buffer_load_dwordx4 v[88:91], v204, s[48:51], s9 offen sc1\n"
    "buffer_load_dwordx4 v[92:95], v204, s[48:51], s9 offen offset:3840 sc1\n"
    "buffer_load_dwordx2 v[96:97], v205, s[48:51], s8 offen sc1\n"
    "buffer_load_dwordx2 v[98:99], v205, s[48:51], s10 offen sc1\n"
    "s_waitcnt vmcnt(0)\n"
    "v_max3_u32 v180, v80, v81, v81\n"
    "v_max3_u32 v180, v82, v83, v180\n"
    "v_max3_u32 v180, v84, v85, v180\n"
    "v_max3_u32 v180, v86, v87, v180\n"
    "v_max3_u32 v180, v88, v89, v180\n"
    "v_max3_u32 v180, v90, v91, v180\n"
    "v_max3_u32 v180, v92, v93, v180\n"
    "v_max3_u32 v180, v94, v95, v180\n"
    "v_max3_u32 v180, v96, v97, v180\n"
    "v_max3_u32 v180, v98, v99, v180\n"
    "v_cmp_eq_u32 vcc, -1, v180\n"
    "s_cbranch_vccz OK0h%=\n"
    "s_sub_u32 s3, s3, 1\n"
    "s_cbranch_scc0 FL0h%=\n"
    "s_branch OK0h%=\n"
    "FIX2h%=:\n"
    "s_mov_b32 s3, 262144\n"
    "s_nop 7\n"
    "FL2h%=:\n"
    "buffer_load_dwordx4 v[100:103], v204, s[48:51], s14 offen sc1\n"
    "buffer_load_dwordx4 v[104:107], v204, s[48:51], s14 offen offset:3840 sc1\n"
    "buffer_load_dwordx4 v[108:111], v204, s[48:51], s15 offen sc1\n"
    "buffer_load_dwordx4 v[112:115], v204, s[48:51], s15 offen offset:3840 sc1\n"
    "buffer_load_dwordx2 v[116:117], v205, s[48:51], s14 offen sc1\n"
    "buffer_load_dwordx2 v[118:119], v205, s[48:51], s16 offen sc1\n"
    "s_waitcnt vmcnt(0)\n"
    "v_max3_u32 v180, v100, v101, v101\n"
    "v_max3_u32 v180, v102, v103, v180\n"
    "v_max3_u32 v180, v104, v105, v180\n"
    "v_max3_u32 v180, v106, v107, v180\n"
    "v_max3_u32 v180, v108, v109, v180\n"
    "v_max3_u32 v180, v110, v111, v180\n"
    "v_max3_u32 v180, v112, v113, v180\n"
    "v_max3_u32 v180, v114, v115, v180\n"
    "v_max3_u32 v180, v116, v117, v180\n"
    "v_max3_u32 v180, v118, v119, v180\n"
    "v_cmp_eq_u32 vcc, -1, v180\n"
    "s_cbranch_vccz OK2h%=\n"
    "s_sub_u32 s3, s3, 1\n"
    "s_cbranch_scc0 FL2h%=\n"
    "s_branch OK2h%=\n"
    "FIX3ha%=:\n"
    "s_mov_b32 s3, 262144\n"
    "s_nop 7\n"
    "FL3ha%=:\n"
    "buffer_load_dwordx4 v[140:143], v204, s[48:51], s17 offen sc1\n"
    "buffer_load_dwordx4 v[144:147], v204, s[48:51], s17 offen offset:3840 sc1\n"
    "buffer_load_dwordx4 v[148:151], v204, s[48:51], s18 offen sc1\n"
    "buffer_load_dwordx4 v[152:155], v204, s[48:51], s18 offen offset:3840 sc1\n"
    "buffer_load_dwordx2 v[156:157], v205, s[48:51], s17 offen sc1\n"
    "buffer_load_dwordx2 v[158:159], v205, s[48:51], s19 offen sc1\n"
    "s_waitcnt vmcnt(0)\n"
    "v_max3_u32 v180, v140, v141, v141\n"
    "v_max3_u32 v180, v142, v143, v180\n"
    "v_max3_u32 v180, v144, v145, v180\n"
    "v_max3_u32 v180, v146, v147, v180\n"
    "v_max3_u32 v180, v148, v149, v180\n"
    "v_max3_u32 v180, v150, v151, v180\n"
    "v_max3_u32 v180, v152, v153, v180\n"
    "v_max3_u32 v180, v154, v155, v180\n"
    "v_max3_u32 v180, v156, v157, v180\n"
    "v_max3_u32 v180, v158, v159, v180\n"
    "v_cmp_eq_u32 vcc, -1, v180\n"
    "s_cbranch_vccz OK3ha%=\n"
    "s_sub_u32 s3, s3, 1\n"
    "s_cbranch_scc0 FL3ha%=\n"
    "s_branch OK3ha%=\n"
    "FIX3hb%=:\n"
    "s_mov_b32 s3, 262144\n"
    "s_nop 7\n"
    "FL3hb%=:\n"
    "buffer_load_dwordx4 v[140:143], v204, s[48:51], s17 offen sc1\n"
    "buffer_load_dwordx4 v[144:147], v204, s[48:51], s17 offen offset:3840 sc1\n"
    "buffer_load_dwordx4 v[148:151], v204, s[48:51], s18 offen sc1\n"
    "buffer_load_dwordx4 v[152:155], v204, s[48:51], s18 offen offset:3840 sc1\n"
    "buffer_load_dwordx2 v[156:157], v205, s[48:51], s17 offen sc1\n"
    "buffer_load_dwordx2 v[158:159], v205, s[48:51], s19 offen sc1\n"
    "s_waitcnt vmcnt(0)\n"
    "v_max3_u32 v180, v140, v141, v141\n"
    "v_max3_u32 v180, v142, v143, v180\n"
    "v_max3_u32 v180, v144, v145, v180\n"
    "v_max3_u32 v180, v146, v147, v180\n"
    "v_max3_u32 v180, v148, v149, v180\n"
    "v_max3_u32 v180, v150, v151, v180\n"
    "v_max3_u32 v180, v152, v153, v180\n"
    "v_max3_u32 v180, v154, v155, v180\n"
    "v_max3_u32 v180, v156, v157, v180\n"
    "v_max3_u32 v180, v158, v159, v180\n"
    "v_cmp_eq_u32 vcc, -1, v180\n"
    "s_cbranch_vccz OK3hb%=\n"
    "s_sub_u32 s3, s3, 1\n"
    "s_cbranch_scc0 FL3hb%=\n"
    "s_branch OK3hb%=\n"
    "FIX0f%=:\n"
    "s_mov_b32 s3, 262144\n"
    "s_nop 7\n"
    "FL0f%=:\n"
    "buffer_load_dwordx4 v[80:83], v204, s[48:51], s8 offen sc1\n"
    "buffer_load_dwordx4 v[84:87], v204, s[48:51], s8 offen offset:3840 sc1\n"
    "buffer_load_dwordx4 v[88:91], v204, s[48:51], s9 offen sc1\n"
    "buffer_load_dwordx4 v[92:95], v204, s[48:51], s9 offen offset:3840 sc1\n"
    "buffer_load_dwordx2 v[96:97], v205, s[48:51], s8 offen sc1\n"
    "buffer_load_dwordx2 v[98:99], v205, s[48:51], s10 offen sc1\n"
    "s_waitcnt vmcnt(0)\n"
    "v_max3_u32 v180, v80, v81, v81\n"
    "v_max3_u32 v180, v82, v83, v180\n"
    "v_max3_u32 v180, v84, v85, v180\n"
    "v_max3_u32 v180, v86, v87, v180\n"
    "v_max3_u32 v180, v88, v89, v180\n"
    "v_max3_u32 v180, v90, v91, v180\n"
    "v_max3_u32 v180, v92, v93, v180\n"
    "v_max3_u32 v180, v94, v95, v180\n"
    "v_max3_u32 v180, v96, v97, v180\n"
    "v_max3_u32 v180, v98, v99, v180\n"
    "v_cmp_eq_u32 vcc, -1, v180\n"
    "s_cbranch_vccz OK0f%=\n"
    "s_sub_u32 s3, s3, 1\n"
    "s_cbranch_scc0 FL0f%=\n"
    "s_branch OK0f%=\n"
    "FIX1f%=:\n"
    "s_mov_b32 s3, 262144\n"
    "s_nop 7\n"
    "FL1f%=:\n"
    "buffer_load_dwordx4 v[120:123], v204, s[48:51], s11 offen sc1\n"
    "buffer_load_dwordx4 v[124:127], v204, s[48:51], s11 offen offset:3840 sc1\n"
    "buffer_load_dwordx4 v[128:131], v204, s[48:51], s12 offen sc1\n"
    "buffer_load_dwordx4 v[132:135], v204, s[48:51], s12 offen offset:3840 sc1\n"
    "buffer_load_dwordx2 v[136:137], v205, s[48:51], s11 offen sc1\n"
    "buffer_load_dwordx2 v[138:139], v205, s[48:51], s13 offen sc1\n"
    "s_waitcnt vmcnt(0)\n"
    "v_max3_u32 v180, v120, v121, v121\n"
    "v_max3_u32 v180, v122, v123, v180\n"
    "v_max3_u32 v180, v124, v125, v180\n"
    "v_max3_u32 v180, v126, v127, v180\n"
    "v_max3_u32 v180, v128, v129, v180\n"
    "v_max3_u32 v180, v130, v131, v180\n"
    "v_max3_u32 v180, v132, v133, v180\n"
    "v_max3_u32 v180, v134, v135, v180\n"
    "v_max3_u32 v180, v136, v137, v180\n"
    "v_max3_u32 v180, v138, v139, v180\n"
    "v_cmp_eq_u32 vcc, -1, v180\n"
    "s_cbranch_vccz OK1f%=\n"
    "s_sub_u32 s3, s3, 1\n"
    "s_cbranch_scc0 FL1f%=\n"
    "s_branch OK1f%=\n"
    "FIX3fa%=:\n"
    "s_mov_b32 s3, 262144\n"
    "s_nop 7\n"
    "FL3fa%=:\n"
    "buffer_load_dwordx4 v[140:143], v204, s[48:51], s17 offen sc1\n"
    "buffer_load_dwordx4 v[144:147], v204, s[48:51], s17 offen offset:3840 sc1\n"
    "buffer_load_dwordx4 v[148:151], v204, s[48:51], s18 offen sc1\n"
    "buffer_load_dwordx4 v[152:155], v204, s[48:51], s18 offen offset:3840 sc1\n"
    "buffer_load_dwordx2 v[156:157], v205, s[48:51], s17 offen sc1\n"
    "buffer_load_dwordx2 v[158:159], v205, s[48:51], s19 offen sc1\n"
    "s_waitcnt vmcnt(0)\n"
    "v_max3_u32 v180, v140, v141, v141\n"
    "v_max3_u32 v180, v142, v143, v180\n"
    "v_max3_u32 v180, v144, v145, v180\n"
    "v_max3_u32 v180, v146, v147, v180\n"
    "v_max3_u32 v180, v148, v149, v180\n"
    "v_max3_u32 v180, v150, v151, v180\n"
    "v_max3_u32 v180, v152, v153, v180\n"
    "v_max3_u32 v180, v154, v155, v180\n"
    "v_max3_u32 v180, v156, v157, v180\n"
    "v_max3_u32 v180, v158, v159, v180\n"
    "v_cmp_eq_u32 vcc, -1, v180\n"
    "s_cbranch_vccz OK3fa%=\n"
    "s_sub_u32 s3, s3, 1\n"
    "s_cbranch_scc0 FL3fa%=\n"
    "s_branch OK3fa%=\n"
    "FIX3fb%=:\n"
    "s_mov_b32 s3, 262144\n"
    "s_nop 7\n"
    "FL3fb%=:\n"
    "buffer_load_dwordx4 v[140:143], v204, s[48:51], s17 offen sc1\n"
    "buffer_load_dwordx4 v[144:147], v204, s[48:51], s17 offen offset:3840 sc1\n"
    "buffer_load_dwordx4 v[148:151], v204, s[48:51], s18 offen sc1\n"
    "buffer_load_dwordx4 v[152:155], v204, s[48:51], s18 offen offset:3840 sc1\n"
    "buffer_load_dwordx2 v[156:157], v205, s[48:51], s17 offen sc1\n"
    "buffer_load_dwordx2 v[158:159], v205, s[48:51], s19 offen sc1\n"
    "s_waitcnt vmcnt(0)\n"
    "v_max3_u32 v180, v140, v141, v141\n"
    "v_max3_u32 v180, v142, v143, v180\n"
    "v_max3_u32 v180, v144, v145, v180\n"
    "v_max3_u32 v180, v146, v147, v180\n"
    "v_max3_u32 v180, v148, v149, v180\n"
    "v_max3_u32 v180, v150, v151, v180\n"
    "v_max3_u32 v180, v152, v153, v180\n"
    "v_max3_u32 v180, v154, v155, v180\n"
    "v_max3_u32 v180, v156, v157, v180\n"
    "v_max3_u32 v180, v158, v159, v180\n"
    "v_cmp_eq_u32 vcc, -1, v180\n"
    "s_cbranch_vccz OK3fb%=\n"
    "s_sub_u32 s3, s3, 1\n"
    "s_cbranch_scc0 FL3fb%=\n"
    "s_branch OK3fb%=\n"
    "ELATE%=:\n"
    "s_waitcnt vmcnt(4)\n"
    "ds_add_u32 v181, v180\n"
    "s_add_u32 s2, s34, 65\n"
    "UE%=:\n"
    "s_memrealtime s[0:1]\n"
    "s_waitcnt lgkmcnt(0)\n"
    "s_sub_u32 s0, s0, s2\n"
    "s_cmp_lt_i32 s0, 0\n"
    "s_cbranch_scc1 UE%=\n"
    "buffer_load_dwordx4 v[80:83], v204, s[48:51], s8 offen sc1\n"
    "buffer_load_dwordx4 v[84:87], v204, s[48:51], s8 offen offset:3840 sc1\n"
    "buffer_load_dwordx4 v[88:91], v204, s[48:51], s9 offen sc1\n"
    "buffer_load_dwordx4 v[92:95], v204, s[48:51], s9 offen offset:3840 sc1\n"
    "buffer_load_dwordx2 v[96:97], v205, s[48:51], s8 offen sc1\n"
    "buffer_load_dwordx2 v[98:99], v205, s[48:51], s10 offen sc1\n"
    "s_waitcnt vmcnt(0)\n"
    "s_branch EBACK%=\n"
    "YPRE%=:\n"
    "s_cmp_lg_u32 s69, 3\n"
    "s_cbranch_scc1 YB0%=\n"
    "v_mbcnt_lo_u32_b32 v192, -1, 0\n"
    "v_mbcnt_hi_u32_b32 v192, -1, v192\n"
    "v_lshrrev_b32 v194, 5, v192\n"
    "v_mul_u32_u24 v194, 10240, v194\n"
    "v_and_b32 v195, 24, v192\n"
    "v_and_b32 v196, 7, v192\n"
    "v_lshlrev_b32 v197, 5, v195\n"
    "v_lshl_add_u32 v197, v196, 4, v197\n"
    "s_lshl_b32 s15, s78, 3\n"
    "v_add_u32 v197, s15, v197\n"
    "v_add_u32 v194, v194, v197\n"
    "s_cmp_lt_u32 s22, 96\n"
    "s_cselect_b32 s27, 0, 128\n"
    "s_cselect_b32 s28, s22, s23\n"
    "s_cselect_b32 s29, s20, s21\n"
    "s_cselect_b32 s24, s23, -1\n"
    "v_and_b32 v196, 6, v192\n"
    "v_lshl_add_u32 v195, v196, 4, v195\n"
    "v_and_b32 v196, 1, v192\n"
    "v_lshl_add_u32 v195, v196, 2, v195\n"
    "v_cmp_gt_u32 vcc, 32, v192\n"
    "s_nop 1\n"
    "v_cndmask_b32 v192, 0, 1.0, vcc\n"
    "s_lshl_b32 s15, s28, 3\n"
    "s_and_b32 s15, s15, 112\n"
    "v_xor_b32 v193, s15, v195\n"
    "s_lshl_b32 s15, s28, 7\n"
    "s_add_u32 s15, s15, 133120\n"
    "v_add_u32 v193, s15, v193\n"
    "s_bitcmp1_b32 s29, 31\n"
    "s_cselect_b32 s15, 0x80000000, -1\n"
    "s_xor_b32 s15, s15, s29\n"
    "s_and_b32 s15, s15, 0xffff0000\n"
    "v_mov_b32 v196, s15\n"
    "v_mul_f32 v196, 0xbfb8aa3b, v196\n"
    "v_exp_f32 v196, v196\n"
    "s_nop 0\n"
    "v_add_f32 v196, 1.0, v196\n"
    "v_rcp_f32 v196, v196\n"
    "s_nop 0\n"
    "v_cvt_pk_bf16_f32 v196, v196, v196\n"
    "v_lshlrev_b32 v196, 16, v196\n"
    "s_mov_b32 s35, 262144\n"
    "s_branch YB0%=\n"
    "YJOB%=:\n"
    "s_cmp_lg_u32 s69, 3\n"
    "s_cbranch_scc1 YB1%=\n"
    "s_add_u32 s15, s27, 0\n"
    "buffer_load_dwordx4 v[80:83], v194, s[64:67], s15 offen sc1\n"
    "s_add_u32 s15, s27, 20480\n"
    "buffer_load_dwordx4 v[84:87], v194, s[64:67], s15 offen sc1\n"
    "s_add_u32 s15, s27, 40960\n"
    "buffer_load_dwordx4 v[88:91], v194, s[64:67], s15 offen sc1\n"
    "s_add_u32 s15, s27, 61440\n"
    "buffer_load_dwordx4 v[92:95], v194, s[64:67], s15 offen sc1\n"
    "s_add_u32 s15, s27, 81920\n"
    "buffer_load_dwordx4 v[96:99], v194, s[64:67], s15 offen sc1\n"
    "s_add_u32 s15, s27, 102400\n"
    "buffer_load_dwordx4 v[128:131], v194, s[64:67], s15 offen sc1\n"
    "s_add_u32 s15, s27, 122880\n"
    "buffer_load_dwordx4 v[132:135], v194, s[64:67], s15 offen sc1\n"
    "s_add_u32 s15, s27, 143360\n"
    "buffer_load_dwordx4 v[136:139], v194, s[64:67], s15 offen sc1\n"
    "s_add_u32 s15, s27, 163840\n"
    "buffer_load_dwordx4 v[184:187], v194, s[64:67], s15 offen sc1\n"
    "s_add_u32 s15, s27, 184320\n"
    "buffer_load_dwordx4 v[236:239], v194, s[64:67], s15 offen sc1\n"
    "s_add_u32 s15, s27, 204800\n"
    "buffer_load_dwordx4 v[240:243], v194, s[64:67], s15 offen sc1\n"
    "s_add_u32 s15, s27, 225280\n"
    "buffer_load_dwordx4 v[244:247], v194, s[64:67], s15 offen sc1\n"
    "s_add_u32 s15, s27, 245760\n"
    "buffer_load_dwordx4 v[248:251], v194, s[64:67], s15 offen sc1\n"
    "s_add_u32 s15, s27, 266240\n"
    "buffer_load_dwordx4 v[252:255], v197, s[64:67], s15 offen sc1\n"
    "s_branch YB1%=\n"
    "YW%=:\n"
    "s_cmp_lg_u32 s69, 3\n"
    "s_cbranch_scc1 YB2%=\n"
    "YS%=:\n"
    "ds_read_b32 v201, v193\n"
    "s_waitcnt vmcnt(6)\n"
    "v_pk_mul_f32 v[198:199], v[252:253], v[192:193] op_sel_hi:[1,0]\n"
    "v_xor_b32 v200, s68, v254\n"
    "v_pk_add_f32 v[198:199], v[198:199], v[80:81]\n"
    "v_bitop3_b32 v200, v200, v82, s68 bitop3:0xf6\n"
    "v_pk_add_f32 v[198:199], v[198:199], v[84:85]\n"
    "v_bitop3_b32 v200, v200, v86, s68 bitop3:0xf6\n"
    "v_pk_add_f32 v[198:199], v[198:199], v[88:89]\n"
    "v_bitop3_b32 v200, v200, v90, s68 bitop3:0xf6\n"
    "v_pk_add_f32 v[198:199], v[198:199], v[92:93]\n"
    "v_bitop3_b32 v200, v200, v94, s68 bitop3:0xf6\n"
    "v_pk_add_f32 v[198:199], v[198:199], v[96:97]\n"
    "v_bitop3_b32 v200, v200, v98, s68 bitop3:0xf6\n"
    "v_pk_add_f32 v[198:199], v[198:199], v[128:129]\n"
    "v_bitop3_b32 v200, v200, v130, s68 bitop3:0xf6\n"
    "v_pk_add_f32 v[198:199], v[198:199], v[132:133]\n"
    "v_bitop3_b32 v200, v200, v134, s68 bitop3:0xf6\n"
    "v_pk_add_f32 v[198:199], v[198:199], v[136:137]\n"
    "v_bitop3_b32 v200, v200, v138, s68 bitop3:0xf6\n"
    "v_pk_add_f32 v[198:199], v[198:199], v[184:185]\n"
    "v_bitop3_b32 v200, v200, v186, s68 bitop3:0xf6\n"
    "v_pk_add_f32 v[198:199], v[198:199], v[236:237]\n"
    "v_bitop3_b32 v200, v200, v238, s68 bitop3:0xf6\n"
    "v_pk_add_f32 v[198:199], v[198:199], v[240:241]\n"
    "v_bitop3_b32 v200, v200, v242, s68 bitop3:0xf6\n"
    "v_pk_add_f32 v[198:199], v[198:199], v[244:245]\n"
    "v_bitop3_b32 v200, v200, v246, s68 bitop3:0xf6\n"
    "v_pk_add_f32 v[198:199], v[198:199], v[248:249]\n"
    "v_bitop3_b32 v200, v200, v250, s68 bitop3:0xf6\n"
    "s_waitcnt lgkmcnt(0)\n"
    "v_cmp_ne_u32 vcc, 0, v200\n"
    "s_cbranch_vccz YK%=\n"
    "s_sleep 4\n"
    "s_sub_u32 s35, s35, 1\n"
    "s_cbranch_scc0 YL%=\n"
    "YK%=:\n"
    "v_mov_b32 v202, v198\n"
    "v_mov_b32 v203, v199\n"
    "s_nop 0\n"
    "v_permlane32_swap_b32 v198, v202\n"
    "v_permlane32_swap_b32 v199, v203\n"
    "v_pk_add_f32 v[198:199], v[198:199], v[202:203]\n"
    "v_cvt_pk_bf16_f32 v198, v198, v199\n"
    "v_and_b32 v199, 0xffff0000, v198\n"
    "v_lshlrev_b32 v198, 16, v198\n"
    "v_pk_mul_f32 v[198:199], v[198:199], v[196:197] op_sel_hi:[1,0]\n"
    "v_cvt_pk_bf16_f32 v198, v198, v199\n"
    "v_and_b32 v199, 0xffff0000, v198\n"
    "v_lshlrev_b32 v198, 16, v198\n"
    "v_lshlrev_b32 v200, 16, v201\n"
    "v_and_b32 v201, 0xffff0000, v201\n"
    "v_pk_add_f32 v[198:199], v[198:199], v[200:201]\n"
    "v_cvt_pk_bf16_f32 v198, v198, v199\n"
    "ds_write_b32 v193, v198\n"
    "s_cmp_ge_u32 s24, 96\n"
    "s_cbranch_scc1 YD%=\n"
    "s_movk_i32 s27, 128\n"
    "s_mov_b32 s28, s24\n"
    "s_mov_b32 s29, s21\n"
    "s_mov_b32 s24, -1\n"
    "s_lshl_b32 s15, s28, 3\n"
    "s_and_b32 s15, s15, 112\n"
    "v_xor_b32 v193, s15, v195\n"
    "s_lshl_b32 s15, s28, 7\n"
    "s_add_u32 s15, s15, 133120\n"
    "v_add_u32 v193, s15, v193\n"
    "s_bitcmp1_b32 s29, 31\n"
    "s_cselect_b32 s15, 0x80000000, -1\n"
    "s_xor_b32 s15, s15, s29\n"
    "s_and_b32 s15, s15, 0xffff0000\n"
    "v_mov_b32 v196, s15\n"
    "v_mul_f32 v196, 0xbfb8aa3b, v196\n"
    "v_exp_f32 v196, v196\n"
    "s_nop 0\n"
    "v_add_f32 v196, 1.0, v196\n"
    "v_rcp_f32 v196, v196\n"
    "s_nop 0\n"
    "v_cvt_pk_bf16_f32 v196, v196, v196\n"
    "v_lshlrev_b32 v196, 16, v196\n"
    "YL%=:\n"
    "s_add_u32 s15, s27, 0\n"
    "buffer_load_dwordx4 v[80:83], v194, s[64:67], s15 offen sc1\n"
    "s_add_u32 s15, s27, 20480\n"
    "buffer_load_dwordx4 v[84:87], v194, s[64:67], s15 offen sc1\n"
    "s_add_u32 s15, s27, 40960\n"
    "buffer_load_dwordx4 v[88:91], v194, s[64:67], s15 offen sc1\n"
    "s_add_u32 s15, s27, 61440\n"
    "buffer_load_dwordx4 v[92:95], v194, s[64:67], s15 offen sc1\n"
    "s_add_u32 s15, s27, 81920\n"
    "buffer_load_dwordx4 v[96:99], v194, s[64:67], s15 offen sc1\n"
    "s_add_u32 s15, s27, 102400\n"
    "buffer_load_dwordx4 v[128:131], v194, s[64:67], s15 offen sc1\n"
    "s_add_u32 s15, s27, 122880\n"
    "buffer_load_dwordx4 v[132:135], v194, s[64:67], s15 offen sc1\n"
    "s_add_u32 s15, s27, 143360\n"
    "buffer_load_dwordx4 v[136:139], v194, s[64:67], s15 offen sc1\n"
    "s_add_u32 s15, s27, 163840\n"
    "buffer_load_dwordx4 v[184:187], v194, s[64:67], s15 offen sc1\n"
    "s_add_u32 s15, s27, 184320\n"
    "buffer_load_dwordx4 v[236:239], v194, s[64:67], s15 offen sc1\n"
    "s_add_u32 s15, s27, 204800\n"
    "buffer_load_dwordx4 v[240:243], v194, s[64:67], s15 offen sc1\n"
    "s_add_u32 s15, s27, 225280\n"
    "buffer_load_dwordx4 v[244:247], v194, s[64:67], s15 offen sc1\n"
    "s_add_u32 s15, s27, 245760\n"
    "buffer_load_dwordx4 v[248:251], v194, s[64:67], s15 offen sc1\n"
    "s_add_u32 s15, s27, 266240\n"
    "buffer_load_dwordx4 v[252:255], v197, s[64:67], s15 offen sc1\n"
    "s_waitcnt vmcnt(0)\n"
    "s_branch YS%=\n"
    "YD%=:\n"
    "v_mov_b32 v198, 156696\n"
    "ds_write_b32 v198, v198\n"
    "s_branch YB2%=\n"
    "YOWN%=:\n"
    "s_lshr_b32 s22, s22, 4\n"
    "s_lshr_b32 s23, s23, 4\n"
    "s_cmp_eq_u32 s22, s83\n"
    "s_cbranch_scc1 YOW2%=\n"
    "s_cmp_lg_u32 s23, s83\n"
    "s_cbranch_scc1 YBK%=\n"
    "YOW2%=:\n"
    "s_mov_b32 s3, 16777216\n"
    "v_mov_b32 v181, 156696\n"
    "YWL%=:\n"
    "ds_read_b32 v180, v181\n"
    "s_waitcnt lgkmcnt(0)\n"
    "v_cmp_ne_u32 vcc, 0, v180\n"
    "s_cbranch_vccnz YWLD%=\n"
    "s_sleep 1\n"
    "s_sub_u32 s3, s3, 1\n"
    "s_cbranch_scc0 YWL%=\n"
    "YWLD%=:\n"
    "s_cmp_ge_u32 s69, 4\n"
    "s_cbranch_scc1 YC2%=\n"
    "ds_read_b32 v176, v234\n"
    "ds_read_b64 v[104:105], v228\n"
    "ds_read_b64 v[106:107], v229\n"
    "ds_read_b64 v[108:109], v230\n"
    "ds_read_b64 v[110:111], v231\n"
    "s_waitcnt lgkmcnt(4)\n"
    "v_sub_f32 v178, 1.0, v176\n"
    "s_waitcnt lgkmcnt(3)\n"
    "v_lshlrev_b32 v208, 16, v104\n"
    "v_and_b32 v209, 0xffff0000, v104\n"
    "v_pk_mul_f32 v[208:209], v[208:209], v[178:179] op_sel_hi:[1,0]\n"
    "v_lshlrev_b32 v210, 16, v105\n"
    "v_and_b32 v211, 0xffff0000, v105\n"
    "v_pk_mul_f32 v[210:211], v[210:211], v[178:179] op_sel_hi:[1,0]\n"
    "s_waitcnt lgkmcnt(2)\n"
    "v_lshlrev_b32 v212, 16, v106\n"
    "v_and_b32 v213, 0xffff0000, v106\n"
    "v_pk_mul_f32 v[212:213], v[212:213], v[178:179] op_sel_hi:[1,0]\n"
    "v_lshlrev_b32 v214, 16, v107\n"
    "v_and_b32 v215, 0xffff0000, v107\n"
    "v_pk_mul_f32 v[214:215], v[214:215], v[178:179] op_sel_hi:[1,0]\n"
    "s_waitcnt lgkmcnt(1)\n"
    "v_lshlrev_b32 v216, 16, v108\n"
    "v_and_b32 v217, 0xffff0000, v108\n"
    "v_pk_mul_f32 v[216:217], v[216:217], v[178:179] op_sel_hi:[1,0]\n"
    "v_lshlrev_b32 v218, 16, v109\n"
    "v_and_b32 v219, 0xffff0000, v109\n"
    "v_pk_mul_f32 v[218:219], v[218:219], v[178:179] op_sel_hi:[1,0]\n"
    "s_waitcnt lgkmcnt(0)\n"
    "v_lshlrev_b32 v220, 16, v110\n"
    "v_and_b32 v221, 0xffff0000, v110\n"
    "v_pk_mul_f32 v[220:221], v[220:221], v[178:179] op_sel_hi:[1,0]\n"
    "v_lshlrev_b32 v222, 16, v111\n"
    "v_and_b32 v223, 0xffff0000, v111\n"
    "v_pk_mul_f32 v[222:223], v[222:223], v[178:179] op_sel_hi:[1,0]\n"
    "s_branch YBF%=\n"
    "YC2%=:\n"
    "ds_read_b32 v176, v234\n"
    "ds_read_b64 v[104:105], v228\n"
    "ds_read_b64 v[106:107], v229\n"
    "s_waitcnt lgkmcnt(2)\n"
    "v_sub_f32 v178, 1.0, v176\n"
    "s_waitcnt lgkmcnt(1)\n"
    "v_lshlrev_b32 v208, 16, v104\n"
    "v_and_b32 v209, 0xffff0000, v104\n"
    "v_pk_mul_f32 v[208:209], v[208:209], v[178:179] op_sel_hi:[1,0]\n"
    "v_lshlrev_b32 v210, 16, v105\n"
    "v_and_b32 v211, 0xffff0000, v105\n"
    "v_pk_mul_f32 v[210:211], v[210:211], v[178:179] op_sel_hi:[1,0]\n"
    "s_waitcnt lgkmcnt(0)\n"
    "v_lshlrev_b32 v212, 16, v106\n"
    "v_and_b32 v213, 0xffff0000, v106\n"
    "v_pk_mul_f32 v[212:213], v[212:213], v[178:179] op_sel_hi:[1,0]\n"
    "v_lshlrev_b32 v214, 16, v107\n"
    "v_and_b32 v215, 0xffff0000, v107\n"
    "v_pk_mul_f32 v[214:215], v[214:215], v[178:179] op_sel_hi:[1,0]\n"
    "YBK%=:\n"
    "s_cmp_ge_u32 s69, 4\n"
    "s_cbranch_scc1 YBH%=\n"
    "s_branch YBF%=\n"
    ::"{v[192:207]}"(p0), "{v[208:223]}"(p1), "{v[224:239]}"(p2), "{v[240:255]}"(p3), "{v[176:191]}"(r0), "{s[68:83]}"(uc), "{s84}"(C_H + gb * 128), "{s85}"(b), "{s[90:91]}"(kp0)
    : "memory", "s99");
  __builtin_unreachable();
}

extern "C" void kernel_launch(void* const* d_in, const int* in_sizes, int n_in, void* d_out, int out_size, void* d_ws,
                              size_t ws_size, hipStream_t stream) {
  kf<<<256, 512, 0, stream>>>((const u16*)d_in[0], (const u16*)d_in[1], (const u16*)d_in[2], (const u16*)d_in[4],
                              (const u16*)d_in[8], (const u16*)d_in[5], (const u16*)d_in[6], (const u16*)d_in[7],
                              (char*)d_ws, (u16*)d_out);
}
```

```cpp
#include <hip/hip_runtime.h>
#include <stdint.h>

typedef unsigned short u16;
typedef unsigned long long u64;
typedef __attribute__((ext_vector_type(8))) __bf16 bf16x8;
typedef __attribute__((ext_vector_type(2))) __bf16 bf16x2;
typedef __attribute__((ext_vector_type(4))) short s16x4;
typedef __attribute__((ext_vector_type(4))) float f32x4;
typedef __attribute__((ext_vector_type(2))) float f32x2;
typedef __attribute__((ext_vector_type(4))) unsigned u32x4;
typedef __attribute__((ext_vector_type(2))) unsigned u32x2;
typedef __attribute__((ext_vector_type(8))) unsigned u32x8;
typedef __attribute__((ext_vector_type(16))) unsigned u32x16;
#define GAS __attribute__((address_space(1)))
#define LAS __attribute__((address_space(3)))
typedef GAS unsigned gu32;

#define S_LEN 2048
#define DM 640
#define NQKV 1920
#define FF 1728

#define WS_O 0
#define WS_O_B 2703360
#define WS_H WS_O_B
#define INB 10240
#define WS_H_B (220 * INB)
#define WS_C (WS_H + WS_H_B)
#define C_K 0
#define C_H 56320
#define C_R 84480
#define C_X (C_R + 256)
#define C_F (C_X + 5120)
#define C_A (C_F + 23552)
#define C_E (C_A + 32768)
#define C_B (C_E + 256)
static_assert(C_E == 146176, "(used in the assembly)");
#define WS_YP (WS_C + C_B)
#define TAG_STEP 0x9E3779B9u
#define SENT 0xFFFFFFFFu
#define SPIN_CAP (1u << 18)
#define OOB 0x7FFF0000
#define RLX_AGENT __ATOMIC_RELAXED, __HIP_MEMORY_SCOPE_AGENT

__device__ __forceinline__ float bflo(unsigned u) { return __uint_as_float(u << 16); }
__device__ __forceinline__ float bfhi(unsigned u) { return __uint_as_float(u & 0xFFFF0000u); }
__device__ __forceinline__ unsigned pk2(float a, float b) {
  f32x2 v = {a, b};
  return __builtin_bit_cast(unsigned, __builtin_convertvector(v, bf16x2));
}
__device__ __forceinline__ float rbf(float f) { return bflo(pk2(f, f)); }
__device__ __forceinline__ unsigned perm_lo(unsigned hi_src, unsigned lo_src) {
  return __builtin_amdgcn_perm(hi_src, lo_src, 0x05040100u);
}
__device__ __forceinline__ unsigned perm_hi(unsigned hi_src, unsigned lo_src) {
  return __builtin_amdgcn_perm(hi_src, lo_src, 0x07060302u);
}
__device__ __forceinline__ f32x4 mfma16(u32x4 a, u32x4 b, f32x4 c) {
  return __builtin_amdgcn_mfma_f32_16x16x32_bf16(__builtin_bit_cast(bf16x8, a), __builtin_bit_cast(bf16x8, b), c, 0, 0, 0);
}
__device__ __forceinline__ u32x2 tr_read(const char* p) {
  return __builtin_bit_cast(u32x2, __builtin_amdgcn_ds_read_tr16_b64_v4i16((LAS s16x4*)(p)));
}
#define BARRIER() asm volatile("s_barrier" ::: "memory")
#define WAITV(n) asm volatile("s_waitcnt vmcnt(" #n ")" ::: "memory")
#define WAITV_(n) asm volatile("s_waitcnt vmcnt(%0)" ::"i"(n) : "memory")
#define WAITL0() do { asm volatile("" ::: "memory"); __builtin_amdgcn_s_waitcnt(0xC07F); asm volatile("" ::: "memory"); } while (0)

__device__ __forceinline__ void top2_mrg(unsigned& k1, unsigned& k2, unsigned b1, unsigned b2) {
  k2 = max(max(k2, b2), min(k1, b1));
  k1 = max(k1, b1);
}
template <int CTRL>
__device__ __forceinline__ void top2_dpp(unsigned& k1, unsigned& k2) {
  const unsigned b1 = (unsigned)__builtin_amdgcn_update_dpp(0, (int)k1, CTRL, 0xF, 0xF, true);
  const unsigned b2 = (unsigned)__builtin_amdgcn_update_dpp(0, (int)k2, CTRL, 0xF, 0xF, true);
  top2_mrg(k1, k2, b1, b2);
}
__device__ __forceinline__ void top2_all(unsigned& k1, unsigned& k2) {
  top2_dpp<0xB1>(k1, k2);
  top2_dpp<0x4E>(k1, k2);
  top2_dpp<0x141>(k1, k2);
  top2_dpp<0x140>(k1, k2);
  {
    auto a = __builtin_amdgcn_permlane16_swap(k1, k1, false, false);
    auto c = __builtin_amdgcn_permlane16_swap(k2, k2, false, false);
    k1 = a[0];
    k2 = c[0];
    top2_mrg(k1, k2, a[1], c[1]);
  }
  {
    auto a = __builtin_amdgcn_permlane32_swap(k1, k1, false, false);
    auto c = __builtin_amdgcn_permlane32_swap(k2, k2, false, false);
    k1 = a[0];
    k2 = c[0];
    top2_mrg(k1, k2, a[1], c[1]);
  }
}
__device__ __forceinline__ void touch_line(const char* p, unsigned lds_off) {
  asm volatile("s_mov_b32 m0, %1\n\ts_nop 0\n\tglobal_load_lds_dword %0, off" ::"v"(p), "s"(lds_off) : "memory");
}
__device__ __forceinline__ void dma16(unsigned voff, const void* sbase, unsigned lds_off) {
  asm volatile("s_mov_b32 m0, %2\n\ts_nop 0\n\tglobal_load_lds_dwordx4 %0, %1" ::"v"(voff), "s"(sbase), "s"(lds_off) : "memory");
}
__device__ __forceinline__ void dma16_sc1(unsigned voff, const void* sbase, unsigned lds_off) {
  asm volatile("s_mov_b32 m0, %2\n\ts_nop 0\n\tglobal_load_lds_dwordx4 %0, %1 sc1" ::"v"(voff), "s"(sbase), "s"(lds_off) : "memory");
}
__device__ __forceinline__ void bdma16(unsigned voff, u32x4 srd, unsigned soff, unsigned lds_off) {
  asm volatile("s_mov_b32 m0, %3\n\ts_nop 0\n\tbuffer_load_dwordx4 %0, %1, %2 offen lds" ::"v"(voff), "s"(srd), "s"(soff), "s"(lds_off) : "memory");
}
__device__ __forceinline__ u32x4 make_srd(const void* p, unsigned bytes) {
  return (u32x4){(unsigned)(uintptr_t)p, (unsigned)((uintptr_t)p >> 32) & 0xFFFFu, bytes, 0x00020000u};
}
template <class T> __device__ __forceinline__ T* uni(T* p) {
  const unsigned lo = __builtin_amdgcn_readfirstlane((unsigned)(uintptr_t)p), hi = __builtin_amdgcn_readfirstlane((unsigned)((uintptr_t)p >> 32));
  return (T*)(((u64)hi << 32) | lo);
}
#define RSRC(p, bytes) __builtin_amdgcn_make_buffer_rsrc((void*)(p), 0, bytes, 0x00020000)
__device__ __forceinline__ float key_val(unsigned key) {
  unsigned u = key >> 16;
  u ^= (u & 0x8000u) ? 0x8000u : 0xFFFFu;
  return bflo(u);
}
__device__ __forceinline__ float fmx(float a, float b) { return __builtin_elementwise_maximum(a, b); }
#define SB() __builtin_amdgcn_sched_barrier(0)
__device__ __forceinline__ float dot2(unsigned a, unsigned b, float c) {
  return __builtin_amdgcn_fdot2_f32_bf16(__builtin_bit_cast(bf16x2, a), __builtin_bit_cast(bf16x2, b), c, false);
}
template <int CTRL>
__device__ __forceinline__ float dpp_add(float v) {
  return v + __int_as_float(__builtin_amdgcn_update_dpp(0, __float_as_int(v), CTRL, 0xF, 0xF, true));
}
__device__ __forceinline__ float row_sum(float v) {
  v = dpp_add<0xB1>(v);
  v = dpp_add<0x4E>(v);
  v = dpp_add<0x141>(v);
  return dpp_add<0x140>(v);
}
__device__ __forceinline__ unsigned max4(u32x4 v) { return max(max(v[0], v[1]), max(v[2], v[3])); }
template <int N> struct IC { static constexpr int value = N; };
#define PIN(v) asm volatile("" : "+v"(v))
#define PINS(v) do { v = __builtin_amdgcn_readfirstlane(v); asm volatile("" : "+s"(v)); } while (0)
#define AI __attribute__((always_inline))

#define BUF(q) ((q) % 3 == 2 ? 0 : (q) % 3 == 1 ? 51200 : 112640)
#define RS 168
#define TS (16 * RS)
#define SO (104 * RS)
#define L_WO 51200
#define L_TAB 88064
#define L_XT 133120
#define L_EX 147456
#define L_MISC 155648
#define L_SMP L_MISC
#define L_Z (L_MISC + 1024)
#define L_FL (L_Z + 8)
#define L_Y (L_Z + 64)
#define L_SM2 (L_MISC + 2048)
#define L_TOUCH (L_MISC + 3072)
#define L_TOTAL (L_MISC + 3584)
#define CODE_LINES 266
#define FOFF 0
#define CT64 1
#define MK_D 20
#define FF_D 12
#define RW_T 540
#define RW_P 50
#define LD_T 270
#define LD_S 2
#define KP_T 850
#define POS_OF(hl) ((int)((0xBA9F73E62D51C408ull >> ((hl) * 4)) & 15))
#define HL_OF(pos) ((int)((0xC963FED0B852A741ull >> ((pos) * 4)) & 15))

__global__ __launch_bounds__(512, 2) void kf(const u16*, const u16*, const u16*, const u16*, const u16*, const u16*, const u16*, const u16*, char*, u16*) {
  __shared__ __attribute__((aligned(16))) char smem[L_TOTAL];
  typedef const __attribute__((address_space(4))) u64* karg_t;
  const karg_t kp0 = (karg_t)__builtin_amdgcn_kernarg_segment_ptr();
  u64 pc;
  asm volatile("s_load_dword s100, %1, 0x0\n\ts_load_dword s101, %1, 0x40\n\ts_getpc_b64 %0" : "=s"(pc) : "s"(kp0) : "s100", "s101");
  const u16 *x, *wqkv, *wo, *w_r, *gate_w, *w_gate, *w_up, *w_down;
  char* ws;
  u16* out;
  auto args = [&]() AI {
    SB();
    karg_t kp = kp0;
    asm volatile("" : "+s"(kp));
    x = (const u16*)kp[0]; wqkv = (const u16*)kp[1]; wo = (const u16*)kp[2]; w_r = (const u16*)kp[3]; gate_w = (const u16*)kp[4];
    w_gate = (const u16*)kp[5]; w_up = (const u16*)kp[6]; w_down = (const u16*)kp[7]; ws = (char*)kp[8]; out = (u16*)kp[9];
  };
  const int tid = threadIdx.x, b = blockIdx.x;
  {
    const int ln = ((b >> 3) & 7) + 8 * (tid >> 1);
    if (ln < CODE_LINES)
      touch_line((const char*)((pc & ~255ull) + ln * 128 + (tid & 1) * 64), (unsigned)(uintptr_t)(LAS char*)smem + L_TOUCH);
  }
  unsigned tag;
  auto get_tag = [&]() AI {
    asm volatile("s_load_dword %0, %1, 0x0\n\ts_waitcnt lgkmcnt(0)" : "=s"(tag) : "s"(ws + WS_C + C_E) : "memory");
    tag += TAG_STEP;
  };
  const int lane = tid & 63;
  const int wave = __builtin_amdgcn_readfirstlane(tid >> 6);
  const int xcd = b & 7, jj = b >> 3;
  const int c15 = lane & 15, g4 = lane >> 4, q4 = c15 >> 2, p4 = lane & 3;
  const int nb = 30 - (xcd & 1) * 5, j5 = (jj * 205) >> 10;
  const int par = (xcd >> 1) & 1, hf = xcd >> 2;
  const int gi = j5 == 5 ? 10 : j5 + (xcd & 1) * 5, c = jj - j5 * 5;
  const int rg = par * 11 + gi, cg = hf * 5 + c, gb = rg * 10 + cg;
  const int tb = 96 * gi;
  const unsigned lds0 = (unsigned)(uintptr_t)(LAS char*)smem;
#define rc_ RSRC(ws + WS_C, C_B)

  if (__builtin_expect(jj >= nb, 0)) {
    const int f = (jj - nb) * 4 + (xcd >> 1) + ((xcd & 1) ? FOFF : 28 - FOFF * 7 / 2);
    args();
    if (f > 27) {
      const int mi = f - 28;
      if (MK_D) __builtin_amdgcn_s_sleep(MK_D);
      const __amdgpu_buffer_rsrc_t r = RSRC(ws + WS_O, WS_O_B);
      const u32x4 sent = {SENT, SENT, SENT, SENT};
      const int l3 = lane < 8 ? lane * 16 + 3072 : OOB, l4 = lane < 48 ? lane * 16 + 3072 : OOB, l0 = lane < 40 ? lane * 16 : OOB;
#pragma unroll 1
      for (int k = mi * 66 + wave; k < mi * 66 + 66; k += 8) {
        const int k12 = (k * 1366) >> 14, so = (k + k12 * 4) * 3840 + 640;
#pragma unroll
        for (int j = 0; j < 3; ++j) __builtin_amdgcn_raw_buffer_store_b128(sent, r, lane * 16 + j * 1024, so, 16);
        __builtin_amdgcn_raw_buffer_store_b128(sent, r, l3, so, 16);
      }
      get_tag();
      const int m2 = tid < 176 ? C_F + 1024 + (tid >> 3) * 1024 + mi * 128 + (tid & 7) * 16 : OOB;
      if (wave < 3) __builtin_amdgcn_raw_buffer_store_b128((u32x4){~tag, ~tag, ~tag, ~tag}, rc_, m2, 0, 16);
#pragma unroll 1
      for (int k = mi * 22 + 7 - wave; k < mi * 22 + 22; k += 8) {
        const int so = ((k >> 2) * 16 + 12 + (k & 3)) * 3840;
#pragma unroll
        for (int j = 0; j < 3; ++j) {
          __builtin_amdgcn_raw_buffer_store_b128(sent, r, lane * 16 + j * 1024, so, 16);
          __builtin_amdgcn_raw_buffer_store_b128(sent, r, l0, so - (j + 1) * 4 * 3840, 16);
        }
        __builtin_amdgcn_raw_buffer_store_b128(sent, r, l4, so, 16);
        if (k < mi * 22 + 8) {
          WAITV(7);
          BARRIER();
          if (wave == 0) __builtin_amdgcn_raw_buffer_store_b128((u32x4){tag, tag, tag, tag}, rc_, lane < 8 ? C_F + mi * 128 + lane * 16 : OOB, 0, 16);
        }
      }
      WAITV(0);
      BARRIER();
      if (wave < 3) __builtin_amdgcn_raw_buffer_store_b128((u32x4){tag, tag, tag, tag}, rc_, m2, 0, 16);
      return;
    }
    if (f == 27) {
      const unsigned t0 = (unsigned)__builtin_amdgcn_s_memrealtime();
      auto until = [&](unsigned d) AI {
        while ((unsigned)__builtin_amdgcn_s_memrealtime() - t0 < d) __builtin_amdgcn_s_sleep(1);
      };
      get_tag();
      __builtin_amdgcn_raw_buffer_store_b128((u32x4){~tag, ~tag, ~tag, ~tag}, rc_, tid < 320 ? C_X + tid * 16 : OOB, 0, 16);
      u32x2 kv[3];
      int ko[3];
      ko[0] = (tid >> 2) * 256 + (tid & 2) * 32 + (tid & 1) * 16;
      ko[1] = tid < 368 ? ko[0] + 128 * 256 : OOB;
      ko[2] = tid < 176 ? ((tid >> 2) * 5 + ((tid >> 1) & 1)) * 256 + 128 + (tid & 1) * 16 : OOB;
      kv[0] = kv[1] = kv[2] = (u32x2){0u, 0u};
#pragma unroll 1
      for (unsigned spins = 0; spins < SPIN_CAP; ++spins) {
        asm volatile("" ::: "memory");
        u32x4 t_[3];
#pragma unroll
        for (int q = 0; q < 3; ++q) t_[q] = __builtin_amdgcn_raw_buffer_load_b128(rc_, ko[q], C_K, 16);
        bool ok = true;
#pragma unroll
        for (int q = 0; q < 3; ++q)
          if (ko[q] != OOB) {
            if ((t_[q][1] == tag) & (t_[q][3] == tag)) {
              kv[q] = (u32x2){t_[q][0], t_[q][2]};
              ko[q] = OOB;
            } else {
              ok = false;
            }
          }
        if (__all(ok)) break;
        until(LD_T);
        __builtin_amdgcn_s_sleep(LD_S);
      }
      unsigned k1 = max(kv[0][0], kv[0][1]), k2 = min(kv[0][0], kv[0][1]);
      top2_mrg(k1, k2, max(kv[1][0], kv[1][1]), min(kv[1][0], kv[1][1]));
      top2_mrg(k1, k2, max(kv[2][0], kv[2][1]), min(kv[2][0], kv[2][1]));
      top2_all(k1, k2);
      unsigned* lk = (unsigned*)smem;
      if (lane == 0) *(u32x2*)(lk + wave * 2) = (u32x2){k1, k2};
      WAITL0();
      BARRIER();
      k1 = 0u;
      k2 = 0u;
#pragma unroll
      for (int q = 0; q < 4; ++q) {
        const u32x4 a_ = *(const u32x4*)(lk + q * 4);
        top2_mrg(k1, k2, a_[0], a_[1]);
        top2_mrg(k1, k2, a_[2], a_[3]);
      }
      if (tid < 320) {
        const u32x2 v = *(const u32x2*)(x + (2047 - (int)((tid < 160 ? k1 : k2) & 2047u)) * DM + (tid < 160 ? tid : tid - 160) * 4);
        __builtin_amdgcn_raw_buffer_store_b128((u32x4){v[0], tag, v[1], tag}, rc_, C_X + tid * 16, 0, 16);
      } else if (tid < 328) {
        if (tid & 1) {
          k1 = __float_as_uint(rbf(__builtin_amdgcn_rcpf(1.0f + __expf(-key_val(k1)))));
          k2 = __float_as_uint(rbf(__builtin_amdgcn_rcpf(1.0f + __expf(-key_val(k2)))));
        }
        __builtin_amdgcn_raw_buffer_store_b128((u32x4){k1, tag, k2, tag}, rc_, C_R + (tid - 320) * 16, 0, 16);
      }
      until(KP_T);
      int ao = tid < 220 ? C_H + tid * 128 + 4 : tid < 228 ? C_F + (tid - 220) * 128 : tid < 255 ? C_A + (tid - 228) * 128 : OOB;
#pragma unroll 1
      for (unsigned spins = 0; spins < SPIN_CAP; ++spins) {
        asm volatile("" ::: "memory");
        if (__builtin_amdgcn_raw_buffer_load_b32(rc_, ao, 0, 16) == tag) ao = OOB;
        if (__all(ao == OOB)) break;
        __builtin_amdgcn_s_sleep(16);
      }
      BARRIER();
      if (tid == 0) __hip_atomic_store((gu32*)(ws + WS_C + C_E), tag, RLX_AGENT);
      return;
    }
    volatile LAS unsigned* cnt = (volatile LAS unsigned*)(LAS char*)(smem + 98304);
    if (tid == 0) cnt[0] = 0u;
    const unsigned t0 = (unsigned)__builtin_amdgcn_s_memrealtime();
    if (FF_D) __builtin_amdgcn_s_sleep(FF_D);
    WAITL0();
    BARRIER();
    if (wave == 7) {
      get_tag();
#pragma unroll 1
      for (unsigned spins = 0; spins < (SPIN_CAP << 8); ++spins) {
        if (__builtin_amdgcn_readfirstlane(cnt[0]) >= 7) break;
        __builtin_amdgcn_s_sleep(1);
      }
      __builtin_amdgcn_raw_buffer_store_b128((u32x4){tag, tag, tag, tag}, rc_, lane < 8 ? C_A + f * 128 + lane * 16 : OOB, 0, 16);
      const u32x4 rsd = make_srd(w_down, DM * FF * 2);
      const int kr = lane >> 3;
      int vd = kr * (DM * 2) + (((lane & 7) ^ (kr & 2)) * 16);
#pragma unroll 1
      for (int o = 0; o < 8; ++o) {
#pragma unroll
        for (int j = 0; j < 10; ++j)
          asm volatile("s_mov_b32 m0, %3\n\ts_nop 0\n\tbuffer_load_dwordx4 %0, %1, %2 offen nt lds" ::"v"(vd), "s"(rsd), "s"((f * 64 + o * 8) * (DM * 2) + j * 128),
                       "s"(lds0 + (o * 10 + j) * 1024)
                       : "memory");
        vd ^= 64;
      }
      BARRIER();
      WAITV(0);
      BARRIER();
      return;
    }
    u32x4 r1[3][8];
    {
      const char* pa = (const char*)(c15 < 8 ? w_gate : w_up) + f * 128 + (c15 & 7) * 16 + (wave * 96 + g4 * 8) * (FF * 2);
      const GAS char* pp[12];
#pragma unroll
      for (int j = 0; j < 12; ++j) {
        pp[j] = (const GAS char*)pa + ((j >> 2) * 32 + (j & 3) * 2) * (FF * 2) + FF;
        PIN(pp[j]);
      }
      SB();
#pragma unroll
      for (int j = 0; j < 24; ++j)
        if (j < 16 || wave < 6) r1[j >> 3][j & 7] = __builtin_nontemporal_load((const GAS u32x4*)(pp[j >> 1] + ((j & 1) ? FF : -FF)));
    }
    SB();
    get_tag();
    if (lane == 0) __hip_atomic_fetch_add((LAS unsigned*)cnt, 1u, __ATOMIC_RELAXED, __HIP_MEMORY_SCOPE_WORKGROUP);
#pragma unroll
    for (int k = 0; k < 3; ++k) {
      if (k == 2 && wave == 6) break;
      u32x4 t[8];
#pragma unroll
      for (int d = 0; d < 4; ++d)
#pragma unroll
        for (int p = 0; p < 4; ++p) {
          t[2 * d][p] = perm_lo(r1[k][2 * p + 1][d], r1[k][2 * p][d]);
          t[2 * d + 1][p] = perm_hi(r1[k][2 * p + 1][d], r1[k][2 * p][d]);
        }
#pragma unroll
      for (int j = 0; j < 8; ++j) {
        r1[k][j] = t[j];
        PIN(r1[k][j]);
      }
    }
    int xa = 90112 + (c15 & 1) * 2560 + (wave * 96 + g4 * 8) * 4, x2 = xa + (wave < 6 ? 256 : 0), ra = 81920 + wave * 1024 + c15 * 64 + g4 * 16;
    int rr = 81920 + lane * 16, hw_ = 95232 + wave * 256 + (lane & 31) * 4, hr = 95232 + wave * 256 + (c15 >> 3) * 128 + g4 * 16, da[4];
    int yo = ((wave * 4 + (c15 >> 3)) * 27 + f) * 512 + g4 * 128 + (lane & 7) * 16;
#pragma unroll
    for (int t = 0; t < 4; ++t) {
      da[t] = (g4 * 10 + wave * 2) * 1024 + q4 * 128 + ((t ^ (q4 >> 1) ^ ((g4 & 1) << 1)) * 32) + p4 * 8;
      PIN(da[t]);
    }
    PIN(xa); PIN(x2); PIN(ra); PIN(rr); PIN(hw_); PIN(hr); PIN(yo);
    if (wave == 0) {
      unsigned nt = RW_T;
#pragma unroll 1
      for (unsigned spins = 0; spins < (SPIN_CAP << 4); ++spins) {
        const unsigned now = (unsigned)__builtin_amdgcn_s_memrealtime() - t0;
        if (now >= nt) {
#pragma unroll
          for (int k = 0; k < 5; ++k) dma16_sc1(lane * 16, ws + WS_C + C_X + k * 1024, lds0 + 90112 + k * 1024);
          nt = now + RW_P;
        }
        unsigned d = 0;
#pragma unroll
        for (int k = 0; k < 5; ++k) {
          const u32x4 v = *(volatile LAS u32x4*)(LAS char*)(smem + 90112 + k * 1024 + lane * 16);
          d |= (v[1] ^ tag) | (v[3] ^ tag);
        }
        if (__all(d == 0)) break;
        __builtin_amdgcn_s_sleep(2);
      }
    }
    u32x4 xf[3] = {};
#pragma unroll 1
    for (unsigned spins = 0; spins < (SPIN_CAP << 6); ++spins) {
      if (__builtin_amdgcn_readfirstlane(*(volatile LAS unsigned*)(LAS char*)(smem + 90112 + 5116)) != tag) {
        __builtin_amdgcn_s_sleep(2);
        continue;
      }
      unsigned d = 0;
#pragma unroll
      for (int k = 0; k < 3; ++k) {
        const int o = k < 2 ? xa + k * 128 : x2;
        const u32x4 v0 = *(volatile LAS u32x4*)(LAS char*)(smem + o), v1 = *(volatile LAS u32x4*)(LAS char*)(smem + o + 16);
        d |= ((v0[1] ^ tag) | (v0[3] ^ tag)) | ((v1[1] ^ tag) | (v1[3] ^ tag));
        xf[k] = (u32x4){v0[0], v0[2], v1[0], v1[2]};
      }
      if (__all(d == 0)) break;
      __builtin_amdgcn_s_sleep(4);
    }
    {
      f32x4 acc[8];
#pragma unroll
      for (int k = 0; k < 3; ++k)
        if (k < 2 || wave < 6) {
#pragma unroll
          for (int j = 0; j < 8; ++j) acc[j] = mfma16(xf[k], r1[k][j], k ? acc[j] : (f32x4){0.f, 0.f, 0.f, 0.f});
        }
      f32x4 pv;
#pragma unroll
      for (int q = 0; q < 4; ++q) {
        const float lo = (g4 & 1) ? acc[2 + (q >> 1)][q & 1] : acc[q >> 1][q & 1], hi = (g4 & 1) ? acc[6 + (q >> 1)][q & 1] : acc[4 + (q >> 1)][q & 1];
        pv[q] = (g4 & 2) ? hi : lo;
      }
      *(f32x4*)(smem + ra) = pv;
    }
    WAITL0();
    BARRIER();
    u32x4 h0, h1;
    if (wave < 5) {
      f32x4 sum = *(const f32x4*)(smem + rr);
#pragma unroll
      for (int w = 1; w < 7; ++w) sum += *(const f32x4*)(smem + rr + w * 1024);
      float hv[4];
#pragma unroll
      for (int q = 0; q < 4; ++q) {
        const auto r_ = __builtin_amdgcn_permlane32_swap(__float_as_uint(sum[q]), __float_as_uint(sum[q]), false, false);
        const float gv = rbf(__uint_as_float(r_[0])), uv = rbf(__uint_as_float(r_[1]));
        hv[q] = rbf(gv * __builtin_amdgcn_rcpf(1.0f + __expf(-gv))) * uv;
      }
      *(unsigned*)(smem + hw_ + (lane >> 5) * 128) = lane < 32 ? pk2(hv[0], hv[2]) : pk2(hv[1], hv[3]);
      asm volatile("" ::: "memory");
      h0 = *(const u32x4*)(smem + hr);
      h1 = *(const u32x4*)(smem + hr + 64);
      PIN(h0); PIN(h1);
    }
    WAITV(0);
    BARRIER();
    if (wave < 5) {
      const __amdgpu_buffer_rsrc_t ryp = RSRC(ws + WS_YP, 27 * 10240);
      u32x4 wd[2][4][2];
#pragma unroll
      for (int e = 0; e < 4; ++e)
#pragma unroll
        for (int t = 0; t < 4; ++t) {
          const char* p = smem + da[t] + (e >> 1) * 1024 + (e & 1) * 40960;
          const u32x2 w0 = tr_read(p), w1 = tr_read(p + 512);
          wd[e >> 1][t][e & 1] = (u32x4){w0[0], w0[1], w1[0], w1[1]};
          SB();
        }
      f32x4 a[2][4];
#pragma unroll
      for (int e = 0; e < 4; ++e)
#pragma unroll
        for (int t = 0; t < 4; ++t) a[e >> 1][t] = mfma16(wd[e >> 1][t][e & 1], (e & 1) ? h1 : h0, (e & 1) ? a[e >> 1][t] : (f32x4){0.f, 0.f, 0.f, 0.f});
#pragma unroll
      for (int e = 0; e < 2; ++e) {
        float y[2];
#pragma unroll
        for (int q = 0; q < 2; ++q) {
          float s_[4];
#pragma unroll
          for (int t = 0; t < 4; ++t) s_[t] = (lane & 1) ? a[e][t][2 + q] : a[e][t][q];
          y[q] = (lane & 4) ? ((lane & 2) ? s_[3] : s_[2]) : ((lane & 2) ? s_[1] : s_[0]);
        }
        __builtin_amdgcn_raw_buffer_store_b128((u32x4){__float_as_uint(y[0]), __float_as_uint(y[1]), tag, tag}, ryp, yo + e * (54 * 512), 0, 16);
      }
    }
    return;
  }

  u32x16 p0, p1, p2, p3, r0;
  unsigned sc[16];
#define UNI(x) __builtin_amdgcn_readfirstlane(x)
  const int kr = lane >> 3, hw = wave & 3;
  const int mt = wave < 4 ? wave + 1 : wave < 6 ? 0 : 5;

  if (__builtin_expect(wave >= 4, 1)) {
    asm volatile("" : "=v"(p0), "=v"(p1), "=v"(p2), "=v"(p3), "=v"(r0));
    const int sw16 = cg * 128 + (((lane & 7) ^ ((kr & 2) | ((hw & 1) << 2))) * 16);
#pragma unroll
    for (int v = 0; v < 3; ++v) {
      r0[v] = ((tb + (hw >> 1) * 48 + v * 16 + (hw & 1) * 8 + kr) * 2 + par) * (DM * 2) + (((lane & 7) ^ (((hw & 1) * 4 + g4) & 7)) * 16) + (v == 2 ? 3072 : 0);
      r0[3 + v] = (hw * 8 + kr) * (NQKV * 2) + sw16 + v * 256;
      r0[6 + v] = r0[3 + v] + 32 * (NQKV * 2);
    }
    int kc = j5 == 5 ? 1 : j5 * 2;
    args();
    asm volatile("s_mov_b32 m0, %9\n\ts_nop 0\n\tbuffer_load_dwordx4 %0, %5, %7 offen lds\n\tbuffer_load_dwordx4 %1, %5, %7 offen offset:1024 lds\n\t"
                 "buffer_load_dwordx4 %2, %5, %7 offen offset:2048 lds\n\tbuffer_load_dwordx4 %3, %6, %8 offen offset:3072 lds\n\t"
                 "s_add_u32 m0, m0, 8192\n\ts_nop 0\n\tbuffer_load_dwordx4 %4, %6, %8 offen lds" ::"v"(r0[3]),
                 "v"(r0[4]), "v"(r0[5]), "v"(r0[0]), "v"(r0[2]), "s"(make_srd(wqkv, DM * NQKV * 2)), "s"(make_srd((const char*)x - 3072, S_LEN * DM * 2 + 3072)),
                 "s"(kc * (64 * NQKV * 2)), "s"(kc * 128), "s"(BUF(0) + hw * 9216)
                 : "memory", "scc");
    SB();
    r0[9] = lane < 16 ? (gb * 4 + hw) * 1024 + lane * 64 : OOB;
    {
      const int sct = (gb * 4 + hw) * 22 + lane, ck = (sct * 17477) >> 26;
      r0[14] = lane < 22 ? (sct + (ck + 1) * 3840) * 64 : OOB;
    }
    tag = 0;
    sc[2] = kc;
    sc[4] = sc[5] = 0;
  } else {
    const bool wjob = wave < 2 || (wave == 2 && c < 2);
    const int tj = tb + (wave < 2 ? wave * 10 + cg : 20 + hf * 2 + c) * 4, tj0 = min(tj, 1020);
    u32x4 xs[5], wv[4];
    int jo0 = lane * 16, jo1 = g4 * 2560 + c15 * 16, jo2 = c15 * 16;
    PIN(jo0); PIN(jo1); PIN(jo2);
    args();
    if (wjob) {
      const u16* xr = x + (2 * tj0 + par) * DM;
      asm volatile("global_load_dwordx4 %0, %9, %12\n\tglobal_load_dwordx4 %1, %9, %12 offset:2560\n\t"
                   "global_load_dwordx4 %2, %9, %13\n\tglobal_load_dwordx4 %3, %9, %13 offset:2560\n\t"
                   "global_load_dwordx4 %4, %10, %12 offset:1024\n\tglobal_load_dwordx4 %5, %9, %14\n\t"
                   "global_load_dwordx4 %6, %11, %14 offset:1024\n\tglobal_load_dwordx4 %7, %9, %15\n\tglobal_load_dwordx4 %8, %11, %15 offset:1024"
                   : "=&v"(xs[0]), "=&v"(xs[1]), "=&v"(xs[2]), "=&v"(xs[3]), "=&v"(xs[4]), "=&v"(wv[0]), "=&v"(wv[1]), "=&v"(wv[2]), "=&v"(wv[3])
                   : "v"(jo0), "v"(jo1), "v"(jo2), "s"(xr), "s"(xr + 4 * DM), "s"(w_r), "s"(gate_w)
                   : "memory");
    }
    const int rh = wave >> 1, ch = wave & 1;
    {
      const int e0 = c15 - 4 * g4;
#pragma unroll
      for (int i = 0; i < 4; ++i) {
        p0[i] = e0 - i <= -9 ? 0u : 0xFF800000u;
        p0[4 + i] = (e0 - i >= 0 && e0 - i <= 7) ? 0u : 0xFF800000u;
      }
      p0[8] = (e0 <= -9 ? 0xFFFFu : 0u) | (e0 - 1 <= -9 ? 0xFFFF0000u : 0u);
      p0[9] = (e0 - 2 <= -9 ? 0xFFFFu : 0u) | (e0 - 3 <= -9 ? 0xFFFF0000u : 0u);
      p0[10] = ~p0[8];
      p0[11] = ~p0[9];
      p0[12] = (g4 >> 1) * 61440 + (lane & 31) * 16;
      p0[13] = g4 * 3840 + c15 * 8 + 512;
      p0[14] = lane < 32 ? lane * 16 : OOB;
      p0[15] = g4 == 2 ? c15 * 8 + 512 : OOB;
      const int sw = (q4 >> 1) | ((g4 & 1) << 1), ho = (rg * 2 + hf) * 16;
      const unsigned aq = c15 * RS + g4 * 8, akh = c15 * RS + 32, av = 2 * SO - TS + (4 * g4 + q4) * RS + (p4 < 2 ? p4 * 8 : 32);
      unsigned xa[4];
#pragma unroll
      for (int f = 0; f < 4; ++f) {
        p2[f] = L_WO + (8 * g4 + q4) * 128 + ((f ^ sw) * 32) + p4 * 8;
        xa[f] = L_XT + c15 * 128 + ((((f * 2 + (g4 >> 1)) ^ (c15 >> 1)) & 7) * 16) + (g4 & 1) * 8;
      }
      if (wave == 3) {
        u32x4* tp = (u32x4*)(smem + L_TAB + lane * 16);
#pragma unroll
        for (int k = 0; k < 4; ++k) tp[k * 64] = (u32x4){p0[4 * k], p0[4 * k + 1], p0[4 * k + 2], p0[4 * k + 3]};
        const int s = ((c ? 0x2FDFCFC9u : 0x20D0C0C9u) >> (c15 * 2)) & 3, m = ((c == 0 ? 0x53204215310455ull : c == 4 ? 0x3053204255310455ull : 0x3053204215310455ull) >> (c15 * 4)) & 7;
        const bool nv = ((c == 0 ? 0x4u : c == 4 ? 0x800u : 0x330u) >> c15) & 1;
        tp[4 * 64] = (u32x4){aq, akh, g4 == 0 ? akh : L_Z, av};
        tp[5 * 64] = (u32x4){g4 == 0 ? 1u : 0u, (unsigned)(m * TS + s * 40), nv ? ~0u : (unsigned)((ho + POS_OF(3 * c + s)) * 3840 + m * 640), 0u};
        tp[6 * 64] = (u32x4){p2[0], p2[1], p2[2], p2[3]};
        tp[7 * 64] = (u32x4){xa[0], xa[1], xa[2], xa[3]};
        const int row = lane >> 2, sw0 = cg * 128 + (((lane & 7) ^ (kr & 2)) * 16), j = lane >> 1, w = j / 10;
        tp[8 * 64] = (u32x4){(unsigned)(L_XT + row * 128 + ((p4 ^ (row >> 1)) * 16)), (unsigned)(((tb + row) * 2 + par) * (DM * 2) + cg * 128 + p4 * 16),
                             (unsigned)(L_SMP + c15 * 8), (unsigned)(L_EX + lane * 16)};
        tp[9 * 64] = (u32x4){(unsigned)((kr + (kr & 4)) * (DM * 2) + sw0), (unsigned)(((kr & 3) + (kr >> 2) * 320 + 16) * (DM * 2) + sw0), 0u,
                             (unsigned)(lane < 48 ? C_K + (rg * 10 + j - w * 10 + (j >= 22 ? 3 : 0)) * 256 + w * 64 + 32 + (lane & 1) * 16 : lane < 56 ? C_F + (lane - 48) * 128 : C_R + (lane & 1) * 16)};
        const int hi = lane >> 5, g = kr & 3, r8 = lane & 7;
        const unsigned g2 = gi ? gb * INB + 6144 + hi * 1024 + lane * 16 : OOB;
        const unsigned g3 = hi ? OOB : g >= 4 - c ? gb * INB + 9216 + lane * 16 : gi ? (gb + 1) * INB + 6144 + lane * 16 : OOB;
        tp[10 * 64] = (u32x4){(unsigned)(C_H + (hi ? gb + (p4 == 0 ? -(c > 0) : (p4 & 1) ? c < 4 : 0) - (gi && p4 >= 2 ? 10 : 0) : rg * 10 + min(c15, 9) - (gi && g4 ? 10 : 0)) * 128), 0u, (unsigned)(C_F + 1024 + rg * 1024 + r8 * 128), (unsigned)(gb * INB + lane * 16)};
        tp[11 * 64] = (u32x4){(gi && hi && c < 4 && g >= 3 - c) ? (gb - 9) * INB + 4096 + (g * 16 + 8 + r8) * 16 : g2, g2, g3,
                              (unsigned)(g4 < 4 - c ? c15 * RS + 8 * c + 128 + 8 * g4 : (48 + c15) * RS + 8 * (g4 - 4 + c))};
        tp[12 * 64] = (u32x4){(unsigned)(SO + (r8 - 8) * RS + 8 * c + 8 * (hi * 12 + g)), (unsigned)(SO + (r8 - 8) * RS + 8 * c + 8 * (hi * 4 + 4 + g)),
                              (unsigned)(hi ? 160 : g >= 4 - c ? SO + (40 + r8) * RS + 8 * (g - 4 + c) : SO + (r8 - 8) * RS + 8 * c + 128 + 8 * g), g3 == OOB ? 0u : ~0u};
        if (lane < 32) *(unsigned*)(smem + L_Z + lane * 4) = 0u;
        *(unsigned*)(smem + 2 * SO + (32 + kr) * RS + (lane & 7) * 4) = 0u;
      }
      SB();
      const int swz = (c15 >> 1) & 7;
      asm volatile("" : "=v"(r0), "=v"(p3), "=v"(p1));
#pragma unroll
      for (int h = 0; h < 2; ++h) {
        const int tl = ch ? 3 - h : h, tba = mt * TS + 40 + 40 * h;
        p1[4 * h] = aq + tba;
        p1[4 * h + 1] = akh + tba;
        p1[4 * h + 2] = g4 == 0 ? akh + tba : L_Z;
        p1[4 * h + 3] = av + tba;
        sc[4 + h] = (ho + (c < 4 ? c + 4 * h : 9 + h)) * 3840 + mt * 640;
#pragma unroll
        for (int q = 0; q < 3; ++q) {
          r0[8 + 2 * q + h] = BUF(q) + (rh * 2 + (c15 >> 3)) * 9216 + (c15 & 7) * 128 + (((4 * h + g4) ^ swz) * 16);
          const unsigned wa = BUF(q) + g4 * 9216 + q4 * 128 + ((tl ^ sw) * 32) + p4 * 8;
          if (q < 2) p2[12 + 2 * q + h] = wa;
          else p3[h] = wa;
        }
        p1[8 + h] = (rh * 48 + c15) * RS + 8 * c + 8 * (tl * 4 + g4);
      }
#pragma unroll
      for (int f = 0; f < 4; ++f) {
        p2[4 + f] = xa[f] + mt * 2048;
      }
      p2[8] = L_XT + (mt * 16 + kr) * 128 + (((lane & 7) ^ (kr >> 1)) * 16);
      p2[9] = ((tb + mt * 16 + kr) * 2 + par) * (DM * 2) + cg * 128 + (lane & 7) * 16;
      p2[10] = L_SMP + (mt * 16 + c15) * 8;
      p2[11] = (p2[8] + 1024) ^ 64;
      const bool tor = c < 4 && g4 >= 3 - c, dn = rh == 1 && c15 >= 8 && gi < 10;
      const int hidx = (g4 * 8 + c15 - 8) * 16;
      p3[2] = (wave == 0 && g4 < 5 - c && c > 0) ? (gb - 1) * INB + lane * 16 : (wave == 3 && tor) ? (gb + 1) * INB + lane * 16 : OOB;
      p3[5] = dn ? (gb + 10) * INB + 6144 + ch * 1536 + hidx : OOB;
      p3[4] = (wave == 1 && tor && c15 >= 8) ? (gb + 1) * INB + 9216 + hidx : p3[5];
      p3[3] = p3[2] != OOB ? p3[2] + 4096 : p3[4];
      p3[12] = p3[13] = p3[14] = p3[15] = 0x3F803F80u;
      PIN(p0); PIN(p1); PIN(p2); PIN(p3); PIN(r0);
      sc[2] = 0;
    }
    int ko = wave == 3 ? C_H + gb * 128 + c15 * 8 : c15 > 1 ? OOB : C_K + gb * 256 + wave * 64 + c15 * 32 + g4 * 8;
    PIN(ko);
    auto rows4 = [&](float (&p)[5]) AI {
      const auto a = __builtin_amdgcn_permlane32_swap(__float_as_uint(p[0]), __float_as_uint(p[2]), false, false);
      const auto c_ = __builtin_amdgcn_permlane32_swap(__float_as_uint(p[1]), __float_as_uint(p[3]), false, false);
      const auto e = __builtin_amdgcn_permlane16_swap(__float_as_uint(__uint_as_float(a[0]) + __uint_as_float(a[1])),
                                                      __float_as_uint(__uint_as_float(c_[0]) + __uint_as_float(c_[1])), false, false);
      return row_sum(__uint_as_float(e[0]) + __uint_as_float(e[1]) + p[4]);
    };
    SB();
    get_tag();
    if (wave == 3) __builtin_amdgcn_raw_buffer_store_b64((u32x2){0u, ~tag}, rc_, ko, 0, 16);
    int rowi = tj < 1024 ? 2047 - (2 * (tj0 + g4) + par) : -1;
    PIN(rowi);
    SB();
    if (wjob) {
      WAITV(0);
#pragma unroll
      for (int i = 0; i < 5; ++i) PIN(xs[i]);
#pragma unroll
      for (int i = 0; i < 4; ++i) PIN(wv[i]);
      float p[5], pg[5];
#pragma unroll
      for (int i = 0; i < 5; ++i) {
        p[i] = pg[i] = 0.f;
#pragma unroll
        for (int q = 0; q < 4; ++q) {
          p[i] = dot2(xs[i][q], wv[i == 4][q], p[i]);
          pg[i] = dot2(xs[i][q], wv[2 + (i == 4)][q], pg[i]);
        }
      }
      const float r_ = rows4(p), g_ = rbf(rows4(pg));
      unsigned u = pk2(r_, r_) & 0xFFFFu;
      u ^= (u & 0x8000u) ? 0xFFFFu : 0x8000u;
      const unsigned key = rowi >= 0 ? (u << 16) | (unsigned)rowi : 0u;
      __builtin_amdgcn_raw_buffer_store_b64((u32x2){c15 ? __float_as_uint(rbf(__builtin_amdgcn_rcpf(1.0f + __expf(-g_)))) : key, tag}, rc_, ko, 0, 16);
      SB();
    }
    WAITL0();
  }
  sc[0] = tag;
  sc[1] = wave;
  sc[3] = hw * 9216;
  sc[6] = 0;
  sc[7] = 0;
  sc[8] = rg * 122880 + mt * 640 + (wave < 4 ? 0 : (wave & 1) * 30720);
  sc[9] = (gi > 0 ? 8 : 0) | ((0x7801Eu >> (hw * 5 + c)) & 1);
  sc[10] = cg * 128;
  sc[11] = hw * 1024;
  sc[12] = ((hw >> 1) * 6 + (hw & 1)) * 1024;
  sc[13] = par;
  sc[14] = tb;
  sc[15] = mt;
  u32x16 uc;
#pragma unroll
  for (int i = 0; i < 16; ++i) {
    PINS(sc[i]);
    uc[i] = sc[i];
  }
  asm volatile(
    "s_nop 4\n"
    "s_load_dwordx4 s[92:95], s[90:91], 0x0\n"
    "s_load_dwordx2 s[44:45], s[90:91], 0x10\n"
    "s_load_dwordx4 s[96:99], s[90:91], 0x40\n"
    "s_waitcnt lgkmcnt(0)\n"
    "s_cmp_ge_u32 s69, 4\n"
    "s_cbranch_scc1 LOADER%=\n"
    "s_barrier\n"
    "ds_read_b64_tr_b16 v[72:73], v236 offset:1024\n"
    "ds_read_b64_tr_b16 v[74:75], v236 offset:1536\n"
    "ds_read_b128 v[96:99], v184 offset:3072\n"
    "ds_read_b64_tr_b16 v[76:77], v236 offset:2048\n"
    "ds_read_b64_tr_b16 v[78:79], v236 offset:2560\n"
    "ds_read_b64_tr_b16 v[80:81], v236\n"
    "ds_read_b64_tr_b16 v[82:83], v236 offset:512\n"
    "ds_read_b64_tr_b16 v[84:85], v237 offset:1024\n"
    "ds_read_b64_tr_b16 v[86:87], v237 offset:1536\n"
    "ds_read_b64_tr_b16 v[88:89], v237 offset:2048\n"
    "ds_read_b64_tr_b16 v[90:91], v237 offset:2560\n"
    "ds_read_b64_tr_b16 v[92:93], v237\n"
    "ds_read_b64_tr_b16 v[94:95], v237 offset:512\n"
    "ds_read_b128 v[100:103], v184 offset:7168\n"
    "ds_read_b128 v[104:107], v184 offset:8192\n"
    "s_waitcnt lgkmcnt(12)\n"
    "v_mfma_f32_16x16x32_bf16 v[0:3], v[72:75], v[96:99], 0\n"
    "ds_read_b64_tr_b16 v[108:109], v236 offset:5120\n"
    "s_waitcnt lgkmcnt(11)\n"
    "v_mfma_f32_16x16x32_bf16 v[4:7], v[76:79], v[96:99], 0\n"
    "ds_read_b64_tr_b16 v[110:111], v236 offset:5632\n"
    "s_waitcnt lgkmcnt(10)\n"
    "v_mfma_f32_16x16x32_bf16 v[8:11], v[80:83], v[96:99], 0\n"
    "ds_read_b128 v[132:135], v185 offset:3072\n"
    "s_waitcnt lgkmcnt(9)\n"
    "v_mfma_f32_16x16x32_bf16 v[12:15], v[84:87], v[96:99], 0\n"
    "ds_read_b64_tr_b16 v[112:113], v236 offset:6144\n"
    "s_waitcnt lgkmcnt(8)\n"
    "v_mfma_f32_16x16x32_bf16 v[16:19], v[88:91], v[96:99], 0\n"
    "ds_read_b64_tr_b16 v[114:115], v236 offset:6656\n"
    "s_waitcnt lgkmcnt(7)\n"
    "v_mfma_f32_16x16x32_bf16 v[20:23], v[92:95], v[96:99], 0\n"
    "ds_read_b64_tr_b16 v[116:117], v236 offset:4096\n"
    "s_waitcnt lgkmcnt(7)\n"
    "v_mfma_f32_16x16x32_bf16 v[24:27], v[72:75], v[100:103], 0\n"
    "ds_read_b64_tr_b16 v[118:119], v236 offset:4608\n"
    "v_mfma_f32_16x16x32_bf16 v[28:31], v[76:79], v[100:103], 0\n"
    "ds_read_b64_tr_b16 v[120:121], v237 offset:5120\n"
    "v_mfma_f32_16x16x32_bf16 v[32:35], v[80:83], v[100:103], 0\n"
    "ds_read_b64_tr_b16 v[122:123], v237 offset:5632\n"
    "v_mfma_f32_16x16x32_bf16 v[36:39], v[84:87], v[100:103], 0\n"
    "ds_read_b64_tr_b16 v[124:125], v237 offset:6144\n"
    "v_mfma_f32_16x16x32_bf16 v[40:43], v[88:91], v[100:103], 0\n"
    "ds_read_b64_tr_b16 v[126:127], v237 offset:6656\n"
    "v_mfma_f32_16x16x32_bf16 v[44:47], v[92:95], v[100:103], 0\n"
    "ds_read_b64_tr_b16 v[128:129], v237 offset:4096\n"
    "s_waitcnt lgkmcnt(12)\n"
    "v_mfma_f32_16x16x32_bf16 v[48:51], v[72:75], v[104:107], 0\n"
    "ds_read_b64_tr_b16 v[130:131], v237 offset:4608\n"
    "v_mfma_f32_16x16x32_bf16 v[52:55], v[76:79], v[104:107], 0\n"
    "ds_read_b128 v[136:139], v185 offset:7168\n"
    "v_mfma_f32_16x16x32_bf16 v[56:59], v[80:83], v[104:107], 0\n"
    "ds_read_b128 v[140:143], v185 offset:8192\n"
    "v_mfma_f32_16x16x32_bf16 v[60:63], v[84:87], v[104:107], 0\n"
    "s_mov_b32 s48, s96\n"
    "s_and_b32 s49, s97, 65535\n"
    "v_mfma_f32_16x16x32_bf16 v[64:67], v[88:91], v[104:107], 0\n"
    "s_mov_b32 s50, 2703360\n"
    "s_mov_b32 s51, 131072\n"
    "v_mfma_f32_16x16x32_bf16 v[68:71], v[92:95], v[104:107], 0\n"
    "s_add_u32 s52, s96, 2703360\n"
    "s_addc_u32 s53, s97, 0\n"
    "s_waitcnt lgkmcnt(6)\n"
    "v_mfma_f32_16x16x32_bf16 v[0:3], v[108:111], v[132:135], v[0:3]\n"
    "s_and_b32 s53, s53, 65535\n"
    "s_mov_b32 s54, 2252800\n"
    "v_mfma_f32_16x16x32_bf16 v[4:7], v[112:115], v[132:135], v[4:7]\n"
    "s_mov_b32 s55, 131072\n"
    "s_mov_b32 s56, s98\n"
    "v_mfma_f32_16x16x32_bf16 v[8:11], v[116:119], v[132:135], v[8:11]\n"
    "s_and_b32 s57, s99, 65535\n"
    "s_mov_b32 s58, 2621440\n"
    "s_waitcnt lgkmcnt(0)\n"
    "s_barrier\n"
    "v_mfma_f32_16x16x32_bf16 v[12:15], v[120:123], v[132:135], v[12:15]\n"
    "ds_read_b64_tr_b16 v[72:73], v238 offset:1024\n"
    "ds_read_b64_tr_b16 v[74:75], v238 offset:1536\n"
    "v_mfma_f32_16x16x32_bf16 v[16:19], v[124:127], v[132:135], v[16:19]\n"
    "ds_read_b128 v[96:99], v186 offset:3072\n"
    "v_mfma_f32_16x16x32_bf16 v[20:23], v[128:131], v[132:135], v[20:23]\n"
    "ds_read_b64_tr_b16 v[76:77], v238 offset:2048\n"
    "v_mfma_f32_16x16x32_bf16 v[24:27], v[108:111], v[136:139], v[24:27]\n"
    "ds_read_b64_tr_b16 v[78:79], v238 offset:2560\n"
    "v_mfma_f32_16x16x32_bf16 v[28:31], v[112:115], v[136:139], v[28:31]\n"
    "ds_read_b64_tr_b16 v[80:81], v238\n"
    "v_mfma_f32_16x16x32_bf16 v[32:35], v[116:119], v[136:139], v[32:35]\n"
    "ds_read_b64_tr_b16 v[82:83], v238 offset:512\n"
    "v_mfma_f32_16x16x32_bf16 v[36:39], v[120:123], v[136:139], v[36:39]\n"
    "ds_read_b64_tr_b16 v[84:85], v239 offset:1024\n"
    "v_mfma_f32_16x16x32_bf16 v[40:43], v[124:127], v[136:139], v[40:43]\n"
    "ds_read_b64_tr_b16 v[86:87], v239 offset:1536\n"
    "v_mfma_f32_16x16x32_bf16 v[44:47], v[128:131], v[136:139], v[44:47]\n"
    "ds_read_b64_tr_b16 v[88:89], v239 offset:2048\n"
    "v_mfma_f32_16x16x32_bf16 v[48:51], v[108:111], v[140:143], v[48:51]\n"
    "ds_read_b64_tr_b16 v[90:91], v239 offset:2560\n"
    "v_mfma_f32_16x16x32_bf16 v[52:55], v[112:115], v[140:143], v[52:55]\n"
    "ds_read_b64_tr_b16 v[92:93], v239\n"
    "v_mfma_f32_16x16x32_bf16 v[56:59], v[116:119], v[140:143], v[56:59]\n"
    "ds_read_b64_tr_b16 v[94:95], v239 offset:512\n"
    "v_mfma_f32_16x16x32_bf16 v[60:63], v[120:123], v[140:143], v[60:63]\n"
    "ds_read_b128 v[100:103], v186 offset:7168\n"
    "v_mfma_f32_16x16x32_bf16 v[64:67], v[124:127], v[140:143], v[64:67]\n"
    "ds_read_b128 v[104:107], v186 offset:8192\n"
    "v_mfma_f32_16x16x32_bf16 v[68:71], v[128:131], v[140:143], v[68:71]\n"
    "s_mov_b32 s59, 131072\n"
    "s_add_u32 s64, s96, 5102592\n"
    "s_waitcnt lgkmcnt(9)\n"
    "v_mfma_f32_16x16x32_bf16 v[0:3], v[72:75], v[96:99], v[0:3]\n"
    "ds_read_b64_tr_b16 v[108:109], v238 offset:5120\n"
    "v_mfma_f32_16x16x32_bf16 v[4:7], v[76:79], v[96:99], v[4:7]\n"
    "ds_read_b64_tr_b16 v[110:111], v238 offset:5632\n"
    "s_waitcnt lgkmcnt(10)\n"
    "v_mfma_f32_16x16x32_bf16 v[8:11], v[80:83], v[96:99], v[8:11]\n"
    "ds_read_b128 v[132:135], v187 offset:3072\n"
    "s_waitcnt lgkmcnt(9)\n"
    "v_mfma_f32_16x16x32_bf16 v[12:15], v[84:87], v[96:99], v[12:15]\n"
    "ds_read_b64_tr_b16 v[112:113], v238 offset:6144\n"
    "s_waitcnt lgkmcnt(4)\n"
    "v_mfma_f32_16x16x32_bf16 v[16:19], v[88:91], v[96:99], v[16:19]\n"
    "ds_read_b64_tr_b16 v[114:115], v238 offset:6656\n"
    "v_mfma_f32_16x16x32_bf16 v[20:23], v[92:95], v[96:99], v[20:23]\n"
    "ds_read_b64_tr_b16 v[116:117], v238 offset:4096\n"
    "v_mfma_f32_16x16x32_bf16 v[24:27], v[72:75], v[100:103], v[24:27]\n"
    "ds_read_b64_tr_b16 v[118:119], v238 offset:4608\n"
    "v_mfma_f32_16x16x32_bf16 v[28:31], v[76:79], v[100:103], v[28:31]\n"
    "ds_read_b64_tr_b16 v[120:121], v239 offset:5120\n"
    "v_mfma_f32_16x16x32_bf16 v[32:35], v[80:83], v[100:103], v[32:35]\n"
    "ds_read_b64_tr_b16 v[122:123], v239 offset:5632\n"
    "v_mfma_f32_16x16x32_bf16 v[36:39], v[84:87], v[100:103], v[36:39]\n"
    "ds_read_b64_tr_b16 v[124:125], v239 offset:6144\n"
    "v_mfma_f32_16x16x32_bf16 v[40:43], v[88:91], v[100:103], v[40:43]\n"
    "ds_read_b64_tr_b16 v[126:127], v239 offset:6656\n"
    "v_mfma_f32_16x16x32_bf16 v[44:47], v[92:95], v[100:103], v[44:47]\n"
    "ds_read_b64_tr_b16 v[128:129], v239 offset:4096\n"
    "v_mfma_f32_16x16x32_bf16 v[48:51], v[72:75], v[104:107], v[48:51]\n"
    "ds_read_b64_tr_b16 v[130:131], v239 offset:4608\n"
    "v_mfma_f32_16x16x32_bf16 v[52:55], v[76:79], v[104:107], v[52:55]\n"
    "ds_read_b128 v[136:139], v187 offset:7168\n"
    "v_mfma_f32_16x16x32_bf16 v[56:59], v[80:83], v[104:107], v[56:59]\n"
    "ds_read_b128 v[140:143], v187 offset:8192\n"
    "v_mfma_f32_16x16x32_bf16 v[60:63], v[84:87], v[104:107], v[60:63]\n"
    "s_addc_u32 s65, s97, 0\n"
    "s_and_b32 s65, s65, 65535\n"
    "v_mfma_f32_16x16x32_bf16 v[64:67], v[88:91], v[104:107], v[64:67]\n"
    "s_mov_b32 s66, 276480\n"
    "s_mov_b32 s67, 131072\n"
    "v_mfma_f32_16x16x32_bf16 v[68:71], v[92:95], v[104:107], v[68:71]\n"
    "s_waitcnt lgkmcnt(6)\n"
    "v_mfma_f32_16x16x32_bf16 v[0:3], v[108:111], v[132:135], v[0:3]\n"
    "v_mfma_f32_16x16x32_bf16 v[4:7], v[112:115], v[132:135], v[4:7]\n"
    "v_mfma_f32_16x16x32_bf16 v[8:11], v[116:119], v[132:135], v[8:11]\n"
    "s_waitcnt lgkmcnt(0)\n"
    "s_barrier\n"
    "v_mfma_f32_16x16x32_bf16 v[12:15], v[120:123], v[132:135], v[12:15]\n"
    "ds_read_b64_tr_b16 v[72:73], v240 offset:1024\n"
    "ds_read_b64_tr_b16 v[74:75], v240 offset:1536\n"
    "v_mfma_f32_16x16x32_bf16 v[16:19], v[124:127], v[132:135], v[16:19]\n"
    "ds_read_b128 v[96:99], v188 offset:3072\n"
    "v_mfma_f32_16x16x32_bf16 v[20:23], v[128:131], v[132:135], v[20:23]\n"
    "ds_read_b64_tr_b16 v[76:77], v240 offset:2048\n"
    "v_mfma_f32_16x16x32_bf16 v[24:27], v[108:111], v[136:139], v[24:27]\n"
    "ds_read_b64_tr_b16 v[78:79], v240 offset:2560\n"
    "v_mfma_f32_16x16x32_bf16 v[28:31], v[112:115], v[136:139], v[28:31]\n"
    "ds_read_b64_tr_b16 v[80:81], v240\n"
    "v_mfma_f32_16x16x32_bf16 v[32:35], v[116:119], v[136:139], v[32:35]\n"
    "ds_read_b64_tr_b16 v[82:83], v240 offset:512\n"
    "v_mfma_f32_16x16x32_bf16 v[36:39], v[120:123], v[136:139], v[36:39]\n"
    "ds_read_b64_tr_b16 v[84:85], v241 offset:1024\n"
    "v_mfma_f32_16x16x32_bf16 v[40:43], v[124:127], v[136:139], v[40:43]\n"
    "ds_read_b64_tr_b16 v[86:87], v241 offset:1536\n"
    "v_mfma_f32_16x16x32_bf16 v[44:47], v[128:131], v[136:139], v[44:47]\n"
    "ds_read_b64_tr_b16 v[88:89], v241 offset:2048\n"
    "v_mfma_f32_16x16x32_bf16 v[48:51], v[108:111], v[140:143], v[48:51]\n"
    "ds_read_b64_tr_b16 v[90:91], v241 offset:2560\n"
    "v_mfma_f32_16x16x32_bf16 v[52:55], v[112:115], v[140:143], v[52:55]\n"
    "ds_read_b64_tr_b16 v[92:93], v241\n"
    "v_mfma_f32_16x16x32_bf16 v[56:59], v[116:119], v[140:143], v[56:59]\n"
    "ds_read_b64_tr_b16 v[94:95], v241 offset:512\n"
    "v_mfma_f32_16x16x32_bf16 v[60:63], v[120:123], v[140:143], v[60:63]\n"
    "ds_read_b128 v[100:103], v188 offset:7168\n"
    "v_mfma_f32_16x16x32_bf16 v[64:67], v[124:127], v[140:143], v[64:67]\n"
    "ds_read_b128 v[104:107], v188 offset:8192\n"
    "v_mfma_f32_16x16x32_bf16 v[68:71], v[128:131], v[140:143], v[68:71]\n"
    "s_waitcnt lgkmcnt(9)\n"
    "v_mfma_f32_16x16x32_bf16 v[0:3], v[72:75], v[96:99], v[0:3]\n"
    "ds_read_b64_tr_b16 v[108:109], v240 offset:5120\n"
    "v_mfma_f32_16x16x32_bf16 v[4:7], v[76:79], v[96:99], v[4:7]\n"
    "ds_read_b64_tr_b16 v[110:111], v240 offset:5632\n"
    "s_waitcnt lgkmcnt(10)\n"
    "v_mfma_f32_16x16x32_bf16 v[8:11], v[80:83], v[96:99], v[8:11]\n"
    "ds_read_b128 v[132:135], v189 offset:3072\n"
    "s_waitcnt lgkmcnt(9)\n"
    "v_mfma_f32_16x16x32_bf16 v[12:15], v[84:87], v[96:99], v[12:15]\n"
    "ds_read_b64_tr_b16 v[112:113], v240 offset:6144\n"
    "s_waitcnt lgkmcnt(4)\n"
    "v_mfma_f32_16x16x32_bf16 v[16:19], v[88:91], v[96:99], v[16:19]\n"
    "ds_read_b64_tr_b16 v[114:115], v240 offset:6656\n"
    "v_mfma_f32_16x16x32_bf16 v[20:23], v[92:95], v[96:99], v[20:23]\n"
    "ds_read_b64_tr_b16 v[116:117], v240 offset:4096\n"
    "v_mfma_f32_16x16x32_bf16 v[24:27], v[72:75], v[100:103], v[24:27]\n"
    "ds_read_b64_tr_b16 v[118:119], v240 offset:4608\n"
    "v_mfma_f32_16x16x32_bf16 v[28:31], v[76:79], v[100:103], v[28:31]\n"
    "ds_read_b64_tr_b16 v[120:121], v241 offset:5120\n"
    "v_mfma_f32_16x16x32_bf16 v[32:35], v[80:83], v[100:103], v[32:35]\n"
    "ds_read_b64_tr_b16 v[122:123], v241 offset:5632\n"
    "v_mfma_f32_16x16x32_bf16 v[36:39], v[84:87], v[100:103], v[36:39]\n"
    "ds_read_b64_tr_b16 v[124:125], v241 offset:6144\n"
    "v_mfma_f32_16x16x32_bf16 v[40:43], v[88:91], v[100:103], v[40:43]\n"
    "ds_read_b64_tr_b16 v[126:127], v241 offset:6656\n"
    "v_mfma_f32_16x16x32_bf16 v[44:47], v[92:95], v[100:103], v[44:47]\n"
    "ds_read_b64_tr_b16 v[128:129], v241 offset:4096\n"
    "v_mfma_f32_16x16x32_bf16 v[48:51], v[72:75], v[104:107], v[48:51]\n"
    "ds_read_b64_tr_b16 v[130:131], v241 offset:4608\n"
    "v_mfma_f32_16x16x32_bf16 v[52:55], v[76:79], v[104:107], v[52:55]\n"
    "ds_read_b128 v[136:139], v189 offset:7168\n"
    "v_mfma_f32_16x16x32_bf16 v[56:59], v[80:83], v[104:107], v[56:59]\n"
    "ds_read_b128 v[140:143], v189 offset:8192\n"
    "v_mfma_f32_16x16x32_bf16 v[60:63], v[84:87], v[104:107], v[60:63]\n"
    "v_mfma_f32_16x16x32_bf16 v[64:67], v[88:91], v[104:107], v[64:67]\n"
    "v_mfma_f32_16x16x32_bf16 v[68:71], v[92:95], v[104:107], v[68:71]\n"
    "s_waitcnt lgkmcnt(6)\n"
    "v_mfma_f32_16x16x32_bf16 v[0:3], v[108:111], v[132:135], v[0:3]\n"
    "v_mfma_f32_16x16x32_bf16 v[4:7], v[112:115], v[132:135], v[4:7]\n"
    "v_mfma_f32_16x16x32_bf16 v[8:11], v[116:119], v[132:135], v[8:11]\n"
    "s_waitcnt lgkmcnt(0)\n"
    "s_barrier\n"
    "v_mfma_f32_16x16x32_bf16 v[12:15], v[120:123], v[132:135], v[12:15]\n"
    "ds_read_b64_tr_b16 v[72:73], v236 offset:1024\n"
    "ds_read_b64_tr_b16 v[74:75], v236 offset:1536\n"
    "v_mfma_f32_16x16x32_bf16 v[16:19], v[124:127], v[132:135], v[16:19]\n"
    "ds_read_b128 v[96:99], v184 offset:3072\n"
    "v_mfma_f32_16x16x32_bf16 v[20:23], v[128:131], v[132:135], v[20:23]\n"
    "ds_read_b64_tr_b16 v[76:77], v236 offset:2048\n"
    "v_mfma_f32_16x16x32_bf16 v[24:27], v[108:111], v[136:139], v[24:27]\n"
    "ds_read_b64_tr_b16 v[78:79], v236 offset:2560\n"
    "v_mfma_f32_16x16x32_bf16 v[28:31], v[112:115], v[136:139], v[28:31]\n"
    "ds_read_b64_tr_b16 v[80:81], v236\n"
    "v_mfma_f32_16x16x32_bf16 v[32:35], v[116:119], v[136:139], v[32:35]\n"
    "ds_read_b64_tr_b16 v[82:83], v236 offset:512\n"
    "v_mfma_f32_16x16x32_bf16 v[36:39], v[120:123], v[136:139], v[36:39]\n"
    "ds_read_b64_tr_b16 v[84:85], v237 offset:1024\n"
    "v_mfma_f32_16x16x32_bf16 v[40:43], v[124:127], v[136:139], v[40:43]\n"
    "ds_read_b64_tr_b16 v[86:87], v237 offset:1536\n"
    "v_mfma_f32_16x16x32_bf16 v[44:47], v[128:131], v[136:139], v[44:47]\n"
    "ds_read_b64_tr_b16 v[88:89], v237 offset:2048\n"
    "v_mfma_f32_16x16x32_bf16 v[48:51], v[108:111], v[140:143], v[48:51]\n"
    "ds_read_b64_tr_b16 v[90:91], v237 offset:2560\n"
    "v_mfma_f32_16x16x32_bf16 v[52:55], v[112:115], v[140:143], v[52:55]\n"
    "ds_read_b64_tr_b16 v[92:93], v237\n"
    "v_mfma_f32_16x16x32_bf16 v[56:59], v[116:119], v[140:143], v[56:59]\n"
    "ds_read_b64_tr_b16 v[94:95], v237 offset:512\n"
    "v_mfma_f32_16x16x32_bf16 v[60:63], v[120:123], v[140:143], v[60:63]\n"
    "ds_read_b128 v[100:103], v184 offset:7168\n"
    "v_mfma_f32_16x16x32_bf16 v[64:67], v[124:127], v[140:143], v[64:67]\n"
    "ds_read_b128 v[104:107], v184 offset:8192\n"
    "v_mfma_f32_16x16x32_bf16 v[68:71], v[128:131], v[140:143], v[68:71]\n"
    "s_waitcnt lgkmcnt(9)\n"
    "v_mfma_f32_16x16x32_bf16 v[0:3], v[72:75], v[96:99], v[0:3]\n"
    "ds_read_b64_tr_b16 v[108:109], v236 offset:5120\n"
    "v_mfma_f32_16x16x32_bf16 v[4:7], v[76:79], v[96:99], v[4:7]\n"
    "ds_read_b64_tr_b16 v[110:111], v236 offset:5632\n"
    "s_waitcnt lgkmcnt(10)\n"
    "v_mfma_f32_16x16x32_bf16 v[8:11], v[80:83], v[96:99], v[8:11]\n"
    "ds_read_b128 v[132:135], v185 offset:3072\n"
    "s_waitcnt lgkmcnt(9)\n"
    "v_mfma_f32_16x16x32_bf16 v[12:15], v[84:87], v[96:99], v[12:15]\n"
    "ds_read_b64_tr_b16 v[112:113], v236 offset:6144\n"
    "s_waitcnt lgkmcnt(4)\n"
    "v_mfma_f32_16x16x32_bf16 v[16:19], v[88:91], v[96:99], v[16:19]\n"
    "ds_read_b64_tr_b16 v[114:115], v236 offset:6656\n"
    "v_mfma_f32_16x16x32_bf16 v[20:23], v[92:95], v[96:99], v[20:23]\n"
    "ds_read_b64_tr_b16 v[116:117], v236 offset:4096\n"
    "v_mfma_f32_16x16x32_bf16 v[24:27], v[72:75], v[100:103], v[24:27]\n"
    "ds_read_b64_tr_b16 v[118:119], v236 offset:4608\n"
    "v_mfma_f32_16x16x32_bf16 v[28:31], v[76:79], v[100:103], v[28:31]\n"
    "ds_read_b64_tr_b16 v[120:121], v237 offset:5120\n"
    "v_mfma_f32_16x16x32_bf16 v[32:35], v[80:83], v[100:103], v[32:35]\n"
    "ds_read_b64_tr_b16 v[122:123], v237 offset:5632\n"
    "v_mfma_f32_16x16x32_bf16 v[36:39], v[84:87], v[100:103], v[36:39]\n"
    "ds_read_b64_tr_b16 v[124:125], v237 offset:6144\n"
    "v_mfma_f32_16x16x32_bf16 v[40:43], v[88:91], v[100:103], v[40:43]\n"
    "ds_read_b64_tr_b16 v[126:127], v237 offset:6656\n"
    "v_mfma_f32_16x16x32_bf16 v[44:47], v[92:95], v[100:103], v[44:47]\n"
    "ds_read_b64_tr_b16 v[128:129], v237 offset:4096\n"
    "v_mfma_f32_16x16x32_bf16 v[48:51], v[72:75], v[104:107], v[48:51]\n"
    "ds_read_b64_tr_b16 v[130:131], v237 offset:4608\n"
    "v_mfma_f32_16x16x32_bf16 v[52:55], v[76:79], v[104:107], v[52:55]\n"
    "ds_read_b128 v[136:139], v185 offset:7168\n"
    "v_mfma_f32_16x16x32_bf16 v[56:59], v[80:83], v[104:107], v[56:59]\n"
    "ds_read_b128 v[140:143], v185 offset:8192\n"
    "v_mfma_f32_16x16x32_bf16 v[60:63], v[84:87], v[104:107], v[60:63]\n"
    "v_mfma_f32_16x16x32_bf16 v[64:67], v[88:91], v[104:107], v[64:67]\n"
    "v_mfma_f32_16x16x32_bf16 v[68:71], v[92:95], v[104:107], v[68:71]\n"
    "s_waitcnt lgkmcnt(6)\n"
    "v_mfma_f32_16x16x32_bf16 v[0:3], v[108:111], v[132:135], v[0:3]\n"
    "v_mfma_f32_16x16x32_bf16 v[4:7], v[112:115], v[132:135], v[4:7]\n"
    "v_mfma_f32_16x16x32_bf16 v[8:11], v[116:119], v[132:135], v[8:11]\n"
    "s_waitcnt lgkmcnt(0)\n"
    "s_barrier\n"
    "v_mfma_f32_16x16x32_bf16 v[12:15], v[120:123], v[132:135], v[12:15]\n"
    "ds_read_b64_tr_b16 v[72:73], v238 offset:1024\n"
    "ds_read_b64_tr_b16 v[74:75], v238 offset:1536\n"
    "v_mfma_f32_16x16x32_bf16 v[16:19], v[124:127], v[132:135], v[16:19]\n"
    "ds_read_b128 v[96:99], v186 offset:3072\n"
    "v_mfma_f32_16x16x32_bf16 v[20:23], v[128:131], v[132:135], v[20:23]\n"
    "ds_read_b64_tr_b16 v[76:77], v238 offset:2048\n"
    "v_mfma_f32_16x16x32_bf16 v[24:27], v[108:111], v[136:139], v[24:27]\n"
    "ds_read_b64_tr_b16 v[78:79], v238 offset:2560\n"
    "v_mfma_f32_16x16x32_bf16 v[28:31], v[112:115], v[136:139], v[28:31]\n"
    "ds_read_b64_tr_b16 v[80:81], v238\n"
    "v_mfma_f32_16x16x32_bf16 v[32:35], v[116:119], v[136:139], v[32:35]\n"
    "ds_read_b64_tr_b16 v[82:83], v238 offset:512\n"
    "v_mfma_f32_16x16x32_bf16 v[36:39], v[120:123], v[136:139], v[36:39]\n"
    "ds_read_b64_tr_b16 v[84:85], v239 offset:1024\n"
    "v_mfma_f32_16x16x32_bf16 v[40:43], v[124:127], v[136:139], v[40:43]\n"
    "ds_read_b64_tr_b16 v[86:87], v239 offset:1536\n"
    "v_mfma_f32_16x16x32_bf16 v[44:47], v[128:131], v[136:139], v[44:47]\n"
    "ds_read_b64_tr_b16 v[88:89], v239 offset:2048\n"
    "v_mfma_f32_16x16x32_bf16 v[48:51], v[108:111], v[140:143], v[48:51]\n"
    "ds_read_b64_tr_b16 v[90:91], v239 offset:2560\n"
    "v_mfma_f32_16x16x32_bf16 v[52:55], v[112:115], v[140:143], v[52:55]\n"
    "ds_read_b64_tr_b16 v[92:93], v239\n"
    "v_mfma_f32_16x16x32_bf16 v[56:59], v[116:119], v[140:143], v[56:59]\n"
    "ds_read_b64_tr_b16 v[94:95], v239 offset:512\n"
    "v_mfma_f32_16x16x32_bf16 v[60:63], v[120:123], v[140:143], v[60:63]\n"
    "ds_read_b128 v[100:103], v186 offset:7168\n"
    "v_mfma_f32_16x16x32_bf16 v[64:67], v[124:127], v[140:143], v[64:67]\n"
    "ds_read_b128 v[104:107], v186 offset:8192\n"
    "v_mfma_f32_16x16x32_bf16 v[68:71], v[128:131], v[140:143], v[68:71]\n"
    "s_waitcnt lgkmcnt(9)\n"
    "v_mfma_f32_16x16x32_bf16 v[0:3], v[72:75], v[96:99], v[0:3]\n"
    "ds_read_b64_tr_b16 v[108:109], v238 offset:5120\n"
    "v_mfma_f32_16x16x32_bf16 v[4:7], v[76:79], v[96:99], v[4:7]\n"
    "ds_read_b64_tr_b16 v[110:111], v238 offset:5632\n"
    "s_waitcnt lgkmcnt(10)\n"
    "v_mfma_f32_16x16x32_bf16 v[8:11], v[80:83], v[96:99], v[8:11]\n"
    "ds_read_b128 v[132:135], v187 offset:3072\n"
    "s_waitcnt lgkmcnt(9)\n"
    "v_mfma_f32_16x16x32_bf16 v[12:15], v[84:87], v[96:99], v[12:15]\n"
    "ds_read_b64_tr_b16 v[112:113], v238 offset:6144\n"
    "s_waitcnt lgkmcnt(4)\n"
    "v_mfma_f32_16x16x32_bf16 v[16:19], v[88:91], v[96:99], v[16:19]\n"
    "ds_read_b64_tr_b16 v[114:115], v238 offset:6656\n"
    "v_mfma_f32_16x16x32_bf16 v[20:23], v[92:95], v[96:99], v[20:23]\n"
    "ds_read_b64_tr_b16 v[116:117], v238 offset:4096\n"
    "v_mfma_f32_16x16x32_bf16 v[24:27], v[72:75], v[100:103], v[24:27]\n"
    "ds_read_b64_tr_b16 v[118:119], v238 offset:4608\n"
    "v_mfma_f32_16x16x32_bf16 v[28:31], v[76:79], v[100:103], v[28:31]\n"
    "ds_read_b64_tr_b16 v[120:121], v239 offset:5120\n"
    "v_mfma_f32_16x16x32_bf16 v[32:35], v[80:83], v[100:103], v[32:35]\n"
    "ds_read_b64_tr_b16 v[122:123], v239 offset:5632\n"
    "v_mfma_f32_16x16x32_bf16 v[36:39], v[84:87], v[100:103], v[36:39]\n"
    "ds_read_b64_tr_b16 v[124:125], v239 offset:6144\n"
    "v_mfma_f32_16x16x32_bf16 v[40:43], v[88:91], v[100:103], v[40:43]\n"
    "ds_read_b64_tr_b16 v[126:127], v239 offset:6656\n"
    "v_mfma_f32_16x16x32_bf16 v[44:47], v[92:95], v[100:103], v[44:47]\n"
    "ds_read_b64_tr_b16 v[128:129], v239 offset:4096\n"
    "v_mfma_f32_16x16x32_bf16 v[48:51], v[72:75], v[104:107], v[48:51]\n"
    "ds_read_b64_tr_b16 v[130:131], v239 offset:4608\n"
    "v_mfma_f32_16x16x32_bf16 v[52:55], v[76:79], v[104:107], v[52:55]\n"
    "ds_read_b128 v[136:139], v187 offset:7168\n"
    "v_mfma_f32_16x16x32_bf16 v[56:59], v[80:83], v[104:107], v[56:59]\n"
    "ds_read_b128 v[140:143], v187 offset:8192\n"
    "v_mfma_f32_16x16x32_bf16 v[60:63], v[84:87], v[104:107], v[60:63]\n"
    "v_mfma_f32_16x16x32_bf16 v[64:67], v[88:91], v[104:107], v[64:67]\n"
    "v_mfma_f32_16x16x32_bf16 v[68:71], v[92:95], v[104:107], v[68:71]\n"
    "s_waitcnt lgkmcnt(6)\n"
    "v_mfma_f32_16x16x32_bf16 v[0:3], v[108:111], v[132:135], v[0:3]\n"
    "v_mfma_f32_16x16x32_bf16 v[4:7], v[112:115], v[132:135], v[4:7]\n"
    "v_mfma_f32_16x16x32_bf16 v[8:11], v[116:119], v[132:135], v[8:11]\n"
    "s_waitcnt lgkmcnt(0)\n"
    "s_barrier\n"
    "v_mfma_f32_16x16x32_bf16 v[12:15], v[120:123], v[132:135], v[12:15]\n"
    "ds_read_b64_tr_b16 v[72:73], v240 offset:1024\n"
    "ds_read_b64_tr_b16 v[74:75], v240 offset:1536\n"
    "v_mfma_f32_16x16x32_bf16 v[16:19], v[124:127], v[132:135], v[16:19]\n"
    "ds_read_b128 v[96:99], v188 offset:3072\n"
    "v_mfma_f32_16x16x32_bf16 v[20:23], v[128:131], v[132:135], v[20:23]\n"
    "ds_read_b64_tr_b16 v[76:77], v240 offset:2048\n"
    "v_mfma_f32_16x16x32_bf16 v[24:27], v[108:111], v[136:139], v[24:27]\n"
    "ds_read_b64_tr_b16 v[78:79], v240 offset:2560\n"
    "v_mfma_f32_16x16x32_bf16 v[28:31], v[112:115], v[136:139], v[28:31]\n"
    "ds_read_b64_tr_b16 v[80:81], v240\n"
    "v_mfma_f32_16x16x32_bf16 v[32:35], v[116:119], v[136:139], v[32:35]\n"
    "ds_read_b64_tr_b16 v[82:83], v240 offset:512\n"
    "v_mfma_f32_16x16x32_bf16 v[36:39], v[120:123], v[136:139], v[36:39]\n"
    "ds_read_b64_tr_b16 v[84:85], v241 offset:1024\n"
    "v_mfma_f32_16x16x32_bf16 v[40:43], v[124:127], v[136:139], v[40:43]\n"
    "ds_read_b64_tr_b16 v[86:87], v241 offset:1536\n"
    "v_mfma_f32_16x16x32_bf16 v[44:47], v[128:131], v[136:139], v[44:47]\n"
    "ds_read_b64_tr_b16 v[88:89], v241 offset:2048\n"
    "v_mfma_f32_16x16x32_bf16 v[48:51], v[108:111], v[140:143], v[48:51]\n"
    "ds_read_b64_tr_b16 v[90:91], v241 offset:2560\n"
    "v_mfma_f32_16x16x32_bf16 v[52:55], v[112:115], v[140:143], v[52:55]\n"
    "ds_read_b64_tr_b16 v[92:93], v241\n"
    "v_mfma_f32_16x16x32_bf16 v[56:59], v[116:119], v[140:143], v[56:59]\n"
    "ds_read_b64_tr_b16 v[94:95], v241 offset:512\n"
    "v_mfma_f32_16x16x32_bf16 v[60:63], v[120:123], v[140:143], v[60:63]\n"
    "ds_read_b128 v[100:103], v188 offset:7168\n"
    "v_mfma_f32_16x16x32_bf16 v[64:67], v[124:127], v[140:143], v[64:67]\n"
    "ds_read_b128 v[104:107], v188 offset:8192\n"
    "v_mfma_f32_16x16x32_bf16 v[68:71], v[128:131], v[140:143], v[68:71]\n"
    "s_waitcnt lgkmcnt(9)\n"
    "v_mfma_f32_16x16x32_bf16 v[0:3], v[72:75], v[96:99], v[0:3]\n"
    "ds_read_b64_tr_b16 v[108:109], v240 offset:5120\n"
    "v_mfma_f32_16x16x32_bf16 v[4:7], v[76:79], v[96:99], v[4:7]\n"
    "ds_read_b64_tr_b16 v[110:111], v240 offset:5632\n"
    "s_waitcnt lgkmcnt(10)\n"
    "v_mfma_f32_16x16x32_bf16 v[8:11], v[80:83], v[96:99], v[8:11]\n"
    "ds_read_b128 v[132:135], v189 offset:3072\n"
    "s_waitcnt lgkmcnt(9)\n"
    "v_mfma_f32_16x16x32_bf16 v[12:15], v[84:87], v[96:99], v[12:15]\n"
    "ds_read_b64_tr_b16 v[112:113], v240 offset:6144\n"
    "s_waitcnt lgkmcnt(4)\n"
    "v_mfma_f32_16x16x32_bf16 v[16:19], v[88:91], v[96:99], v[16:19]\n"
    "ds_read_b64_tr_b16 v[114:115], v240 offset:6656\n"
    "v_mfma_f32_16x16x32_bf16 v[20:23], v[92:95], v[96:99], v[20:23]\n"
    "ds_read_b64_tr_b16 v[116:117], v240 offset:4096\n"
    "v_mfma_f32_16x16x32_bf16 v[24:27], v[72:75], v[100:103], v[24:27]\n"
    "ds_read_b64_tr_b16 v[118:119], v240 offset:4608\n"
    "v_mfma_f32_16x16x32_bf16 v[28:31], v[76:79], v[100:103], v[28:31]\n"
    "ds_read_b64_tr_b16 v[120:121], v241 offset:5120\n"
    "v_mfma_f32_16x16x32_bf16 v[32:35], v[80:83], v[100:103], v[32:35]\n"
    "ds_read_b64_tr_b16 v[122:123], v241 offset:5632\n"
    "v_mfma_f32_16x16x32_bf16 v[36:39], v[84:87], v[100:103], v[36:39]\n"
    "ds_read_b64_tr_b16 v[124:125], v241 offset:6144\n"
    "v_mfma_f32_16x16x32_bf16 v[40:43], v[88:91], v[100:103], v[40:43]\n"
    "ds_read_b64_tr_b16 v[126:127], v241 offset:6656\n"
    "v_mfma_f32_16x16x32_bf16 v[44:47], v[92:95], v[100:103], v[44:47]\n"
    "ds_read_b64_tr_b16 v[128:129], v241 offset:4096\n"
    "v_mfma_f32_16x16x32_bf16 v[48:51], v[72:75], v[104:107], v[48:51]\n"
    "ds_read_b64_tr_b16 v[130:131], v241 offset:4608\n"
    "v_mfma_f32_16x16x32_bf16 v[52:55], v[76:79], v[104:107], v[52:55]\n"
    "ds_read_b128 v[136:139], v189 offset:7168\n"
    "v_mfma_f32_16x16x32_bf16 v[56:59], v[80:83], v[104:107], v[56:59]\n"
    "ds_read_b128 v[140:143], v189 offset:8192\n"
    "v_mfma_f32_16x16x32_bf16 v[60:63], v[84:87], v[104:107], v[60:63]\n"
    "v_mfma_f32_16x16x32_bf16 v[64:67], v[88:91], v[104:107], v[64:67]\n"
    "v_mfma_f32_16x16x32_bf16 v[68:71], v[92:95], v[104:107], v[68:71]\n"
    "s_waitcnt lgkmcnt(6)\n"
    "v_mfma_f32_16x16x32_bf16 v[0:3], v[108:111], v[132:135], v[0:3]\n"
    "v_mfma_f32_16x16x32_bf16 v[4:7], v[112:115], v[132:135], v[4:7]\n"
    "v_mfma_f32_16x16x32_bf16 v[8:11], v[116:119], v[132:135], v[8:11]\n"
    "s_waitcnt lgkmcnt(0)\n"
    "s_barrier\n"
    "v_mfma_f32_16x16x32_bf16 v[12:15], v[120:123], v[132:135], v[12:15]\n"
    "ds_read_b64_tr_b16 v[72:73], v236 offset:1024\n"
    "ds_read_b64_tr_b16 v[74:75], v236 offset:1536\n"
    "v_mfma_f32_16x16x32_bf16 v[16:19], v[124:127], v[132:135], v[16:19]\n"
    "ds_read_b128 v[96:99], v184 offset:3072\n"
    "v_mfma_f32_16x16x32_bf16 v[20:23], v[128:131], v[132:135], v[20:23]\n"
    "ds_read_b64_tr_b16 v[76:77], v236 offset:2048\n"
    "v_mfma_f32_16x16x32_bf16 v[24:27], v[108:111], v[136:139], v[24:27]\n"
    "ds_read_b64_tr_b16 v[78:79], v236 offset:2560\n"
    "v_mfma_f32_16x16x32_bf16 v[28:31], v[112:115], v[136:139], v[28:31]\n"
    "ds_read_b64_tr_b16 v[80:81], v236\n"
    "v_mfma_f32_16x16x32_bf16 v[32:35], v[116:119], v[136:139], v[32:35]\n"
    "ds_read_b64_tr_b16 v[82:83], v236 offset:512\n"
    "v_mfma_f32_16x16x32_bf16 v[36:39], v[120:123], v[136:139], v[36:39]\n"
    "ds_read_b64_tr_b16 v[84:85], v237 offset:1024\n"
    "v_mfma_f32_16x16x32_bf16 v[40:43], v[124:127], v[136:139], v[40:43]\n"
    "ds_read_b64_tr_b16 v[86:87], v237 offset:1536\n"
    "v_mfma_f32_16x16x32_bf16 v[44:47], v[128:131], v[136:139], v[44:47]\n"
    "ds_read_b64_tr_b16 v[88:89], v237 offset:2048\n"
    "v_mfma_f32_16x16x32_bf16 v[48:51], v[108:111], v[140:143], v[48:51]\n"
    "ds_read_b64_tr_b16 v[90:91], v237 offset:2560\n"
    "v_mfma_f32_16x16x32_bf16 v[52:55], v[112:115], v[140:143], v[52:55]\n"
    "ds_read_b64_tr_b16 v[92:93], v237\n"
    "v_mfma_f32_16x16x32_bf16 v[56:59], v[116:119], v[140:143], v[56:59]\n"
    "ds_read_b64_tr_b16 v[94:95], v237 offset:512\n"
    "v_mfma_f32_16x16x32_bf16 v[60:63], v[120:123], v[140:143], v[60:63]\n"
    "ds_read_b128 v[100:103], v184 offset:7168\n"
    "v_mfma_f32_16x16x32_bf16 v[64:67], v[124:127], v[140:143], v[64:67]\n"
    "ds_read_b128 v[104:107], v184 offset:8192\n"
    "v_mfma_f32_16x16x32_bf16 v[68:71], v[128:131], v[140:143], v[68:71]\n"
    "s_waitcnt lgkmcnt(9)\n"
    "v_mfma_f32_16x16x32_bf16 v[0:3], v[72:75], v[96:99], v[0:3]\n"
    "ds_read_b64_tr_b16 v[108:109], v236 offset:5120\n"
    "v_mfma_f32_16x16x32_bf16 v[4:7], v[76:79], v[96:99], v[4:7]\n"
    "ds_read_b64_tr_b16 v[110:111], v236 offset:5632\n"
    "s_waitcnt lgkmcnt(10)\n"
    "v_mfma_f32_16x16x32_bf16 v[8:11], v[80:83], v[96:99], v[8:11]\n"
    "ds_read_b128 v[132:135], v185 offset:3072\n"
    "s_waitcnt lgkmcnt(9)\n"
    "v_mfma_f32_16x16x32_bf16 v[12:15], v[84:87], v[96:99], v[12:15]\n"
    "ds_read_b64_tr_b16 v[112:113], v236 offset:6144\n"
    "s_waitcnt lgkmcnt(4)\n"
    "v_mfma_f32_16x16x32_bf16 v[16:19], v[88:91], v[96:99], v[16:19]\n"
    "ds_read_b64_tr_b16 v[114:115], v236 offset:6656\n"
    "v_mfma_f32_16x16x32_bf16 v[20:23], v[92:95], v[96:99], v[20:23]\n"
    "ds_read_b64_tr_b16 v[116:117], v236 offset:4096\n"
    "v_mfma_f32_16x16x32_bf16 v[24:27], v[72:75], v[100:103], v[24:27]\n"
    "ds_read_b64_tr_b16 v[118:119], v236 offset:4608\n"
    "v_mfma_f32_16x16x32_bf16 v[28:31], v[76:79], v[100:103], v[28:31]\n"
    "ds_read_b64_tr_b16 v[120:121], v237 offset:5120\n"
    "v_mfma_f32_16x16x32_bf16 v[32:35], v[80:83], v[100:103], v[32:35]\n"
    "ds_read_b64_tr_b16 v[122:123], v237 offset:5632\n"
    "v_mfma_f32_16x16x32_bf16 v[36:39], v[84:87], v[100:103], v[36:39]\n"
    "ds_read_b64_tr_b16 v[124:125], v237 offset:6144\n"
    "v_mfma_f32_16x16x32_bf16 v[40:43], v[88:91], v[100:103], v[40:43]\n"
    "ds_read_b64_tr_b16 v[126:127], v237 offset:6656\n"
    "v_mfma_f32_16x16x32_bf16 v[44:47], v[92:95], v[100:103], v[44:47]\n"
    "ds_read_b64_tr_b16 v[128:129], v237 offset:4096\n"
    "v_mfma_f32_16x16x32_bf16 v[48:51], v[72:75], v[104:107], v[48:51]\n"
    "ds_read_b64_tr_b16 v[130:131], v237 offset:4608\n"
    "v_mfma_f32_16x16x32_bf16 v[52:55], v[76:79], v[104:107], v[52:55]\n"
    "ds_read_b128 v[136:139], v185 offset:7168\n"
    "v_mfma_f32_16x16x32_bf16 v[56:59], v[80:83], v[104:107], v[56:59]\n"
    "ds_read_b128 v[140:143], v185 offset:8192\n"
    "v_mfma_f32_16x16x32_bf16 v[60:63], v[84:87], v[104:107], v[60:63]\n"
    "v_mfma_f32_16x16x32_bf16 v[64:67], v[88:91], v[104:107], v[64:67]\n"
    "v_mfma_f32_16x16x32_bf16 v[68:71], v[92:95], v[104:107], v[68:71]\n"
    "s_waitcnt lgkmcnt(6)\n"
    "v_mfma_f32_16x16x32_bf16 v[0:3], v[108:111], v[132:135], v[0:3]\n"
    "v_mfma_f32_16x16x32_bf16 v[4:7], v[112:115], v[132:135], v[4:7]\n"
    "v_mfma_f32_16x16x32_bf16 v[8:11], v[116:119], v[132:135], v[8:11]\n"
    "s_waitcnt lgkmcnt(0)\n"
    "s_barrier\n"
    "v_mfma_f32_16x16x32_bf16 v[12:15], v[120:123], v[132:135], v[12:15]\n"
    "ds_read_b64_tr_b16 v[72:73], v238 offset:1024\n"
    "ds_read_b64_tr_b16 v[74:75], v238 offset:1536\n"
    "v_mfma_f32_16x16x32_bf16 v[16:19], v[124:127], v[132:135], v[16:19]\n"
    "ds_read_b128 v[96:99], v186 offset:3072\n"
    "v_mfma_f32_16x16x32_bf16 v[20:23], v[128:131], v[132:135], v[20:23]\n"
    "ds_read_b64_tr_b16 v[76:77], v238 offset:2048\n"
    "v_mfma_f32_16x16x32_bf16 v[24:27], v[108:111], v[136:139], v[24:27]\n"
    "ds_read_b64_tr_b16 v[78:79], v238 offset:2560\n"
    "v_mfma_f32_16x16x32_bf16 v[28:31], v[112:115], v[136:139], v[28:31]\n"
    "ds_read_b64_tr_b16 v[80:81], v238\n"
    "v_mfma_f32_16x16x32_bf16 v[32:35], v[116:119], v[136:139], v[32:35]\n"
    "ds_read_b64_tr_b16 v[82:83], v238 offset:512\n"
    "v_mfma_f32_16x16x32_bf16 v[36:39], v[120:123], v[136:139], v[36:39]\n"
    "ds_read_b64_tr_b16 v[84:85], v239 offset:1024\n"
    "v_mfma_f32_16x16x32_bf16 v[40:43], v[124:127], v[136:139], v[40:43]\n"
    "ds_read_b64_tr_b16 v[86:87], v239 offset:1536\n"
    "v_mfma_f32_16x16x32_bf16 v[44:47], v[128:131], v[136:139], v[44:47]\n"
    "ds_read_b64_tr_b16 v[88:89], v239 offset:2048\n"
    "v_mfma_f32_16x16x32_bf16 v[48:51], v[108:111], v[140:143], v[48:51]\n"
    "ds_read_b64_tr_b16 v[90:91], v239 offset:2560\n"
    "v_mfma_f32_16x16x32_bf16 v[52:55], v[112:115], v[140:143], v[52:55]\n"
    "ds_read_b64_tr_b16 v[92:93], v239\n"
    "v_mfma_f32_16x16x32_bf16 v[56:59], v[116:119], v[140:143], v[56:59]\n"
    "ds_read_b64_tr_b16 v[94:95], v239 offset:512\n"
    "v_mfma_f32_16x16x32_bf16 v[60:63], v[120:123], v[140:143], v[60:63]\n"
    "ds_read_b128 v[100:103], v186 offset:7168\n"
    "v_mfma_f32_16x16x32_bf16 v[64:67], v[124:127], v[140:143], v[64:67]\n"
    "ds_read_b128 v[104:107], v186 offset:8192\n"
    "v_mfma_f32_16x16x32_bf16 v[68:71], v[128:131], v[140:143], v[68:71]\n"
    "s_waitcnt lgkmcnt(9)\n"
    "v_mfma_f32_16x16x32_bf16 v[0:3], v[72:75], v[96:99], v[0:3]\n"
    "ds_read_b64_tr_b16 v[108:109], v238 offset:5120\n"
    "v_mfma_f32_16x16x32_bf16 v[4:7], v[76:79], v[96:99], v[4:7]\n"
    "ds_read_b64_tr_b16 v[110:111], v238 offset:5632\n"
    "s_waitcnt lgkmcnt(10)\n"
    "v_mfma_f32_16x16x32_bf16 v[8:11], v[80:83], v[96:99], v[8:11]\n"
    "ds_read_b128 v[132:135], v187 offset:3072\n"
    "s_waitcnt lgkmcnt(9)\n"
    "v_mfma_f32_16x16x32_bf16 v[12:15], v[84:87], v[96:99], v[12:15]\n"
    "ds_read_b64_tr_b16 v[112:113], v238 offset:6144\n"
    "s_waitcnt lgkmcnt(4)\n"
    "v_mfma_f32_16x16x32_bf16 v[16:19], v[88:91], v[96:99], v[16:19]\n"
    "ds_read_b64_tr_b16 v[114:115], v238 offset:6656\n"
    "v_mfma_f32_16x16x32_bf16 v[20:23], v[92:95], v[96:99], v[20:23]\n"
    "ds_read_b64_tr_b16 v[116:117], v238 offset:4096\n"
    "v_mfma_f32_16x16x32_bf16 v[24:27], v[72:75], v[100:103], v[24:27]\n"
    "ds_read_b64_tr_b16 v[118:119], v238 offset:4608\n"
    "v_mfma_f32_16x16x32_bf16 v[28:31], v[76:79], v[100:103], v[28:31]\n"
    "ds_read_b64_tr_b16 v[120:121], v239 offset:5120\n"
    "v_mfma_f32_16x16x32_bf16 v[32:35], v[80:83], v[100:103], v[32:35]\n"
    "ds_read_b64_tr_b16 v[122:123], v239 offset:5632\n"
    "v_mfma_f32_16x16x32_bf16 v[36:39], v[84:87], v[100:103], v[36:39]\n"
    "ds_read_b64_tr_b16 v[124:125], v239 offset:6144\n"
    "v_mfma_f32_16x16x32_bf16 v[40:43], v[88:91], v[100:103], v[40:43]\n"
    "ds_read_b64_tr_b16 v[126:127], v239 offset:6656\n"
    "v_mfma_f32_16x16x32_bf16 v[44:47], v[92:95], v[100:103], v[44:47]\n"
    "ds_read_b64_tr_b16 v[128:129], v239 offset:4096\n"
    "v_mfma_f32_16x16x32_bf16 v[48:51], v[72:75], v[104:107], v[48:51]\n"
    "ds_read_b64_tr_b16 v[130:131], v239 offset:4608\n"
    "v_mfma_f32_16x16x32_bf16 v[52:55], v[76:79], v[104:107], v[52:55]\n"
    "ds_read_b128 v[136:139], v187 offset:7168\n"
    "v_mfma_f32_16x16x32_bf16 v[56:59], v[80:83], v[104:107], v[56:59]\n"
    "ds_read_b128 v[140:143], v187 offset:8192\n"
    "v_mfma_f32_16x16x32_bf16 v[60:63], v[84:87], v[104:107], v[60:63]\n"
    "v_mfma_f32_16x16x32_bf16 v[64:67], v[88:91], v[104:107], v[64:67]\n"
    "v_mfma_f32_16x16x32_bf16 v[68:71], v[92:95], v[104:107], v[68:71]\n"
    "s_waitcnt lgkmcnt(6)\n"
    "v_mfma_f32_16x16x32_bf16 v[0:3], v[108:111], v[132:135], v[0:3]\n"
    "v_mfma_f32_16x16x32_bf16 v[4:7], v[112:115], v[132:135], v[4:7]\n"
    "v_mfma_f32_16x16x32_bf16 v[8:11], v[116:119], v[132:135], v[8:11]\n"
    "s_cmp_lg_u32 s69, 3\n"
    "s_cbranch_scc1 NVM%=\n"
    "s_waitcnt vmcnt(0)\n"
    "NVM%=:\n"
    "s_waitcnt lgkmcnt(0)\n"
    "s_barrier\n"
    "v_mfma_f32_16x16x32_bf16 v[12:15], v[120:123], v[132:135], v[12:15]\n"
    "ds_read_b64_tr_b16 v[72:73], v240 offset:1024\n"
    "ds_read_b64_tr_b16 v[74:75], v240 offset:1536\n"
    "v_mfma_f32_16x16x32_bf16 v[16:19], v[124:127], v[132:135], v[16:19]\n"
    "ds_read_b128 v[96:99], v188 offset:3072\n"
    "v_mfma_f32_16x16x32_bf16 v[20:23], v[128:131], v[132:135], v[20:23]\n"
    "ds_read_b64_tr_b16 v[76:77], v240 offset:2048\n"
    "v_mfma_f32_16x16x32_bf16 v[24:27], v[108:111], v[136:139], v[24:27]\n"
    "ds_read_b64_tr_b16 v[78:79], v240 offset:2560\n"
    "v_mfma_f32_16x16x32_bf16 v[28:31], v[112:115], v[136:139], v[28:31]\n"
    "ds_read_b64_tr_b16 v[80:81], v240\n"
    "v_mfma_f32_16x16x32_bf16 v[32:35], v[116:119], v[136:139], v[32:35]\n"
    "ds_read_b64_tr_b16 v[82:83], v240 offset:512\n"
    "v_mfma_f32_16x16x32_bf16 v[36:39], v[120:123], v[136:139], v[36:39]\n"
    "ds_read_b64_tr_b16 v[84:85], v241 offset:1024\n"
    "v_mfma_f32_16x16x32_bf16 v[40:43], v[124:127], v[136:139], v[40:43]\n"
    "ds_read_b64_tr_b16 v[86:87], v241 offset:1536\n"
    "v_mfma_f32_16x16x32_bf16 v[44:47], v[128:131], v[136:139], v[44:47]\n"
    "ds_read_b64_tr_b16 v[88:89], v241 offset:2048\n"
    "v_mfma_f32_16x16x32_bf16 v[48:51], v[108:111], v[140:143], v[48:51]\n"
    "ds_read_b64_tr_b16 v[90:91], v241 offset:2560\n"
    "v_mfma_f32_16x16x32_bf16 v[52:55], v[112:115], v[140:143], v[52:55]\n"
    "ds_read_b64_tr_b16 v[92:93], v241\n"
    "v_mfma_f32_16x16x32_bf16 v[56:59], v[116:119], v[140:143], v[56:59]\n"
    "ds_read_b64_tr_b16 v[94:95], v241 offset:512\n"
    "v_mfma_f32_16x16x32_bf16 v[60:63], v[120:123], v[140:143], v[60:63]\n"
    "ds_read_b128 v[100:103], v188 offset:7168\n"
    "v_mfma_f32_16x16x32_bf16 v[64:67], v[124:127], v[140:143], v[64:67]\n"
    "ds_read_b128 v[104:107], v188 offset:8192\n"
    "v_mfma_f32_16x16x32_bf16 v[68:71], v[128:131], v[140:143], v[68:71]\n"
    "s_waitcnt lgkmcnt(9)\n"
    "v_mfma_f32_16x16x32_bf16 v[0:3], v[72:75], v[96:99], v[0:3]\n"
    "ds_read_b64_tr_b16 v[108:109], v240 offset:5120\n"
    "v_mfma_f32_16x16x32_bf16 v[4:7], v[76:79], v[96:99], v[4:7]\n"
    "ds_read_b64_tr_b16 v[110:111], v240 offset:5632\n"
    "s_waitcnt lgkmcnt(10)\n"
    "v_mfma_f32_16x16x32_bf16 v[8:11], v[80:83], v[96:99], v[8:11]\n"
    "ds_read_b128 v[132:135], v189 offset:3072\n"
    "s_waitcnt lgkmcnt(9)\n"
    "v_mfma_f32_16x16x32_bf16 v[12:15], v[84:87], v[96:99], v[12:15]\n"
    "ds_read_b64_tr_b16 v[112:113], v240 offset:6144\n"
    "s_waitcnt lgkmcnt(4)\n"
    "v_mfma_f32_16x16x32_bf16 v[16:19], v[88:91], v[96:99], v[16:19]\n"
    "ds_read_b64_tr_b16 v[114:115], v240 offset:6656\n"
    "v_mfma_f32_16x16x32_bf16 v[20:23], v[92:95], v[96:99], v[20:23]\n"
    "ds_read_b64_tr_b16 v[116:117], v240 offset:4096\n"
    "v_mfma_f32_16x16x32_bf16 v[24:27], v[72:75], v[100:103], v[24:27]\n"
    "ds_read_b64_tr_b16 v[118:119], v240 offset:4608\n"
    "v_mfma_f32_16x16x32_bf16 v[28:31], v[76:79], v[100:103], v[28:31]\n"
    "ds_read_b64_tr_b16 v[120:121], v241 offset:5120\n"
    "v_mfma_f32_16x16x32_bf16 v[32:35], v[80:83], v[100:103], v[32:35]\n"
    "ds_read_b64_tr_b16 v[122:123], v241 offset:5632\n"
    "v_mfma_f32_16x16x32_bf16 v[36:39], v[84:87], v[100:103], v[36:39]\n"
    "ds_read_b64_tr_b16 v[124:125], v241 offset:6144\n"
    "v_mfma_f32_16x16x32_bf16 v[40:43], v[88:91], v[100:103], v[40:43]\n"
    "ds_read_b64_tr_b16 v[126:127], v241 offset:6656\n"
    "v_mfma_f32_16x16x32_bf16 v[44:47], v[92:95], v[100:103], v[44:47]\n"
    "ds_read_b64_tr_b16 v[128:129], v241 offset:4096\n"
    "v_mfma_f32_16x16x32_bf16 v[48:51], v[72:75], v[104:107], v[48:51]\n"
    "ds_read_b64_tr_b16 v[130:131], v241 offset:4608\n"
    "v_mfma_f32_16x16x32_bf16 v[52:55], v[76:79], v[104:107], v[52:55]\n"
    "ds_read_b128 v[136:139], v189 offset:7168\n"
    "v_mfma_f32_16x16x32_bf16 v[56:59], v[80:83], v[104:107], v[56:59]\n"
    "ds_read_b128 v[140:143], v189 offset:8192\n"
    "v_mfma_f32_16x16x32_bf16 v[60:63], v[84:87], v[104:107], v[60:63]\n"
    "v_mfma_f32_16x16x32_bf16 v[64:67], v[88:91], v[104:107], v[64:67]\n"
    "v_mfma_f32_16x16x32_bf16 v[68:71], v[92:95], v[104:107], v[68:71]\n"
    "s_waitcnt lgkmcnt(0)\n"
    "s_barrier\n"
    "v_mfma_f32_16x16x32_bf16 v[48:51], v[108:111], v[140:143], v[48:51]\n"
    "ds_read_b128 v[104:107], v184 offset:8192\n"
    "ds_read_b64_tr_b16 v[72:73], v236 offset:1024\n"
    "v_mfma_f32_16x16x32_bf16 v[52:55], v[112:115], v[140:143], v[52:55]\n"
    "ds_read_b64_tr_b16 v[74:75], v236 offset:1536\n"
    "ds_read_b64_tr_b16 v[76:77], v236 offset:2048\n"
    "v_mfma_f32_16x16x32_bf16 v[0:3], v[108:111], v[132:135], v[0:3]\n"
    "ds_read_b64_tr_b16 v[78:79], v236 offset:2560\n"
    "s_movk_i32 s0, 4096\n"
    "v_mfma_f32_16x16x32_bf16 v[4:7], v[112:115], v[132:135], v[4:7]\n"
    "ds_read_b128 v[176:179], v185 offset:8192\n"
    "s_bitcmp1_b32 s77, 0\n"
    "v_mfma_f32_16x16x32_bf16 v[24:27], v[108:111], v[136:139], v[24:27]\n"
    "ds_read_b64_tr_b16 v[144:145], v236 offset:5120\n"
    "s_cselect_b64 vcc, -1, 0\n"
    "v_mfma_f32_16x16x32_bf16 v[28:31], v[112:115], v[136:139], v[28:31]\n"
    "ds_read_b64_tr_b16 v[146:147], v236 offset:5632\n"
    "s_cmp_ge_u32 s69, 2\n"
    "v_mfma_f32_16x16x32_bf16 v[56:59], v[116:119], v[140:143], v[56:59]\n"
    "ds_read_b64_tr_b16 v[148:149], v236 offset:6144\n"
    "v_mov_b32 v111, s68\n"
    "v_mfma_f32_16x16x32_bf16 v[8:11], v[116:119], v[132:135], v[8:11]\n"
    "ds_read_b64_tr_b16 v[150:151], v236 offset:6656\n"
    "v_mov_b32 v115, s68\n"
    "v_mfma_f32_16x16x32_bf16 v[32:35], v[116:119], v[136:139], v[32:35]\n"
    "ds_read_b128 v[96:99], v184 offset:3072\n"
    "v_mov_b32 v183, s68\n"
    "v_mfma_f32_16x16x32_bf16 v[60:63], v[120:123], v[140:143], v[60:63]\n"
    "ds_read_b128 v[168:171], v185 offset:3072\n"
    "v_mfma_f32_16x16x32_bf16 v[64:67], v[124:127], v[140:143], v[64:67]\n"
    "ds_read_b128 v[100:103], v184 offset:7168\n"
    "v_mov_b32 v119, s68\n"
    "v_mfma_f32_16x16x32_bf16 v[12:15], v[120:123], v[132:135], v[12:15]\n"
    "ds_read_b128 v[172:175], v185 offset:7168\n"
    "s_waitcnt lgkmcnt(11)\n"
    "v_mfma_f32_16x16x32_bf16 v[48:51], v[72:75], v[104:107], v[48:51]\n"
    "ds_read_b64_tr_b16 v[80:81], v236\n"
    "s_waitcnt lgkmcnt(10)\n"
    "v_mfma_f32_16x16x32_bf16 v[52:55], v[76:79], v[104:107], v[52:55]\n"
    "ds_read_b64_tr_b16 v[82:83], v236 offset:512\n"
    "s_waitcnt lgkmcnt(8)\n"
    "v_mfma_f32_16x16x32_bf16 v[48:51], v[144:147], v[176:179], v[48:51]\n"
    "ds_read_b64_tr_b16 v[152:153], v236 offset:4096\n"
    "s_waitcnt lgkmcnt(7)\n"
    "v_mfma_f32_16x16x32_bf16 v[52:55], v[148:151], v[176:179], v[52:55]\n"
    "ds_read_b64_tr_b16 v[154:155], v236 offset:4608\n"
    "s_waitcnt lgkmcnt(7)\n"
    "v_mfma_f32_16x16x32_bf16 v[0:3], v[72:75], v[96:99], v[0:3]\n"
    "ds_read_b64_tr_b16 v[84:85], v237 offset:1024\n"
    "v_mfma_f32_16x16x32_bf16 v[4:7], v[76:79], v[96:99], v[4:7]\n"
    "ds_read_b64_tr_b16 v[86:87], v237 offset:1536\n"
    "s_nop 0\n"
    "v_cvt_pk_bf16_f32 v108, v48, v49\n"
    "v_cvt_pk_bf16_f32 v109, v50, v51\n"
    "s_waitcnt lgkmcnt(8)\n"
    "v_mfma_f32_16x16x32_bf16 v[0:3], v[144:147], v[168:171], v[0:3]\n"
    "ds_read_b64_tr_b16 v[156:157], v237 offset:5120\n"
    "ds_write_b64 v216, v[108:109] offset:22848\n"
    "v_cvt_pk_bf16_f32 v110, v52, v53\n"
    "v_mfma_f32_16x16x32_bf16 v[4:7], v[148:151], v[168:171], v[4:7]\n"
    "ds_read_b64_tr_b16 v[158:159], v237 offset:5632\n"
    "buffer_store_dwordx4 v[108:111], v243, s[52:55], 0 offen sc1\n"
    "s_nop 1\n"
    "v_cvt_pk_bf16_f32 v111, v54, v55\n"
    "s_waitcnt lgkmcnt(10)\n"
    "v_mfma_f32_16x16x32_bf16 v[24:27], v[72:75], v[100:103], v[24:27]\n"
    "ds_read_b64_tr_b16 v[88:89], v237 offset:2048\n"
    "ds_write_b64 v216, v[110:111] offset:40320\n"
    "v_mfma_f32_16x16x32_bf16 v[28:31], v[76:79], v[100:103], v[28:31]\n"
    "ds_read_b64_tr_b16 v[90:91], v237 offset:2560\n"
    "v_cvt_pk_bf16_f32 v112, v0, v1\n"
    "v_cvt_pk_bf16_f32 v113, v2, v3\n"
    "s_waitcnt lgkmcnt(12)\n"
    "v_mfma_f32_16x16x32_bf16 v[24:27], v[144:147], v[172:175], v[24:27]\n"
    "ds_read_b64_tr_b16 v[160:161], v237 offset:6144\n"
    "ds_write_b64 v216, v[112:113] offset:17472\n"
    "v_cvt_pk_bf16_f32 v114, v4, v5\n"
    "v_mfma_f32_16x16x32_bf16 v[28:31], v[148:151], v[172:175], v[28:31]\n"
    "ds_read_b64_tr_b16 v[162:163], v237 offset:6656\n"
    "s_cbranch_vccz SK1%=\n"
    "buffer_store_dwordx4 v[112:115], v242, s[52:55], 0 offen sc1\n"
    "SK1%=:\n"
    "v_mov_b32 v75, s68\n"
    "s_waitcnt lgkmcnt(13)\n"
    "v_mfma_f32_16x16x32_bf16 v[56:59], v[80:83], v[104:107], v[56:59]\n"
    "ds_read_b64_tr_b16 v[92:93], v237\n"
    "v_mov_b32 v79, s68\n"
    "v_cvt_pk_bf16_f32 v182, v54, v55\n"
    "v_mfma_f32_16x16x32_bf16 v[8:11], v[80:83], v[96:99], v[8:11]\n"
    "ds_read_b64_tr_b16 v[94:95], v237 offset:512\n"
    "v_cvt_pk_bf16_f32 v74, v6, v7\n"
    "v_mov_b32 v147, s68\n"
    "v_mfma_f32_16x16x32_bf16 v[32:35], v[80:83], v[100:103], v[32:35]\n"
    "ds_read_b64_tr_b16 v[164:165], v237 offset:4096\n"
    "v_mov_b32 v151, s68\n"
    "v_cvt_pk_bf16_f32 v146, v54, v55\n"
    "s_waitcnt lgkmcnt(14)\n"
    "v_mfma_f32_16x16x32_bf16 v[56:59], v[152:155], v[176:179], v[56:59]\n"
    "ds_read_b64_tr_b16 v[166:167], v237 offset:4608\n"
    "v_cvt_pk_bf16_f32 v116, v24, v25\n"
    "v_mfma_f32_16x16x32_bf16 v[8:11], v[152:155], v[168:171], v[8:11]\n"
    "v_cvt_pk_bf16_f32 v117, v26, v27\n"
    "v_cvt_pk_bf16_f32 v118, v28, v29\n"
    "v_mfma_f32_16x16x32_bf16 v[32:35], v[152:155], v[172:175], v[32:35]\n"
    "ds_write_b64 v216, v[116:117] offset:20160\n"
    "s_cbranch_vccz SK2%=\n"
    "buffer_store_dwordx4 v[116:119], v242, s[52:55], 0 offen offset:2048 sc1\n"
    "SK2%=:\n"
    "s_waitcnt lgkmcnt(14)\n"
    "v_mfma_f32_16x16x32_bf16 v[60:63], v[84:87], v[104:107], v[60:63]\n"
    "v_cvt_pk_bf16_f32 v78, v30, v31\n"
    "v_cvt_pk_bf16_f32 v180, v56, v57\n"
    "s_waitcnt lgkmcnt(8)\n"
    "v_mfma_f32_16x16x32_bf16 v[64:67], v[88:91], v[104:107], v[64:67]\n"
    "s_waitcnt lgkmcnt(0)\n"
    "s_barrier\n"
    "v_cvt_pk_bf16_f32 v181, v58, v59\n"
    "v_mfma_f32_16x16x32_bf16 v[12:15], v[84:87], v[96:99], v[12:15]\n"
    "s_cbranch_vccz SK3%=\n"
    "buffer_store_dwordx4 v[180:183], v242, s[52:55], s0 offen offset:1024 sc1\n"
    "SK3%=:\n"
    "v_cvt_pk_bf16_f32 v72, v8, v9\n"
    "v_mfma_f32_16x16x32_bf16 v[60:63], v[156:159], v[176:179], v[60:63]\n"
    "ds_write_b64 v216, v[180:181] offset:5376\n"
    "v_cvt_pk_bf16_f32 v73, v10, v11\n"
    "v_mfma_f32_16x16x32_bf16 v[64:67], v[160:163], v[176:179], v[64:67]\n"
    "s_cbranch_vccz SK4%=\n"
    "buffer_store_dwordx4 v[72:75], v242, s[52:55], 0 offen offset:1024 sc1\n"
    "SK4%=:\n"
    "v_cvt_pk_bf16_f32 v76, v32, v33\n"
    "v_mfma_f32_16x16x32_bf16 v[12:15], v[156:159], v[168:171], v[12:15]\n"
    "ds_write_b64 v216, v[72:73]\n"
    "v_cvt_pk_bf16_f32 v77, v34, v35\n"
    "v_mfma_f32_16x16x32_bf16 v[16:19], v[124:127], v[132:135], v[16:19]\n"
    "s_cbranch_vccz SK5%=\n"
    "buffer_store_dwordx4 v[76:79], v242, s[52:55], 0 offen offset:3072 sc1\n"
    "SK5%=:\n"
    "v_cvt_pk_bf16_f32 v144, v60, v61\n"
    "v_mfma_f32_16x16x32_bf16 v[16:19], v[88:91], v[96:99], v[16:19]\n"
    "ds_write_b64 v216, v[76:77] offset:2688\n"
    "v_cvt_pk_bf16_f32 v145, v62, v63\n"
    "v_mfma_f32_16x16x32_bf16 v[16:19], v[160:163], v[168:171], v[16:19]\n"
    "buffer_store_dwordx4 v[144:147], v244, s[52:55], 0 offen offset:512 sc1\n"
    "v_cvt_pk_bf16_f32 v148, v64, v65\n"
    "v_mfma_f32_16x16x32_bf16 v[40:43], v[124:127], v[136:139], v[40:43]\n"
    "ds_write_b64 v217, v[144:145] offset:22848\n"
    "v_cvt_pk_bf16_f32 v149, v66, v67\n"
    "v_mfma_f32_16x16x32_bf16 v[40:43], v[88:91], v[100:103], v[40:43]\n"
    "s_cbranch_scc0 SK6%=\n"
    "buffer_store_dwordx4 v[148:151], v245, s[52:55], 0 offen offset:1024 sc1\n"
    "SK6%=:\n"
    "v_cvt_pk_bf16_f32 v115, v6, v7\n"
    "v_mfma_f32_16x16x32_bf16 v[40:43], v[160:163], v[172:175], v[40:43]\n"
    "ds_write_b64 v216, v[114:115] offset:34944\n"
    "v_cvt_pk_bf16_f32 v119, v30, v31\n"
    "v_mfma_f32_16x16x32_bf16 v[36:39], v[120:123], v[136:139], v[36:39]\n"
    "v_cvt_pk_bf16_f32 v12, v12, v13\n"
    "v_cvt_pk_bf16_f32 v13, v14, v15\n"
    "v_mfma_f32_16x16x32_bf16 v[36:39], v[84:87], v[100:103], v[36:39]\n"
    "ds_write_b64 v216, v[118:119] offset:37632\n"
    "v_cvt_pk_bf16_f32 v16, v16, v17\n"
    "v_mfma_f32_16x16x32_bf16 v[36:39], v[156:159], v[172:175], v[36:39]\n"
    "v_cvt_pk_bf16_f32 v17, v18, v19\n"
    "v_cvt_pk_bf16_f32 v40, v40, v41\n"
    "v_mfma_f32_16x16x32_bf16 v[20:23], v[128:131], v[132:135], v[20:23]\n"
    "ds_write_b64 v217, v[148:149] offset:40320\n"
    "v_cvt_pk_bf16_f32 v41, v42, v43\n"
    "v_mfma_f32_16x16x32_bf16 v[20:23], v[92:95], v[96:99], v[20:23]\n"
    "v_mfma_f32_16x16x32_bf16 v[20:23], v[164:167], v[168:171], v[20:23]\n"
    "ds_write_b64 v217, v[12:13] offset:17472\n"
    "v_cvt_pk_bf16_f32 v36, v36, v37\n"
    "v_mfma_f32_16x16x32_bf16 v[44:47], v[128:131], v[136:139], v[44:47]\n"
    "v_cvt_pk_bf16_f32 v37, v38, v39\n"
    "v_mfma_f32_16x16x32_bf16 v[44:47], v[92:95], v[100:103], v[44:47]\n"
    "ds_write_b64 v217, v[16:17] offset:34944\n"
    "v_mfma_f32_16x16x32_bf16 v[44:47], v[164:167], v[172:175], v[44:47]\n"
    "s_nop 0\n"
    "v_cvt_pk_bf16_f32 v20, v20, v21\n"
    "v_cvt_pk_bf16_f32 v21, v22, v23\n"
    "v_mfma_f32_16x16x32_bf16 v[68:71], v[128:131], v[140:143], v[68:71]\n"
    "ds_write_b64 v217, v[40:41] offset:37632\n"
    "v_mfma_f32_16x16x32_bf16 v[68:71], v[92:95], v[104:107], v[68:71]\n"
    "ds_write_b64 v217, v[36:37] offset:20160\n"
    "v_mfma_f32_16x16x32_bf16 v[68:71], v[164:167], v[176:179], v[68:71]\n"
    "v_cvt_pk_bf16_f32 v44, v44, v45\n"
    "v_cvt_pk_bf16_f32 v45, v46, v47\n"
    "ds_write_b64 v217, v[20:21]\n"
    "ds_write_b64 v217, v[44:45] offset:2688\n"
    "s_nop 3\n"
    "v_cvt_pk_bf16_f32 v68, v68, v69\n"
    "v_cvt_pk_bf16_f32 v69, v70, v71\n"
    "ds_write_b64 v217, v[68:69] offset:5376\n"
    "s_waitcnt lgkmcnt(0)\n"
    "s_barrier\n"
    "s_branch TAIL%=\n"
    "LOADER%=:\n"
    "s_mov_b32 s36, s94\n"
    "s_and_b32 s37, s95, 65535\n"
    "s_mov_b32 s38, 2457600\n"
    "s_mov_b32 s39, 131072\n"
    "s_add_u32 s40, s92, 4294964224\n"
    "s_addc_u32 s41, s93, -1\n"
    "s_and_b32 s41, s41, 65535\n"
    "s_mov_b32 s42, 2624512\n"
    "s_mov_b32 s43, 131072\n"
    "s_add_u32 s60, s96, 4956160\n"
    "s_addc_u32 s61, s97, 0\n"
    "s_and_b32 s61, s61, 65535\n"
    "s_mov_b32 s62, 146432\n"
    "s_mov_b32 s63, 131072\n"
    "s_and_b32 s45, s45, 65535\n"
    "s_mov_b32 s46, 819200\n"
    "s_mov_b32 s47, 131072\n"
    "s_mul_i32 s0, s70, 245760\n"
    "s_lshl_b32 s1, s70, 7\n"
    "s_load_dword s68, s[60:61], 146176\n"
    "s_add_u32 m0, s71, 116736\n"
    "s_nop 0\n"
    "buffer_load_dwordx4 v182, s[36:39], s0 offen lds\n"
    "buffer_load_dwordx4 v183, s[36:39], s0 offen offset:1024 lds\n"
    "buffer_load_dwordx4 v184, s[36:39], s0 offen offset:2048 lds\n"
    "buffer_load_dwordx4 v177, s[40:43], s1 offen offset:3072 lds\n"
    "s_add_u32 s70, s70, 1\n"
    "s_sub_u32 s2, s70, 10\n"
    "s_cselect_b32 s70, s70, s2\n"
    "s_mul_i32 s0, s70, 245760\n"
    "s_lshl_b32 s1, s70, 7\n"
    "s_add_u32 m0, s71, 51200\n"
    "s_nop 0\n"
    "buffer_load_dwordx4 v179, s[36:39], s0 offen lds\n"
    "buffer_load_dwordx4 v180, s[36:39], s0 offen offset:1024 lds\n"
    "buffer_load_dwordx4 v181, s[36:39], s0 offen offset:2048 lds\n"
    "buffer_load_dwordx4 v176, s[40:43], s1 offen offset:3072 lds\n"
    "s_add_u32 m0, s71, 59392\n"
    "s_nop 0\n"
    "buffer_load_dwordx4 v178, s[40:43], s1 offen lds\n"
    "s_add_u32 m0, s71, 55296\n"
    "s_nop 0\n"
    "buffer_load_dwordx4 v182, s[36:39], s0 offen lds\n"
    "buffer_load_dwordx4 v183, s[36:39], s0 offen offset:1024 lds\n"
    "buffer_load_dwordx4 v184, s[36:39], s0 offen offset:2048 lds\n"
    "buffer_load_dwordx4 v177, s[40:43], s1 offen offset:3072 lds\n"
    "s_add_u32 s70, s70, 1\n"
    "s_sub_u32 s2, s70, 10\n"
    "s_cselect_b32 s70, s70, s2\n"
    "s_mul_i32 s0, s70, 245760\n"
    "s_lshl_b32 s1, s70, 7\n"
    "s_mov_b32 m0, 158720\n"
    "s_nop 0\n"
    "buffer_load_dword v185, s[44:47], 0 offen lds\n"
    "v_mbcnt_lo_u32_b32 v4, -1, 0\n"
    "v_mbcnt_hi_u32_b32 v4, -1, v4\n"
    "v_lshlrev_b32 v4, 4, v4\n"
    "v_add_u32 v4, 88064, v4\n"
    "s_mov_b32 s48, s96\n"
    "s_and_b32 s49, s97, 65535\n"
    "s_mov_b32 s50, 2703360\n"
    "s_mov_b32 s51, 131072\n"
    "s_add_u32 s52, s96, 2703360\n"
    "s_addc_u32 s53, s97, 0\n"
    "s_and_b32 s53, s53, 65535\n"
    "s_mov_b32 s54, 2252800\n"
    "s_mov_b32 s55, 131072\n"
    "s_mov_b32 s56, s98\n"
    "s_and_b32 s57, s99, 65535\n"
    "s_mov_b32 s58, 2621440\n"
    "s_mov_b32 s59, 131072\n"
    "s_add_u32 s64, s96, 5102592\n"
    "s_addc_u32 s65, s97, 0\n"
    "s_and_b32 s65, s65, 65535\n"
    "s_mov_b32 s66, 276480\n"
    "s_mov_b32 s67, 131072\n"
    "s_waitcnt vmcnt(10)\n"
    "s_barrier\n"
    "ds_read_b128 v[192:195], v4\n"
    "ds_read_b128 v[196:199], v4 offset:1024\n"
    "ds_read_b128 v[200:203], v4 offset:2048\n"
    "ds_read_b128 v[204:207], v4 offset:3072\n"
    "ds_read_b128 v[16:19], v4 offset:4096\n"
    "ds_read_b128 v[20:23], v4 offset:5120\n"
    "ds_read_b128 v[224:227], v4 offset:6144\n"
    "ds_read_b128 v[228:231], v4 offset:7168\n"
    "ds_read_b128 v[232:235], v4 offset:8192\n"
    "ds_read_b128 v[236:239], v4 offset:9216\n"
    "ds_read_b128 v[240:243], v4 offset:10240\n"
    "ds_read_b128 v[244:247], v4 offset:11264\n"
    "ds_read_b128 v[248:251], v4 offset:12288\n"
    "s_add_u32 m0, s71, 0\n"
    "s_nop 0\n"
    "buffer_load_dwordx4 v179, s[36:39], s0 offen lds\n"
    "buffer_load_dwordx4 v180, s[36:39], s0 offen offset:1024 lds\n"
    "buffer_load_dwordx4 v181, s[36:39], s0 offen offset:2048 lds\n"
    "buffer_load_dwordx4 v176, s[40:43], s1 offen offset:3072 lds\n"
    "s_waitcnt lgkmcnt(0)\n"
    "s_add_u32 s68, s68, 0x9E3779B9\n"
    "s_lshl_b32 s20, s69, 2\n"
    "s_and_b32 s23, s69, 1\n"
    "s_lshl_b32 s22, s23, 6\n"
    "v_mov_b32 v6, 0x7fff0000\n"
    "v_mov_b32 v7, 0xff800000\n"
    "s_bfe_u32 s21, s69, 65537\n"
    "s_mul_i32 s21, s21, 80\n"
    "s_add_u32 s21, s21, s23\n"
    "s_mul_i32 s21, s21, 5120\n"
    "v_mov_b32 v8, s21\n"
    "v_xad_u32 v236, v236, s22, v8\n"
    "v_xor_b32 v237, s22, v237\n"
    "s_mul_i32 s21, s79, 25\n"
    "s_add_u32 s24, s21, 307200\n"
    "s_cmp_eq_u32 s79, 0\n"
    "s_cselect_b32 s24, 0, s24\n"
    "v_add_u32 v238, s24, v237\n"
    "s_mul_i32 s21, s21, 3\n"
    "v_add_u32 v237, s21, v237\n"
    "v_mov_b32 v252, 0x3f803f80\n"
    "v_mov_b32 v253, 0x3f803f80\n"
    "v_mov_b32 v254, 0x3f803f80\n"
    "v_mov_b32 v255, 0x3f803f80\n"
    "v_mov_b32 v85, s68\n"
    "v_and_b32 v86, 240, v4\n"
    "v_lshrrev_b32 v86, 1, v86\n"
    "v_add_u32 v86, s84, v86\n"
    "v_readlane_b32 s21, v21, s20\n"
    "v_readlane_b32 s72, v22, s20\n"
    "s_add_u32 s20, s20, 1\n"
    "v_add_u32 v208, s21, v16\n"
    "v_add_u32 v209, s21, v17\n"
    "v_mad_u32_u24 v210, s21, v20, v18\n"
    "v_add_u32 v211, s21, v19\n"
    "v_readlane_b32 s21, v21, s20\n"
    "v_readlane_b32 s73, v22, s20\n"
    "s_add_u32 s20, s20, 1\n"
    "v_add_u32 v212, s21, v16\n"
    "v_add_u32 v213, s21, v17\n"
    "v_mad_u32_u24 v214, s21, v20, v18\n"
    "v_add_u32 v215, s21, v19\n"
    "v_readlane_b32 s21, v21, s20\n"
    "v_readlane_b32 s74, v22, s20\n"
    "s_add_u32 s20, s20, 1\n"
    "v_add_u32 v216, s21, v16\n"
    "v_add_u32 v217, s21, v17\n"
    "v_mad_u32_u24 v218, s21, v20, v18\n"
    "v_add_u32 v219, s21, v19\n"
    "s_bitcmp1_b32 s77, 3\n"
    "s_cselect_b32 s21, 2688, s21\n"
    "s_cmp_lt_u32 s21, 2688\n"
    "s_cselect_b64 vcc, -1, 0\n"
    "v_cndmask_b32 v168, v192, v7, vcc\n"
    "v_cndmask_b32 v169, v193, v7, vcc\n"
    "v_cndmask_b32 v170, v194, v7, vcc\n"
    "v_cndmask_b32 v171, v195, v7, vcc\n"
    "s_cmp_lt_i32 s74, 0\n"
    "s_cselect_b64 vcc, -1, 0\n"
    "s_max_i32 s74, s74, 0\n"
    "v_cndmask_b32 v186, v206, v6, vcc\n"
    "v_cndmask_b32 v187, v207, v6, vcc\n"
    "v_readlane_b32 s21, v21, s20\n"
    "v_readlane_b32 s88, v22, s20\n"
    "s_add_u32 s20, s20, 1\n"
    "v_add_u32 v220, s21, v16\n"
    "v_add_u32 v221, s21, v17\n"
    "v_mad_u32_u24 v222, s21, v20, v18\n"
    "v_add_u32 v223, s21, v19\n"
    "s_bitcmp1_b32 s77, 3\n"
    "s_cselect_b32 s21, 2688, s21\n"
    "s_cmp_lt_u32 s21, 2688\n"
    "s_cselect_b64 vcc, -1, 0\n"
    "v_cndmask_b32 v172, v192, v7, vcc\n"
    "v_cndmask_b32 v173, v193, v7, vcc\n"
    "v_cndmask_b32 v174, v194, v7, vcc\n"
    "v_cndmask_b32 v175, v195, v7, vcc\n"
    "s_cmp_lt_i32 s88, 0\n"
    "s_cselect_b64 vcc, -1, 0\n"
    "s_max_i32 s88, s88, 0\n"
    "v_cndmask_b32 v188, v206, v6, vcc\n"
    "v_cndmask_b32 v189, v207, v6, vcc\n"
    "s_mul_i32 s21, s23, 40960\n"
    "v_mov_b32 v8, s21\n"
    "v_xad_u32 v224, v224, s22, v8\n"
    "v_xad_u32 v225, v225, s22, v8\n"
    "v_xad_u32 v226, v226, s22, v8\n"
    "v_xad_u32 v227, v227, s22, v8\n"
    "s_lshl_b32 s21, s83, 11\n"
    "v_mov_b32 v8, s21\n"
    "v_xad_u32 v228, v228, s22, v8\n"
    "v_xad_u32 v229, v229, s22, v8\n"
    "v_xad_u32 v230, v230, s22, v8\n"
    "v_xad_u32 v231, v231, s22, v8\n"
    "v_xad_u32 v232, v232, s22, v8\n"
    "s_mul_i32 s21, s83, 40960\n"
    "s_add_u32 s21, s21, s22\n"
    "v_add_u32 v233, s21, v233\n"
    "s_lshl_b32 s21, s83, 7\n"
    "v_add_u32 v234, s21, v234\n"
    "s_lshl_b32 s21, s79, 1\n"
    "v_add_u32 v235, s21, v235\n"
    "s_add_u32 m0, s71, 8192\n"
    "s_nop 0\n"
    "buffer_load_dwordx4 v178, s[40:43], s1 offen lds\n"
    "s_add_u32 m0, s71, 4096\n"
    "s_nop 0\n"
    "buffer_load_dwordx4 v182, s[36:39], s0 offen lds\n"
    "buffer_load_dwordx4 v183, s[36:39], s0 offen offset:1024 lds\n"
    "buffer_load_dwordx4 v184, s[36:39], s0 offen offset:2048 lds\n"
    "buffer_load_dwordx4 v177, s[40:43], s1 offen offset:3072 lds\n"
    "s_add_u32 s70, s70, 1\n"
    "s_sub_u32 s2, s70, 10\n"
    "s_cselect_b32 s70, s70, s2\n"
    "s_mul_i32 s0, s70, 245760\n"
    "s_lshl_b32 s1, s70, 7\n"
    "s_waitcnt vmcnt(10)\n"
    "s_barrier\n"
    "s_add_u32 m0, s71, 112640\n"
    "s_nop 0\n"
    "buffer_load_dwordx4 v179, s[36:39], s0 offen lds\n"
    "buffer_load_dwordx4 v180, s[36:39], s0 offen offset:1024 lds\n"
    "buffer_load_dwordx4 v181, s[36:39], s0 offen offset:2048 lds\n"
    "buffer_load_dwordx4 v176, s[40:43], s1 offen offset:3072 lds\n"
    "s_add_u32 m0, s71, 120832\n"
    "s_nop 0\n"
    "buffer_load_dwordx4 v178, s[40:43], s1 offen lds\n"
    "s_add_u32 m0, s71, 116736\n"
    "s_nop 0\n"
    "buffer_load_dwordx4 v182, s[36:39], s0 offen lds\n"
    "buffer_load_dwordx4 v183, s[36:39], s0 offen offset:1024 lds\n"
    "buffer_load_dwordx4 v184, s[36:39], s0 offen offset:2048 lds\n"
    "buffer_load_dwordx4 v177, s[40:43], s1 offen offset:3072 lds\n"
    "s_add_u32 s70, s70, 1\n"
    "s_sub_u32 s2, s70, 10\n"
    "s_cselect_b32 s70, s70, s2\n"
    "s_mul_i32 s0, s70, 245760\n"
    "s_lshl_b32 s1, s70, 7\n"
    "s_waitcnt vmcnt(9)\n"
    "s_barrier\n"
    "s_add_u32 m0, s71, 51200\n"
    "s_nop 0\n"
    "buffer_load_dwordx4 v179, s[36:39], s0 offen lds\n"
    "buffer_load_dwordx4 v180, s[36:39], s0 offen offset:1024 lds\n"
    "buffer_load_dwordx4 v181, s[36:39], s0 offen offset:2048 lds\n"
    "buffer_load_dwordx4 v176, s[40:43], s1 offen offset:3072 lds\n"
    "s_add_u32 m0, s71, 59392\n"
    "s_nop 0\n"
    "buffer_load_dwordx4 v178, s[40:43], s1 offen lds\n"
    "s_add_u32 m0, s71, 55296\n"
    "s_nop 0\n"
    "buffer_load_dwordx4 v182, s[36:39], s0 offen lds\n"
    "buffer_load_dwordx4 v183, s[36:39], s0 offen offset:1024 lds\n"
    "buffer_load_dwordx4 v184, s[36:39], s0 offen offset:2048 lds\n"
    "buffer_load_dwordx4 v177, s[40:43], s1 offen offset:3072 lds\n"
    "s_add_u32 s70, s70, 1\n"
    "s_sub_u32 s2, s70, 10\n"
    "s_cselect_b32 s70, s70, s2\n"
    "s_mul_i32 s0, s70, 245760\n"
    "s_lshl_b32 s1, s70, 7\n"
    "s_waitcnt vmcnt(9)\n"
    "s_barrier\n"
    "s_add_u32 m0, s71, 0\n"
    "s_nop 0\n"
    "buffer_load_dwordx4 v179, s[36:39], s0 offen lds\n"
    "buffer_load_dwordx4 v180, s[36:39], s0 offen offset:1024 lds\n"
    "buffer_load_dwordx4 v181, s[36:39], s0 offen offset:2048 lds\n"
    "buffer_load_dwordx4 v176, s[40:43], s1 offen offset:3072 lds\n"
    "s_add_u32 m0, s71, 8192\n"
    "s_nop 0\n"
    "buffer_load_dwordx4 v178, s[40:43], s1 offen lds\n"
    "s_add_u32 m0, s71, 4096\n"
    "s_nop 0\n"
    "buffer_load_dwordx4 v182, s[36:39], s0 offen lds\n"
    "buffer_load_dwordx4 v183, s[36:39], s0 offen offset:1024 lds\n"
    "buffer_load_dwordx4 v184, s[36:39], s0 offen offset:2048 lds\n"
    "buffer_load_dwordx4 v177, s[40:43], s1 offen offset:3072 lds\n"
    "s_add_u32 s70, s70, 1\n"
    "s_sub_u32 s2, s70, 10\n"
    "s_cselect_b32 s70, s70, s2\n"
    "s_mul_i32 s0, s70, 245760\n"
    "s_lshl_b32 s1, s70, 7\n"
    "s_waitcnt vmcnt(9)\n"
    "s_barrier\n"
    "s_add_u32 m0, s71, 112640\n"
    "s_nop 0\n"
    "buffer_load_dwordx4 v179, s[36:39], s0 offen lds\n"
    "buffer_load_dwordx4 v180, s[36:39], s0 offen offset:1024 lds\n"
    "buffer_load_dwordx4 v181, s[36:39], s0 offen offset:2048 lds\n"
    "buffer_load_dwordx4 v176, s[40:43], s1 offen offset:3072 lds\n"
    "s_add_u32 m0, s71, 120832\n"
    "s_nop 0\n"
    "buffer_load_dwordx4 v178, s[40:43], s1 offen lds\n"
    "s_add_u32 m0, s71, 116736\n"
    "s_nop 0\n"
    "buffer_load_dwordx4 v182, s[36:39], s0 offen lds\n"
    "buffer_load_dwordx4 v183, s[36:39], s0 offen offset:1024 lds\n"
    "buffer_load_dwordx4 v184, s[36:39], s0 offen offset:2048 lds\n"
    "buffer_load_dwordx4 v177, s[40:43], s1 offen offset:3072 lds\n"
    "s_add_u32 s70, s70, 1\n"
    "s_sub_u32 s2, s70, 10\n"
    "s_cselect_b32 s70, s70, s2\n"
    "s_mul_i32 s0, s70, 245760\n"
    "s_lshl_b32 s1, s70, 7\n"
    "s_waitcnt vmcnt(9)\n"
    "s_barrier\n"
    "s_add_u32 m0, s71, 51200\n"
    "s_nop 0\n"
    "buffer_load_dwordx4 v179, s[36:39], s0 offen lds\n"
    "buffer_load_dwordx4 v180, s[36:39], s0 offen offset:1024 lds\n"
    "buffer_load_dwordx4 v181, s[36:39], s0 offen offset:2048 lds\n"
    "buffer_load_dwordx4 v176, s[40:43], s1 offen offset:3072 lds\n"
    "s_add_u32 m0, s71, 59392\n"
    "s_nop 0\n"
    "buffer_load_dwordx4 v178, s[40:43], s1 offen lds\n"
    "s_add_u32 m0, s71, 55296\n"
    "s_nop 0\n"
    "buffer_load_dwordx4 v182, s[36:39], s0 offen lds\n"
    "buffer_load_dwordx4 v183, s[36:39], s0 offen offset:1024 lds\n"
    "buffer_load_dwordx4 v184, s[36:39], s0 offen offset:2048 lds\n"
    "buffer_load_dwordx4 v177, s[40:43], s1 offen offset:3072 lds\n"
    "s_add_u32 s70, s70, 1\n"
    "s_sub_u32 s2, s70, 10\n"
    "s_cselect_b32 s70, s70, s2\n"
    "s_mul_i32 s0, s70, 245760\n"
    "s_lshl_b32 s1, s70, 7\n"
    "s_waitcnt vmcnt(9)\n"
    "s_barrier\n"
    "s_add_u32 m0, s71, 0\n"
    "s_nop 0\n"
    "buffer_load_dwordx4 v179, s[36:39], s0 offen lds\n"
    "buffer_load_dwordx4 v180, s[36:39], s0 offen offset:1024 lds\n"
    "buffer_load_dwordx4 v181, s[36:39], s0 offen offset:2048 lds\n"
    "buffer_load_dwordx4 v176, s[40:43], s1 offen offset:3072 lds\n"
    "s_add_u32 m0, s71, 8192\n"
    "s_nop 0\n"
    "buffer_load_dwordx4 v178, s[40:43], s1 offen lds\n"
    "s_add_u32 m0, s71, 4096\n"
    "s_nop 0\n"
    "buffer_load_dwordx4 v182, s[36:39], s0 offen lds\n"
    "buffer_load_dwordx4 v183, s[36:39], s0 offen offset:1024 lds\n"
    "buffer_load_dwordx4 v184, s[36:39], s0 offen offset:2048 lds\n"
    "buffer_load_dwordx4 v177, s[40:43], s1 offen offset:3072 lds\n"
    "s_add_u32 s70, s70, 1\n"
    "s_sub_u32 s2, s70, 10\n"
    "s_cselect_b32 s70, s70, s2\n"
    "s_mul_i32 s0, s70, 245760\n"
    "s_lshl_b32 s1, s70, 7\n"
    "s_waitcnt vmcnt(9)\n"
    "s_barrier\n"
    "s_add_u32 m0, s71, 112640\n"
    "s_nop 0\n"
    "buffer_load_dwordx4 v179, s[36:39], s0 offen lds\n"
    "buffer_load_dwordx4 v180, s[36:39], s0 offen offset:1024 lds\n"
    "buffer_load_dwordx4 v181, s[36:39], s0 offen offset:2048 lds\n"
    "buffer_load_dwordx4 v176, s[40:43], s1 offen offset:3072 lds\n"
    "s_add_u32 m0, s71, 120832\n"
    "s_nop 0\n"
    "buffer_load_dwordx4 v178, s[40:43], s1 offen lds\n"
    "s_add_u32 m0, s71, 116736\n"
    "s_nop 0\n"
    "buffer_load_dwordx4 v182, s[36:39], s0 offen lds\n"
    "buffer_load_dwordx4 v183, s[36:39], s0 offen offset:1024 lds\n"
    "buffer_load_dwordx4 v184, s[36:39], s0 offen offset:2048 lds\n"
    "buffer_load_dwordx4 v177, s[40:43], s1 offen offset:3072 lds\n"
    "s_waitcnt vmcnt(9)\n"
    "s_barrier\n"
    "s_memrealtime s[28:29]\n"
    "s_cmp_lg_u32 s69, 6\n"
    "s_cbranch_scc1 NH1%=\n"
    "s_waitcnt lgkmcnt(0)\n"
    "v_mov_b32 v84, s28\n"
    "buffer_store_dwordx2 v[84:85], v86, s[60:63], 0 offen sc1\n"
    "s_add_u32 m0, s79, 51200\n"
    "s_mov_b32 s2, 25600\n"
    "buffer_load_dwordx4 v236, s[44:47], s2 offen lds\n"
    "s_add_u32 m0, s79, 55296\n"
    "s_mov_b32 s2, 102400\n"
    "buffer_load_dwordx4 v236, s[44:47], s2 offen lds\n"
    "s_add_u32 m0, s79, 59392\n"
    "s_mov_b32 s2, 179200\n"
    "buffer_load_dwordx4 v236, s[44:47], s2 offen lds\n"
    "s_add_u32 m0, s79, 63488\n"
    "s_mov_b32 s2, 256000\n"
    "buffer_load_dwordx4 v236, s[44:47], s2 offen lds\n"
    "s_add_u32 m0, s79, 67584\n"
    "s_mov_b32 s2, 25600\n"
    "buffer_load_dwordx4 v237, s[44:47], s2 offen lds\n"
    "s_add_u32 m0, s79, 71680\n"
    "s_mov_b32 s2, 51200\n"
    "buffer_load_dwordx4 v236, s[44:47], s2 offen lds\n"
    "s_add_u32 m0, s79, 75776\n"
    "s_mov_b32 s2, 128000\n"
    "buffer_load_dwordx4 v236, s[44:47], s2 offen lds\n"
    "s_add_u32 m0, s79, 79872\n"
    "s_mov_b32 s2, 204800\n"
    "buffer_load_dwordx4 v236, s[44:47], s2 offen lds\n"
    "s_add_u32 m0, s79, 83968\n"
    "s_mov_b32 s2, 281600\n"
    "buffer_load_dwordx4 v236, s[44:47], s2 offen lds\n"
    "s_add_u32 m0, s79, 88064\n"
    "s_mov_b32 s2, 51200\n"
    "buffer_load_dwordx4 v237, s[44:47], s2 offen lds\n"
    "s_waitcnt vmcnt(11)\n"
    "s_branch NH2%=\n"
    "NH1%=:\n"
    "s_add_u32 m0, s79, 51200\n"
    "s_mov_b32 s2, 25600\n"
    "buffer_load_dwordx4 v236, s[44:47], s2 offen lds\n"
    "s_add_u32 m0, s79, 55296\n"
    "s_mov_b32 s2, 102400\n"
    "buffer_load_dwordx4 v236, s[44:47], s2 offen lds\n"
    "s_add_u32 m0, s79, 59392\n"
    "s_mov_b32 s2, 179200\n"
    "buffer_load_dwordx4 v236, s[44:47], s2 offen lds\n"
    "s_add_u32 m0, s79, 63488\n"
    "s_mov_b32 s2, 256000\n"
    "buffer_load_dwordx4 v236, s[44:47], s2 offen lds\n"
    "s_add_u32 m0, s79, 67584\n"
    "s_mov_b32 s2, 25600\n"
    "buffer_load_dwordx4 v237, s[44:47], s2 offen lds\n"
    "s_add_u32 m0, s79, 71680\n"
    "s_mov_b32 s2, 51200\n"
    "buffer_load_dwordx4 v236, s[44:47], s2 offen lds\n"
    "s_add_u32 m0, s79, 75776\n"
    "s_mov_b32 s2, 128000\n"
    "buffer_load_dwordx4 v236, s[44:47], s2 offen lds\n"
    "s_add_u32 m0, s79, 79872\n"
    "s_mov_b32 s2, 204800\n"
    "buffer_load_dwordx4 v236, s[44:47], s2 offen lds\n"
    "s_add_u32 m0, s79, 83968\n"
    "s_mov_b32 s2, 281600\n"
    "buffer_load_dwordx4 v236, s[44:47], s2 offen lds\n"
    "s_add_u32 m0, s79, 88064\n"
    "s_mov_b32 s2, 51200\n"
    "buffer_load_dwordx4 v237, s[44:47], s2 offen lds\n"
    "s_waitcnt vmcnt(10)\n"
    "NH2%=:\n"
    "s_barrier\n"
    "s_add_u32 m0, s79, 92160\n"
    "s_mov_b32 s2, 0\n"
    "buffer_load_dwordx4 v236, s[44:47], s2 offen lds\n"
    "s_add_u32 m0, s79, 96256\n"
    "s_mov_b32 s2, 332800\n"
    "buffer_load_dwordx4 v236, s[44:47], s2 offen lds\n"
    "s_add_u32 m0, s79, 100352\n"
    "s_mov_b32 s2, 358400\n"
    "buffer_load_dwordx4 v236, s[44:47], s2 offen lds\n"
    "s_add_u32 m0, s79, 104448\n"
    "s_mov_b32 s2, 384000\n"
    "buffer_load_dwordx4 v236, s[44:47], s2 offen lds\n"
    "s_add_u32 m0, s79, 108544\n"
    "s_mov_b32 s2, 0\n"
    "buffer_load_dwordx4 v238, s[44:47], s2 offen lds\n"
    "s_cmp_eq_u32 s69, 7\n"
    "s_cbranch_scc1 RX%=\n"
    "s_cmp_lg_u32 s69, 5\n"
    "s_cbranch_scc1 NS2%=\n"
    "s_mov_b32 m0, 155648\n"
    "s_nop 0\n"
    "buffer_load_dwordx4 v239, s[60:63], 0 offen lds\n"
    "NS2%=:\n"
    "s_barrier\n"
    "s_add_u32 s14, s76, 0\n"
    "s_add_u32 s15, s76, 7680\n"
    "s_add_u32 s16, s76, 61440\n"
    "s_add_u32 s17, s76, 15360\n"
    "s_add_u32 s18, s76, 23040\n"
    "s_add_u32 s19, s76, 76800\n"
    "s_waitcnt vmcnt(0)\n"
    "s_cmp_lg_u32 s69, 5\n"
    "s_cbranch_scc1 NV2%=\n"
    "s_mov_b32 s3, 262144\n"
    "v_mbcnt_lo_u32_b32 v4, -1, 0\n"
    "v_mbcnt_hi_u32_b32 v4, -1, v4\n"
    "v_lshlrev_b32 v4, 4, v4\n"
    "v_add_u32 v4, 155648, v4\n"
    "VF2%=:\n"
    "ds_read_b128 v[0:3], v4\n"
    "s_waitcnt lgkmcnt(0)\n"
    "v_xor_b32 v1, s68, v1\n"
    "v_xor_b32 v3, s68, v3\n"
    "v_or_b32 v1, v1, v3\n"
    "v_cmp_ne_u32 vcc, 0, v1\n"
    "s_cbranch_vccz VF2D%=\n"
    "s_sleep 8\n"
    "s_mov_b32 m0, 155648\n"
    "s_sub_u32 s3, s3, 1\n"
    "buffer_load_dwordx4 v239, s[60:63], 0 offen sc1 lds\n"
    "s_waitcnt vmcnt(0)\n"
    "s_cbranch_scc0 VF2%=\n"
    "VF2D%=:\n"
    "NV2%=:\n"
    "s_barrier\n"
    "s_add_u32 m0, s79, 112640\n"
    "s_mov_b32 s2, 76800\n"
    "buffer_load_dwordx4 v236, s[44:47], s2 offen lds\n"
    "s_add_u32 m0, s79, 116736\n"
    "s_mov_b32 s2, 153600\n"
    "buffer_load_dwordx4 v236, s[44:47], s2 offen lds\n"
    "s_add_u32 m0, s79, 120832\n"
    "s_mov_b32 s2, 230400\n"
    "buffer_load_dwordx4 v236, s[44:47], s2 offen lds\n"
    "s_add_u32 m0, s79, 124928\n"
    "s_mov_b32 s2, 307200\n"
    "buffer_load_dwordx4 v236, s[44:47], s2 offen lds\n"
    "s_add_u32 m0, s79, 129024\n"
    "s_mov_b32 s2, 76800\n"
    "buffer_load_dwordx4 v237, s[44:47], s2 offen lds\n"
    "s_add_u32 m0, s80, 130048\n"
    "s_nop 0\n"
    "buffer_load_dwordx4 v176, s[40:43], s78 offen offset:3072 lds\n"
    "s_add_u32 m0, s80, 132096\n"
    "s_nop 0\n"
    "buffer_load_dwordx4 v177, s[40:43], s78 offen offset:3072 lds\n"
    "s_add_u32 m0, s80, 137216\n"
    "s_nop 0\n"
    "buffer_load_dwordx4 v178, s[40:43], s78 offen lds\n"
    "TAIL%=:\n"
    "s_mov_b32 s4, 0x3ea52b5b\n"
    "s_cmp_lt_i32 s72, 0\n"
    "s_cbranch_scc1 PRE%=\n"
    "ds_read_b64 v[100:101], v208\n"
    "ds_read_b64 v[102:103], v210\n"
    "ds_read_b64 v[108:109], v208 offset:17472\n"
    "ds_read_b64 v[110:111], v209 offset:17472\n"
    "ds_read_b64 v[104:105], v208 offset:14784\n"
    "ds_read_b64 v[106:107], v209 offset:14784\n"
    "ds_read_b64 v[132:133], v212\n"
    "ds_read_b64 v[134:135], v214\n"
    "ds_read_b64 v[140:141], v212 offset:17472\n"
    "ds_read_b64 v[142:143], v213 offset:17472\n"
    "ds_read_b64 v[136:137], v212 offset:14784\n"
    "ds_read_b64 v[138:139], v213 offset:14784\n"
    "ds_read_b64_tr_b16 v[112:113], v211\n"
    "ds_read_b64_tr_b16 v[114:115], v211 offset:2688\n"
    "ds_read_b64_tr_b16 v[116:117], v211 offset:16\n"
    "ds_read_b64_tr_b16 v[118:119], v211 offset:2704\n"
    "ds_read_b64_tr_b16 v[144:145], v215\n"
    "ds_read_b64_tr_b16 v[146:147], v215 offset:2688\n"
    "ds_read_b64_tr_b16 v[148:149], v215 offset:16\n"
    "ds_read_b64_tr_b16 v[150:151], v215 offset:2704\n"
    "ds_read_b64_tr_b16 v[0:1], v224\n"
    "ds_read_b64_tr_b16 v[2:3], v224 offset:512\n"
    "ds_read_b64_tr_b16 v[4:5], v225\n"
    "ds_read_b64_tr_b16 v[6:7], v225 offset:512\n"
    "ds_read_b64_tr_b16 v[8:9], v226\n"
    "s_waitcnt lgkmcnt(14)\n"
    "v_mfma_f32_16x16x32_bf16 v[120:123], v[108:111], v[100:103], v[196:199]\n"
    "ds_read_b64_tr_b16 v[10:11], v226 offset:512\n"
    "ds_read_b64_tr_b16 v[12:13], v227\n"
    "v_mfma_f32_16x16x32_bf16 v[124:127], v[104:107], v[100:103], v[192:195]\n"
    "ds_read_b64_tr_b16 v[14:15], v227 offset:512\n"
    "ds_read_b64_tr_b16 v[16:17], v224 offset:4096\n"
    "v_mfma_f32_16x16x32_bf16 v[152:155], v[140:143], v[132:135], v[196:199]\n"
    "ds_read_b64_tr_b16 v[18:19], v224 offset:4608\n"
    "ds_read_b64_tr_b16 v[20:21], v225 offset:4096\n"
    "s_waitcnt lgkmcnt(14)\n"
    "v_mfma_f32_16x16x32_bf16 v[156:159], v[136:139], v[132:135], v[192:195]\n"
    "ds_read_b64_tr_b16 v[22:23], v225 offset:4608\n"
    "ds_read_b64_tr_b16 v[24:25], v226 offset:4096\n"
    "v_maximum3_f32 v120, v120, v124, v124\n"
    "v_maximum3_f32 v121, v121, v125, v125\n"
    "v_maximum3_f32 v122, v122, v126, v126\n"
    "v_maximum3_f32 v123, v123, v127, v127\n"
    "v_maximum3_f32 v128, v120, v121, v122\n"
    "v_maximum3_f32 v129, v128, v123, v123\n"
    "v_maximum3_f32 v128, v128, v123, v123\n"
    "v_maximum3_f32 v152, v152, v156, v156\n"
    "v_maximum3_f32 v153, v153, v157, v157\n"
    "v_permlane32_swap_b32 v128, v129\n"
    "v_maximum3_f32 v124, v128, v129, v129\n"
    "v_maximum3_f32 v129, v128, v129, v129\n"
    "v_maximum3_f32 v154, v154, v158, v158\n"
    "v_maximum3_f32 v155, v155, v159, v159\n"
    "v_permlane16_swap_b32 v124, v129\n"
    "v_maximum3_f32 v128, v124, v129, v129\n"
    "v_mul_f32 v128, 0xbea52b5b, v128\n"
    "v_pk_fma_f32 v[120:121], v[120:121], s[4:5], v[128:129] op_sel_hi:[1,0,0]\n"
    "v_pk_fma_f32 v[122:123], v[122:123], s[4:5], v[128:129] op_sel_hi:[1,0,0]\n"
    "v_exp_f32 v120, v120\n"
    "v_exp_f32 v121, v121\n"
    "v_exp_f32 v122, v122\n"
    "v_exp_f32 v123, v123\n"
    "v_cvt_pk_bf16_f32 v120, v120, v121\n"
    "v_cvt_pk_bf16_f32 v122, v122, v123\n"
    "v_and_b32 v100, v200, v120\n"
    "v_and_b32 v102, v202, v120\n"
    "v_and_b32 v101, v201, v122\n"
    "v_and_b32 v103, v203, v122\n"
    "v_maximum3_f32 v160, v152, v153, v154\n"
    "v_maximum3_f32 v161, v160, v155, v155\n"
    "v_mfma_f32_16x16x32_bf16 v[104:107], v[252:255], v[100:103], 0\n"
    "v_maximum3_f32 v160, v160, v155, v155\n"
    "ds_read_b64_tr_b16 v[26:27], v226 offset:4608\n"
    "v_mfma_f32_16x16x32_bf16 v[108:111], v[112:115], v[100:103], 0\n"
    "v_permlane32_swap_b32 v160, v161\n"
    "v_maximum3_f32 v156, v160, v161, v161\n"
    "v_mfma_f32_16x16x32_bf16 v[124:127], v[116:119], v[100:103], 0\n"
    "v_maximum3_f32 v161, v160, v161, v161\n"
    "ds_read_b64_tr_b16 v[28:29], v227 offset:4096\n"
    "v_rcp_f32 v128, v104\n"
    "v_permlane16_swap_b32 v156, v161\n"
    "v_maximum3_f32 v160, v156, v161, v161\n"
    "v_mul_f32 v160, 0xbea52b5b, v160\n"
    "v_pk_mul_f32 v[108:109], v[108:109], v[128:129] op_sel_hi:[1,0]\n"
    "v_pk_mul_f32 v[110:111], v[110:111], v[128:129] op_sel_hi:[1,0]\n"
    "v_pk_mul_f32 v[124:125], v[124:125], v[128:129] op_sel_hi:[1,0]\n"
    "v_pk_mul_f32 v[126:127], v[126:127], v[128:129] op_sel_hi:[1,0]\n"
    "v_pk_fma_f32 v[152:153], v[152:153], s[4:5], v[160:161] op_sel_hi:[1,0,0]\n"
    "v_pk_fma_f32 v[154:155], v[154:155], s[4:5], v[160:161] op_sel_hi:[1,0,0]\n"
    "v_cvt_pk_bf16_f32 v120, v108, v109\n"
    "v_cvt_pk_bf16_f32 v121, v110, v111\n"
    "v_cvt_pk_bf16_f32 v122, v124, v125\n"
    "v_cvt_pk_bf16_f32 v123, v126, v127\n"
    "v_exp_f32 v152, v152\n"
    "v_exp_f32 v153, v153\n"
    "v_exp_f32 v154, v154\n"
    "v_exp_f32 v155, v155\n"
    "buffer_store_dwordx2 v[120:121], v207, s[48:51], s72 offen sc1\n"
    "buffer_store_dwordx4 v[120:123], v206, s[48:51], s72 offen sc1\n"
    "v_cvt_pk_bf16_f32 v152, v152, v153\n"
    "v_cvt_pk_bf16_f32 v154, v154, v155\n"
    "v_and_b32 v132, v200, v152\n"
    "v_and_b32 v134, v202, v152\n"
    "v_and_b32 v133, v201, v154\n"
    "v_and_b32 v135, v203, v154\n"
    "ds_read_b64_tr_b16 v[30:31], v227 offset:4608\n"
    "ds_read_b64_tr_b16 v[32:33], v224 offset:8192\n"
    "v_mfma_f32_16x16x32_bf16 v[136:139], v[252:255], v[132:135], 0\n"
    "ds_read_b64_tr_b16 v[34:35], v224 offset:8704\n"
    "ds_read_b64_tr_b16 v[36:37], v225 offset:8192\n"
    "s_waitcnt lgkmcnt(14)\n"
    "v_mfma_f32_16x16x32_bf16 v[140:143], v[144:147], v[132:135], 0\n"
    "ds_read_b64_tr_b16 v[38:39], v225 offset:8704\n"
    "ds_read_b64_tr_b16 v[40:41], v226 offset:8192\n"
    "v_mfma_f32_16x16x32_bf16 v[156:159], v[148:151], v[132:135], 0\n"
    "ds_read_b64_tr_b16 v[42:43], v226 offset:8704\n"
    "ds_read_b64_tr_b16 v[44:45], v227 offset:8192\n"
    "v_rcp_f32 v160, v136\n"
    "ds_read_b64_tr_b16 v[46:47], v227 offset:8704\n"
    "ds_read_b64_tr_b16 v[48:49], v224 offset:12288\n"
    "v_pk_mul_f32 v[140:141], v[140:141], v[160:161] op_sel_hi:[1,0]\n"
    "v_pk_mul_f32 v[142:143], v[142:143], v[160:161] op_sel_hi:[1,0]\n"
    "v_cvt_pk_bf16_f32 v152, v140, v141\n"
    "v_pk_mul_f32 v[156:157], v[156:157], v[160:161] op_sel_hi:[1,0]\n"
    "v_pk_mul_f32 v[158:159], v[158:159], v[160:161] op_sel_hi:[1,0]\n"
    "v_cvt_pk_bf16_f32 v153, v142, v143\n"
    "v_cvt_pk_bf16_f32 v154, v156, v157\n"
    "v_cvt_pk_bf16_f32 v155, v158, v159\n"
    "buffer_store_dwordx2 v[152:153], v207, s[48:51], s73 offen sc1\n"
    "buffer_store_dwordx4 v[152:155], v206, s[48:51], s73 offen sc1\n"
    "ds_read_b64_tr_b16 v[50:51], v224 offset:12800\n"
    "ds_read_b64_tr_b16 v[52:53], v225 offset:12288\n"
    "ds_read_b64_tr_b16 v[54:55], v225 offset:12800\n"
    "ds_read_b64_tr_b16 v[56:57], v226 offset:12288\n"
    "ds_read_b64_tr_b16 v[58:59], v226 offset:12800\n"
    "ds_read_b64_tr_b16 v[60:61], v227 offset:12288\n"
    "ds_read_b64_tr_b16 v[62:63], v227 offset:12800\n"
    "ds_read_b64_tr_b16 v[64:65], v224 offset:16384\n"
    "ds_read_b64_tr_b16 v[66:67], v224 offset:16896\n"
    "ds_read_b64_tr_b16 v[68:69], v225 offset:16384\n"
    "ds_read_b64_tr_b16 v[70:71], v225 offset:16896\n"
    "ds_read_b64_tr_b16 v[72:73], v226 offset:16384\n"
    "ds_read_b64_tr_b16 v[74:75], v226 offset:16896\n"
    "ds_read_b64_tr_b16 v[76:77], v227 offset:16384\n"
    "ds_read_b64_tr_b16 v[78:79], v227 offset:16896\n"
    "s_branch PRE%=\n"
    "RX%=:\n"
    "buffer_load_dwordx2 v[102:103], v240, s[60:63], 0 offen sc1\n"
    "s_barrier\n"
    "s_add_u32 m0, s79, 112640\n"
    "s_mov_b32 s2, 76800\n"
    "buffer_load_dwordx4 v236, s[44:47], s2 offen lds\n"
    "s_add_u32 m0, s79, 116736\n"
    "s_mov_b32 s2, 153600\n"
    "buffer_load_dwordx4 v236, s[44:47], s2 offen lds\n"
    "s_add_u32 m0, s79, 120832\n"
    "s_mov_b32 s2, 230400\n"
    "buffer_load_dwordx4 v236, s[44:47], s2 offen lds\n"
    "s_add_u32 m0, s79, 124928\n"
    "s_mov_b32 s2, 307200\n"
    "buffer_load_dwordx4 v236, s[44:47], s2 offen lds\n"
    "s_add_u32 m0, s79, 129024\n"
    "s_mov_b32 s2, 76800\n"
    "buffer_load_dwordx4 v237, s[44:47], s2 offen lds\n"
    "s_add_u32 m0, s80, 130048\n"
    "s_nop 0\n"
    "buffer_load_dwordx4 v176, s[40:43], s78 offen offset:3072 lds\n"
    "s_add_u32 m0, s80, 132096\n"
    "s_nop 0\n"
    "buffer_load_dwordx4 v177, s[40:43], s78 offen offset:3072 lds\n"
    "s_add_u32 m0, s80, 137216\n"
    "s_nop 0\n"
    "buffer_load_dwordx4 v178, s[40:43], s78 offen lds\n"
    "s_add_u32 s14, s76, 0\n"
    "s_add_u32 s15, s76, 7680\n"
    "s_add_u32 s16, s76, 61440\n"
    "s_add_u32 s17, s76, 15360\n"
    "s_add_u32 s18, s76, 23040\n"
    "s_add_u32 s19, s76, 76800\n"
    "s_movk_i32 s6, 4096\n"
    "s_mov_b32 s3, 262144\n"
    "s_mov_b32 s4, 0x3ea52b5b\n"
    "s_bitcmp1_b32 s77, 3\n"
    "s_cselect_b32 s7, s68, 0\n"
    "v_and_b32 v251, s68, v251\n"
    "s_waitcnt vmcnt(8)\n"
    "s_waitcnt lgkmcnt(0)\n"
    "v_subrev_u32 v102, s28, v102\n"
    "s_nop 1\n"
    "v_max_i32_dpp v102, v102, v102 quad_perm:[1,0,3,2] row_mask:0xf bank_mask:0xf\n"
    "s_nop 1\n"
    "v_max_i32_dpp v102, v102, v102 quad_perm:[2,3,0,1] row_mask:0xf bank_mask:0xf\n"
    "s_nop 1\n"
    "v_max_i32_dpp v102, v102, v102 row_half_mirror row_mask:0xf bank_mask:0xf\n"
    "s_nop 1\n"
    "v_max_i32_dpp v102, v102, v102 row_mirror row_mask:0xf bank_mask:0xf\n"
    "s_nop 0\n"
    "v_readlane_b32 s1, v102, 0\n"
    "v_readlane_b32 s2, v102, 16\n"
    "v_readlane_b32 s35, v102, 32\n"
    "s_max_i32 s2, s1, s2\n"
    "s_sub_u32 s5, s2, s1\n"
    "s_min_u32 s5, s5, 200\n"
    "s_min_i32 s1, s1, 200\n"
    "s_mul_i32 s1, s1, 5\n"
    "s_ashr_i32 s34, s1, 2\n"
    "s_min_i32 s35, s35, 200\n"
    "s_mul_i32 s35, s35, 5\n"
    "s_ashr_i32 s35, s35, 2\n"
    "v_cmp_ne_u32 vcc, s68, v103\n"
    "s_barrier\n"
    "s_memrealtime s[26:27]\n"
    "s_setprio 3\n"
    "s_cbranch_vccz HPD%=\n"
    "HPL%=:\n"
    "buffer_load_dwordx2 v[102:103], v240, s[60:63], 0 offen sc1\n"
    "s_waitcnt vmcnt(0)\n"
    "v_cmp_ne_u32 vcc, s68, v103\n"
    "s_cbranch_vccz HPE%=\n"
    "s_sleep 2\n"
    "s_sub_u32 s3, s3, 1\n"
    "s_cbranch_scc0 HPL%=\n"
    "HPE%=:\n"
    "s_waitcnt lgkmcnt(0)\n"
    "v_subrev_u32 v102, s28, v102\n"
    "s_nop 1\n"
    "v_max_i32_dpp v102, v102, v102 quad_perm:[1,0,3,2] row_mask:0xf bank_mask:0xf\n"
    "s_nop 1\n"
    "v_max_i32_dpp v102, v102, v102 quad_perm:[2,3,0,1] row_mask:0xf bank_mask:0xf\n"
    "s_nop 1\n"
    "v_max_i32_dpp v102, v102, v102 row_half_mirror row_mask:0xf bank_mask:0xf\n"
    "s_nop 1\n"
    "v_max_i32_dpp v102, v102, v102 row_mirror row_mask:0xf bank_mask:0xf\n"
    "s_nop 0\n"
    "v_readlane_b32 s1, v102, 0\n"
    "v_readlane_b32 s2, v102, 16\n"
    "v_readlane_b32 s35, v102, 32\n"
    "s_max_i32 s2, s1, s2\n"
    "s_sub_u32 s5, s2, s1\n"
    "s_min_u32 s5, s5, 200\n"
    "s_min_i32 s1, s1, 200\n"
    "s_mul_i32 s1, s1, 5\n"
    "s_ashr_i32 s34, s1, 2\n"
    "s_min_i32 s35, s35, 200\n"
    "s_mul_i32 s35, s35, 5\n"
    "s_ashr_i32 s35, s35, 2\n"
    "HPD%=:\n"
    "s_waitcnt lgkmcnt(0)\n"
    "s_add_u32 s34, s34, s26\n"
    "s_add_u32 s35, s35, s26\n"
    "s_add_u32 s2, s35, 8\n"
    "URX%=:\n"
    "s_memrealtime s[0:1]\n"
    "s_waitcnt lgkmcnt(0)\n"
    "s_sub_u32 s0, s0, s2\n"
    "s_cmp_lt_i32 s0, 0\n"
    "s_cbranch_scc1 URX%=\n"
    "buffer_load_dword v144, v242, s[60:63], 0 offen\n"
    "buffer_load_dwordx4 v[100:103], v244, s[52:55], 0 offen sc1\n"
    "buffer_load_dwordx4 v[104:107], v245, s[52:55], 0 offen offset:512 sc1\n"
    "buffer_load_dwordx4 v[108:111], v245, s[52:55], 0 offen offset:1024 sc1\n"
    "buffer_load_dwordx4 v[112:115], v246, s[52:55], 0 offen sc1\n"
    "buffer_load_dwordx4 v[116:119], v246, s[52:55], 0 offen offset:512 sc1\n"
    "buffer_load_dwordx4 v[120:123], v243, s[52:55], s6 offen sc1\n"
    "buffer_load_dwordx4 v[124:127], v243, s[52:55], s6 offen offset:1024 sc1\n"
    "buffer_load_dwordx4 v[128:131], v243, s[52:55], 0 offen sc1\n"
    "buffer_load_dwordx4 v[132:135], v243, s[52:55], 0 offen offset:1024 sc1\n"
    "buffer_load_dwordx4 v[136:139], v243, s[52:55], 0 offen offset:2048 sc1\n"
    "buffer_load_dwordx4 v[140:143], v243, s[52:55], 0 offen offset:3072 sc1\n"
    "ds_read_b64_tr_b16 v[0:1], v224\n"
    "ds_read_b64_tr_b16 v[2:3], v224 offset:512\n"
    "ds_read_b64_tr_b16 v[4:5], v225\n"
    "ds_read_b64_tr_b16 v[6:7], v225 offset:512\n"
    "ds_read_b64_tr_b16 v[8:9], v226\n"
    "ds_read_b64_tr_b16 v[10:11], v226 offset:512\n"
    "ds_read_b64_tr_b16 v[12:13], v227\n"
    "ds_read_b64_tr_b16 v[14:15], v227 offset:512\n"
    "ds_read_b64_tr_b16 v[16:17], v224 offset:4096\n"
    "ds_read_b64_tr_b16 v[18:19], v224 offset:4608\n"
    "ds_read_b64_tr_b16 v[20:21], v225 offset:4096\n"
    "ds_read_b64_tr_b16 v[22:23], v225 offset:4608\n"
    "ds_read_b64_tr_b16 v[24:25], v226 offset:4096\n"
    "ds_read_b64_tr_b16 v[26:27], v226 offset:4608\n"
    "ds_read_b64_tr_b16 v[28:29], v227 offset:4096\n"
    "ds_read_b64_tr_b16 v[30:31], v227 offset:4608\n"
    "ds_read_b64_tr_b16 v[32:33], v224 offset:8192\n"
    "ds_read_b64_tr_b16 v[34:35], v224 offset:8704\n"
    "ds_read_b64_tr_b16 v[36:37], v225 offset:8192\n"
    "ds_read_b64_tr_b16 v[38:39], v225 offset:8704\n"
    "ds_read_b64_tr_b16 v[40:41], v226 offset:8192\n"
    "ds_read_b64_tr_b16 v[42:43], v226 offset:8704\n"
    "ds_read_b64_tr_b16 v[44:45], v227 offset:8192\n"
    "ds_read_b64_tr_b16 v[46:47], v227 offset:8704\n"
    "ds_read_b64_tr_b16 v[48:49], v224 offset:12288\n"
    "ds_read_b64_tr_b16 v[50:51], v224 offset:12800\n"
    "ds_read_b64_tr_b16 v[52:53], v225 offset:12288\n"
    "ds_read_b64_tr_b16 v[54:55], v225 offset:12800\n"
    "ds_read_b64_tr_b16 v[56:57], v226 offset:12288\n"
    "ds_read_b64_tr_b16 v[58:59], v226 offset:12800\n"
    "ds_read_b64_tr_b16 v[60:61], v227 offset:12288\n"
    "ds_read_b64_tr_b16 v[62:63], v227 offset:12800\n"
    "ds_read_b64_tr_b16 v[64:65], v224 offset:16384\n"
    "ds_read_b64_tr_b16 v[66:67], v224 offset:16896\n"
    "ds_read_b64_tr_b16 v[68:69], v225 offset:16384\n"
    "ds_read_b64_tr_b16 v[70:71], v225 offset:16896\n"
    "ds_read_b64_tr_b16 v[72:73], v226 offset:16384\n"
    "ds_read_b64_tr_b16 v[74:75], v226 offset:16896\n"
    "ds_read_b64_tr_b16 v[76:77], v227 offset:16384\n"
    "ds_read_b64_tr_b16 v[78:79], v227 offset:16896\n"
    "v_mov_b32 v152, 156544\n"
    "ds_read_b128 v[156:159], v152\n"
    "s_waitcnt lgkmcnt(0)\n"
    "v_readfirstlane_b32 s20, v156\n"
    "v_readfirstlane_b32 s21, v158\n"
    "s_andn2_b32 s24, 2047, s20\n"
    "s_lshr_b32 s22, s24, 1\n"
    "s_sub_u32 s22, s22, s82\n"
    "s_and_b32 s24, s24, 1\n"
    "s_cmp_eq_u32 s24, s81\n"
    "s_cselect_b32 s22, s22, -1\n"
    "s_andn2_b32 s24, 2047, s21\n"
    "s_lshr_b32 s23, s24, 1\n"
    "s_sub_u32 s23, s23, s82\n"
    "s_and_b32 s24, s24, 1\n"
    "s_cmp_eq_u32 s24, s81\n"
    "s_cselect_b32 s23, s23, -1\n"
    "s_min_u32 s25, s22, s23\n"
    "s_or_b32 s34, s34, 1\n"
    "v_mov_b32 v154, 156736\n"
    "v_mov_b32 v156, s34\n"
    "v_mov_b32 v157, s5\n"
    "s_mov_b32 s25, 0\n"
    "s_min_u32 s22, s22, 127\n"
    "s_lshr_b32 s24, s22, 4\n"
    "s_bitset1_b32 s25, s24\n"
    "s_min_u32 s23, s23, 127\n"
    "s_lshr_b32 s24, s23, 4\n"
    "s_bitset1_b32 s25, s24\n"
    "s_and_b32 s25, s25, 63\n"
    "s_pack_ll_b32_b16 s22, s22, s23\n"
    "v_mov_b32 v158, s25\n"
    "v_mov_b32 v159, s22\n"
    "RXP%=:\n"
    "s_waitcnt vmcnt(8)\n"
    "ds_write_b64 v248, v[100:101]\n"
    "v_xor_b32 v151, s7, v103\n"
    "v_xor_b32 v152, s7, v107\n"
    "v_mov_b32 v103, v106\n"
    "v_or_b32 v150, v151, v152\n"
    "ds_write_b64 v248, v[102:103] offset:17472\n"
    "ds_write_b64 v249, v[104:105]\n"
    "ds_write_b64 v249, v[108:109] offset:17472\n"
    "v_xor_b32 v151, s7, v111\n"
    "v_or_b32 v150, v150, v151\n"
    "v_xor_b32 v153, s68, v144\n"
    "s_waitcnt vmcnt(6)\n"
    "ds_write_b64 v250, v[112:113]\n"
    "v_xor_b32 v151, v251, v115\n"
    "v_xor_b32 v152, v251, v119\n"
    "v_mov_b32 v115, v118\n"
    "v_or3_b32 v150, v150, v151, v152\n"
    "ds_write_b64 v250, v[114:115] offset:17472\n"
    "s_waitcnt vmcnt(4)\n"
    "ds_write_b64 v247, v[120:121] offset:22848\n"
    "v_xor_b32 v151, s68, v123\n"
    "v_xor_b32 v152, s68, v127\n"
    "v_mov_b32 v123, v126\n"
    "v_or3_b32 v150, v150, v151, v152\n"
    "ds_write_b64 v247, v[122:123] offset:40320\n"
    "ds_write_b64 v247, v[124:125] offset:5376\n"
    "s_waitcnt vmcnt(2)\n"
    "ds_write_b64 v247, v[128:129] offset:17472\n"
    "v_xor_b32 v151, s68, v131\n"
    "v_xor_b32 v152, s68, v135\n"
    "v_mov_b32 v131, v134\n"
    "v_or3_b32 v150, v150, v151, v152\n"
    "ds_write_b64 v247, v[130:131] offset:34944\n"
    "ds_write_b64 v247, v[132:133]\n"
    "s_waitcnt vmcnt(0)\n"
    "ds_write_b64 v247, v[136:137] offset:20160\n"
    "v_xor_b32 v151, s68, v139\n"
    "v_xor_b32 v152, s68, v143\n"
    "v_mov_b32 v139, v142\n"
    "v_or3_b32 v150, v150, v151, v152\n"
    "ds_write_b64 v247, v[138:139] offset:37632\n"
    "ds_write_b64 v247, v[140:141] offset:2688\n"
    "v_or_b32 v153, v153, v150\n"
    "v_cmp_ne_u32 vcc, 0, v153\n"
    "s_cbranch_vccz RXD%=\n"
    "s_waitcnt lgkmcnt(0)\n"
    "s_sub_u32 s3, s3, 1\n"
    "s_cbranch_scc1 RXD%=\n"
    "v_cmp_ne_u32 vcc, s68, v144\n"
    "s_cbranch_vccz RXA%=\n"
    "M2L%=:\n"
    "s_sleep 6\n"
    "buffer_load_dword v144, v242, s[60:63], 0 offen sc1\n"
    "s_waitcnt vmcnt(0)\n"
    "v_cmp_ne_u32 vcc, s68, v144\n"
    "s_cbranch_vccz M2D%=\n"
    "s_sub_u32 s3, s3, 1\n"
    "s_cbranch_scc0 M2L%=\n"
    "M2D%=:\n"
    "v_cmp_ne_u32 vcc, 0, v150\n"
    "s_cbranch_vccz RXD%=\n"
    "RXA%=:\n"
    "buffer_load_dword v144, v242, s[60:63], 0 offen sc1\n"
    "buffer_load_dwordx4 v[100:103], v244, s[52:55], 0 offen sc1\n"
    "buffer_load_dwordx4 v[104:107], v245, s[52:55], 0 offen offset:512 sc1\n"
    "buffer_load_dwordx4 v[108:111], v245, s[52:55], 0 offen offset:1024 sc1\n"
    "buffer_load_dwordx4 v[112:115], v246, s[52:55], 0 offen sc1\n"
    "buffer_load_dwordx4 v[116:119], v246, s[52:55], 0 offen offset:512 sc1\n"
    "buffer_load_dwordx4 v[120:123], v243, s[52:55], s6 offen sc1\n"
    "buffer_load_dwordx4 v[124:127], v243, s[52:55], s6 offen offset:1024 sc1\n"
    "buffer_load_dwordx4 v[128:131], v243, s[52:55], 0 offen sc1\n"
    "buffer_load_dwordx4 v[132:135], v243, s[52:55], 0 offen offset:1024 sc1\n"
    "buffer_load_dwordx4 v[136:139], v243, s[52:55], 0 offen offset:2048 sc1\n"
    "buffer_load_dwordx4 v[140:143], v243, s[52:55], 0 offen offset:3072 sc1\n"
    "s_waitcnt vmcnt(0)\n"
    "s_branch RXP%=\n"
    "RXD%=:\n"
    "ds_write_b128 v154, v[156:159]\n"
    "s_branch LATE%=\n"
    "PRE%=:\n"
    "s_cmp_lt_u32 s69, 4\n"
    "s_cbranch_scc1 FULL%=\n"
    "s_setprio 3\n"
    "s_cmp_ge_i32 s72, 0\n"
    "s_cbranch_scc1 A1B%=\n"
    "ds_read_b64_tr_b16 v[0:1], v224\n"
    "ds_read_b64_tr_b16 v[2:3], v224 offset:512\n"
    "ds_read_b64_tr_b16 v[4:5], v225\n"
    "ds_read_b64_tr_b16 v[6:7], v225 offset:512\n"
    "ds_read_b64_tr_b16 v[8:9], v226\n"
    "ds_read_b64_tr_b16 v[10:11], v226 offset:512\n"
    "ds_read_b64_tr_b16 v[12:13], v227\n"
    "ds_read_b64_tr_b16 v[14:15], v227 offset:512\n"
    "ds_read_b64_tr_b16 v[16:17], v224 offset:4096\n"
    "ds_read_b64_tr_b16 v[18:19], v224 offset:4608\n"
    "ds_read_b64_tr_b16 v[20:21], v225 offset:4096\n"
    "ds_read_b64_tr_b16 v[22:23], v225 offset:4608\n"
    "ds_read_b64_tr_b16 v[24:25], v226 offset:4096\n"
    "ds_read_b64_tr_b16 v[26:27], v226 offset:4608\n"
    "ds_read_b64_tr_b16 v[28:29], v227 offset:4096\n"
    "ds_read_b64_tr_b16 v[30:31], v227 offset:4608\n"
    "ds_read_b64_tr_b16 v[32:33], v224 offset:8192\n"
    "ds_read_b64_tr_b16 v[34:35], v224 offset:8704\n"
    "ds_read_b64_tr_b16 v[36:37], v225 offset:8192\n"
    "ds_read_b64_tr_b16 v[38:39], v225 offset:8704\n"
    "ds_read_b64_tr_b16 v[40:41], v226 offset:8192\n"
    "ds_read_b64_tr_b16 v[42:43], v226 offset:8704\n"
    "ds_read_b64_tr_b16 v[44:45], v227 offset:8192\n"
    "ds_read_b64_tr_b16 v[46:47], v227 offset:8704\n"
    "ds_read_b64_tr_b16 v[48:49], v224 offset:12288\n"
    "ds_read_b64_tr_b16 v[50:51], v224 offset:12800\n"
    "ds_read_b64_tr_b16 v[52:53], v225 offset:12288\n"
    "ds_read_b64_tr_b16 v[54:55], v225 offset:12800\n"
    "ds_read_b64_tr_b16 v[56:57], v226 offset:12288\n"
    "ds_read_b64_tr_b16 v[58:59], v226 offset:12800\n"
    "ds_read_b64_tr_b16 v[60:61], v227 offset:12288\n"
    "ds_read_b64_tr_b16 v[62:63], v227 offset:12800\n"
    "ds_read_b64_tr_b16 v[64:65], v224 offset:16384\n"
    "ds_read_b64_tr_b16 v[66:67], v224 offset:16896\n"
    "ds_read_b64_tr_b16 v[68:69], v225 offset:16384\n"
    "ds_read_b64_tr_b16 v[70:71], v225 offset:16896\n"
    "ds_read_b64_tr_b16 v[72:73], v226 offset:16384\n"
    "ds_read_b64_tr_b16 v[74:75], v226 offset:16896\n"
    "ds_read_b64_tr_b16 v[76:77], v227 offset:16384\n"
    "ds_read_b64_tr_b16 v[78:79], v227 offset:16896\n"
    "A1B%=:\n"
    "s_mov_b32 s3, 16777216\n"
    "v_mov_b32 v144, 156736\n"
    "RXW%=:\n"
    "ds_read_b128 v[140:143], v144\n"
    "s_waitcnt lgkmcnt(0)\n"
    "v_cmp_ne_u32 vcc, 0, v140\n"
    "s_cbranch_vccnz RXWD%=\n"
    "s_sleep 1\n"
    "s_sub_u32 s3, s3, 1\n"
    "s_cbranch_scc0 RXW%=\n"
    "RXWD%=:\n"
    "v_readfirstlane_b32 s34, v140\n"
    "v_readfirstlane_b32 s5, v141\n"
    "v_readfirstlane_b32 s25, v142\n"
    "v_readfirstlane_b32 s22, v143\n"
    "LATE%=:\n"
    "ds_read_b64 v[100:101], v216\n"
    "ds_read_b64 v[102:103], v218\n"
    "ds_read_b64 v[108:109], v216 offset:17472\n"
    "ds_read_b64 v[110:111], v217 offset:17472\n"
    "ds_read_b64 v[104:105], v216 offset:14784\n"
    "ds_read_b64 v[106:107], v217 offset:14784\n"
    "ds_read_b64 v[132:133], v220\n"
    "ds_read_b64 v[134:135], v222\n"
    "ds_read_b64 v[140:141], v220 offset:17472\n"
    "ds_read_b64 v[142:143], v221 offset:17472\n"
    "ds_read_b64 v[136:137], v220 offset:14784\n"
    "ds_read_b64 v[138:139], v221 offset:14784\n"
    "ds_read_b64_tr_b16 v[112:113], v219\n"
    "ds_read_b64_tr_b16 v[114:115], v219 offset:2688\n"
    "ds_read_b64_tr_b16 v[144:145], v223\n"
    "ds_read_b64_tr_b16 v[146:147], v223 offset:2688\n"
    "ds_read_b64_tr_b16 v[116:117], v219 offset:16\n"
    "ds_read_b64_tr_b16 v[118:119], v219 offset:2704\n"
    "ds_read_b64_tr_b16 v[148:149], v223 offset:16\n"
    "ds_read_b64_tr_b16 v[150:151], v223 offset:2704\n"
    "s_waitcnt lgkmcnt(14)\n"
    "v_mfma_f32_16x16x32_bf16 v[120:123], v[108:111], v[100:103], v[196:199]\n"
    "v_mfma_f32_16x16x32_bf16 v[124:127], v[104:107], v[100:103], v[168:171]\n"
    "s_waitcnt lgkmcnt(10)\n"
    "v_mfma_f32_16x16x32_bf16 v[152:155], v[140:143], v[132:135], v[196:199]\n"
    "s_waitcnt lgkmcnt(8)\n"
    "v_mfma_f32_16x16x32_bf16 v[156:159], v[136:139], v[132:135], v[172:175]\n"
    "s_nop 5\n"
    "v_maximum3_f32 v120, v120, v124, v124\n"
    "v_maximum3_f32 v121, v121, v125, v125\n"
    "v_maximum3_f32 v122, v122, v126, v126\n"
    "v_maximum3_f32 v128, v120, v121, v122\n"
    "v_maximum3_f32 v123, v123, v127, v127\n"
    "v_maximum3_f32 v129, v128, v123, v123\n"
    "v_maximum3_f32 v128, v128, v123, v123\n"
    "v_maximum3_f32 v152, v152, v156, v156\n"
    "v_maximum3_f32 v153, v153, v157, v157\n"
    "v_maximum3_f32 v154, v154, v158, v158\n"
    "v_maximum3_f32 v155, v155, v159, v159\n"
    "v_maximum3_f32 v160, v152, v153, v154\n"
    "v_maximum3_f32 v161, v160, v155, v155\n"
    "v_maximum3_f32 v160, v160, v155, v155\n"
    "v_permlane32_swap_b32 v128, v129\n"
    "v_maximum3_f32 v124, v128, v129, v129\n"
    "v_permlane32_swap_b32 v160, v161\n"
    "v_maximum3_f32 v156, v160, v161, v161\n"
    "v_maximum3_f32 v129, v128, v129, v129\n"
    "v_maximum3_f32 v161, v160, v161, v161\n"
    "s_nop 0\n"
    "v_permlane16_swap_b32 v124, v129\n"
    "v_maximum3_f32 v128, v124, v129, v129\n"
    "v_permlane16_swap_b32 v156, v161\n"
    "v_maximum3_f32 v160, v156, v161, v161\n"
    "v_mul_f32 v128, 0xbea52b5b, v128\n"
    "v_mul_f32 v160, 0xbea52b5b, v160\n"
    "v_pk_fma_f32 v[120:121], v[120:121], s[4:5], v[128:129] op_sel_hi:[1,0,0]\n"
    "v_pk_fma_f32 v[122:123], v[122:123], s[4:5], v[128:129] op_sel_hi:[1,0,0]\n"
    "v_pk_fma_f32 v[152:153], v[152:153], s[4:5], v[160:161] op_sel_hi:[1,0,0]\n"
    "v_pk_fma_f32 v[154:155], v[154:155], s[4:5], v[160:161] op_sel_hi:[1,0,0]\n"
    "v_exp_f32 v120, v120\n"
    "v_exp_f32 v121, v121\n"
    "v_exp_f32 v122, v122\n"
    "v_exp_f32 v123, v123\n"
    "v_exp_f32 v152, v152\n"
    "v_exp_f32 v153, v153\n"
    "v_exp_f32 v154, v154\n"
    "v_exp_f32 v155, v155\n"
    "v_cvt_pk_bf16_f32 v120, v120, v121\n"
    "v_cvt_pk_bf16_f32 v122, v122, v123\n"
    "v_cvt_pk_bf16_f32 v152, v152, v153\n"
    "v_cvt_pk_bf16_f32 v154, v154, v155\n"
    "v_and_b32 v100, v200, v120\n"
    "v_and_b32 v102, v202, v120\n"
    "v_and_b32 v101, v201, v122\n"
    "v_and_b32 v103, v203, v122\n"
    "v_and_b32 v132, v200, v152\n"
    "v_and_b32 v134, v202, v152\n"
    "v_mfma_f32_16x16x32_bf16 v[104:107], v[252:255], v[100:103], 0\n"
    "v_and_b32 v133, v201, v154\n"
    "v_and_b32 v135, v203, v154\n"
    "s_waitcnt lgkmcnt(6)\n"
    "v_mfma_f32_16x16x32_bf16 v[108:111], v[112:115], v[100:103], 0\n"
    "s_nop 0\n"
    "v_mfma_f32_16x16x32_bf16 v[136:139], v[252:255], v[132:135], 0\n"
    "s_waitcnt lgkmcnt(4)\n"
    "v_mfma_f32_16x16x32_bf16 v[140:143], v[144:147], v[132:135], 0\n"
    "s_waitcnt lgkmcnt(2)\n"
    "v_mfma_f32_16x16x32_bf16 v[124:127], v[116:119], v[100:103], 0\n"
    "s_waitcnt lgkmcnt(0)\n"
    "v_mfma_f32_16x16x32_bf16 v[156:159], v[148:151], v[132:135], 0\n"
    "v_rcp_f32 v128, v104\n"
    "s_nop 1\n"
    "v_pk_mul_f32 v[108:109], v[108:109], v[128:129] op_sel_hi:[1,0]\n"
    "v_cvt_pk_bf16_f32 v120, v108, v109\n"
    "v_pk_mul_f32 v[110:111], v[110:111], v[128:129] op_sel_hi:[1,0]\n"
    "v_cvt_pk_bf16_f32 v121, v110, v111\n"
    "v_rcp_f32 v160, v136\n"
    "buffer_store_dwordx2 v[120:121], v187, s[48:51], s74 offen sc1\n"
    "v_pk_mul_f32 v[124:125], v[124:125], v[128:129] op_sel_hi:[1,0]\n"
    "v_pk_mul_f32 v[126:127], v[126:127], v[128:129] op_sel_hi:[1,0]\n"
    "v_pk_mul_f32 v[140:141], v[140:141], v[160:161] op_sel_hi:[1,0]\n"
    "v_pk_mul_f32 v[142:143], v[142:143], v[160:161] op_sel_hi:[1,0]\n"
    "v_pk_mul_f32 v[156:157], v[156:157], v[160:161] op_sel_hi:[1,0]\n"
    "v_pk_mul_f32 v[158:159], v[158:159], v[160:161] op_sel_hi:[1,0]\n"
    "v_cvt_pk_bf16_f32 v122, v124, v125\n"
    "v_cvt_pk_bf16_f32 v123, v126, v127\n"
    "v_cvt_pk_bf16_f32 v152, v140, v141\n"
    "v_cvt_pk_bf16_f32 v153, v142, v143\n"
    "v_cvt_pk_bf16_f32 v154, v156, v157\n"
    "v_cvt_pk_bf16_f32 v155, v158, v159\n"
    "buffer_store_dwordx4 v[120:123], v186, s[48:51], s74 offen sc1\n"
    "buffer_store_dwordx2 v[152:153], v189, s[48:51], s88 offen sc1\n"
    "buffer_store_dwordx4 v[152:155], v188, s[48:51], s88 offen sc1\n"
    "v_mov_b32 v181, 156700\n"
    "v_mov_b32 v180, 1\n"
    "s_waitcnt vmcnt(8)\n"
    "s_cmp_ge_i32 s72, 0\n"
    "s_cbranch_scc1 A0%=\n"
    "s_waitcnt vmcnt(4)\n"
    "A0%=:\n"
    "ds_add_u32 v181, v180\n"
    "s_memrealtime s[26:27]\n"
    "s_setprio 2\n"
    "s_lshl_b32 s0, s69, 2\n"
    "s_add_u32 s0, s0, 156664\n"
    "v_mov_b32 v216, s0\n"
    "v_xor_b32 v217, 4, v216\n"
    "v_xor_b32 v218, 2048, v235\n"
    "s_cmp_eq_u32 s69, 6\n"
    "s_cselect_b32 s29, s25, 0\n"
    "s_cbranch_scc0 GL0%=\n"
    "s_add_u32 s2, s34, 65\n"
    "UE%=:\n"
    "s_memrealtime s[0:1]\n"
    "s_waitcnt lgkmcnt(0)\n"
    "s_sub_u32 s0, s0, s2\n"
    "s_cmp_lt_i32 s0, 0\n"
    "s_cbranch_scc1 UE%=\n"
    "s_branch GL1%=\n"
    "GL0%=:\n"
    "s_add_u32 s34, s34, s5\n"
    "s_add_u32 s34, s34, 130\n"
    "s_waitcnt lgkmcnt(0)\n"
    "s_sub_u32 s0, s34, s26\n"
    "s_max_i32 s0, s0, 0\n"
    "s_add_u32 s26, s26, s0\n"
    "s_add_u32 s2, s26, 25\n"
    "US%=:\n"
    "s_memrealtime s[0:1]\n"
    "s_waitcnt lgkmcnt(0)\n"
    "s_sub_u32 s0, s0, s2\n"
    "s_cmp_lt_i32 s0, 0\n"
    "s_cbranch_scc1 US%=\n"
    "v_mov_b32 v181, 156704\n"
    "ds_write_b32 v181, v181\n"
    "GL1%=:\n"
    "buffer_load_dwordx4 v[100:103], v204, s[48:51], s14 offen sc1\n"
    "buffer_load_dwordx4 v[104:107], v204, s[48:51], s14 offen offset:3840 sc1\n"
    "buffer_load_dwordx4 v[108:111], v204, s[48:51], s15 offen sc1\n"
    "buffer_load_dwordx4 v[112:115], v204, s[48:51], s15 offen offset:3840 sc1\n"
    "buffer_load_dwordx2 v[116:117], v205, s[48:51], s14 offen sc1\n"
    "buffer_load_dwordx2 v[118:119], v205, s[48:51], s16 offen sc1\n"
    "buffer_load_dwordx4 v[140:143], v204, s[48:51], s17 offen sc1\n"
    "buffer_load_dwordx4 v[144:147], v204, s[48:51], s17 offen offset:3840 sc1\n"
    "buffer_load_dwordx4 v[148:151], v204, s[48:51], s18 offen sc1\n"
    "buffer_load_dwordx4 v[152:155], v204, s[48:51], s18 offen offset:3840 sc1\n"
    "buffer_load_dwordx2 v[156:157], v205, s[48:51], s17 offen sc1\n"
    "buffer_load_dwordx2 v[158:159], v205, s[48:51], s19 offen sc1\n"
    "s_mov_b32 s3, 16777216\n"
    "v_mov_b32 v181, 156700\n"
    "HA%=:\n"
    "ds_read_b32 v180, v181\n"
    "s_waitcnt lgkmcnt(0)\n"
    "v_cmp_le_u32 vcc, 256, v180\n"
    "s_cbranch_vccnz HAD%=\n"
    "s_sleep 1\n"
    "s_sub_u32 s3, s3, 1\n"
    "s_cbranch_scc0 HA%=\n"
    "HAD%=:\n"
    "ds_read_b32 v176, v234\n"
    "ds_read_b64 v[80:81], v228\n"
    "ds_read_b64 v[82:83], v229\n"
    "s_waitcnt lgkmcnt(2)\n"
    "v_sub_f32 v178, 1.0, v176\n"
    "s_waitcnt lgkmcnt(1)\n"
    "v_lshlrev_b32 v208, 16, v80\n"
    "v_and_b32 v209, 0xffff0000, v80\n"
    "v_pk_mul_f32 v[208:209], v[208:209], v[178:179] op_sel_hi:[1,0]\n"
    "v_lshlrev_b32 v210, 16, v81\n"
    "v_and_b32 v211, 0xffff0000, v81\n"
    "v_pk_mul_f32 v[210:211], v[210:211], v[178:179] op_sel_hi:[1,0]\n"
    "s_waitcnt lgkmcnt(0)\n"
    "v_lshlrev_b32 v212, 16, v82\n"
    "v_and_b32 v213, 0xffff0000, v82\n"
    "v_pk_mul_f32 v[212:213], v[212:213], v[178:179] op_sel_hi:[1,0]\n"
    "v_lshlrev_b32 v214, 16, v83\n"
    "v_and_b32 v215, 0xffff0000, v83\n"
    "v_pk_mul_f32 v[214:215], v[214:215], v[178:179] op_sel_hi:[1,0]\n"
    "ds_read_b64_tr_b16 v[120:121], v224 offset:20480\n"
    "ds_read_b64_tr_b16 v[122:123], v224 offset:20992\n"
    "ds_read_b64_tr_b16 v[124:125], v225 offset:20480\n"
    "ds_read_b64_tr_b16 v[126:127], v225 offset:20992\n"
    "ds_read_b64_tr_b16 v[128:129], v226 offset:20480\n"
    "ds_read_b64_tr_b16 v[130:131], v226 offset:20992\n"
    "ds_read_b64_tr_b16 v[132:133], v227 offset:20480\n"
    "ds_read_b64_tr_b16 v[134:135], v227 offset:20992\n"
    "ds_read_b64_tr_b16 v[136:137], v224 offset:24576\n"
    "ds_read_b64_tr_b16 v[138:139], v224 offset:25088\n"
    "ds_read_b64_tr_b16 v[184:185], v225 offset:24576\n"
    "ds_read_b64_tr_b16 v[186:187], v225 offset:25088\n"
    "ds_read_b64_tr_b16 v[192:193], v226 offset:24576\n"
    "ds_read_b64_tr_b16 v[194:195], v226 offset:25088\n"
    "ds_read_b64_tr_b16 v[196:197], v227 offset:24576\n"
    "ds_read_b64_tr_b16 v[198:199], v227 offset:25088\n"
    "ds_read_b64_tr_b16 v[200:201], v224 offset:28672\n"
    "ds_read_b64_tr_b16 v[202:203], v224 offset:29184\n"
    "ds_read_b64_tr_b16 v[220:221], v225 offset:28672\n"
    "ds_read_b64_tr_b16 v[222:223], v225 offset:29184\n"
    "s_waitcnt vmcnt(6)\n"
    "v_max3_u32 v180, v103, v107, v111\n"
    "v_max3_u32 v180, v180, v115, v117\n"
    "v_max3_u32 v180, v180, v119, v119\n"
    "v_cmp_eq_u32 vcc, -1, v180\n"
    "s_cbranch_vccnz FIX2h%=\n"
    "OK2h%=:\n"
    "s_setprio 0\n"
    "v_mfma_f32_16x16x32_bf16 v[160:163], v[0:3], v[100:103], 0\n"
    "ds_read_b64_tr_b16 v[236:237], v226 offset:28672\n"
    "ds_read_b64_tr_b16 v[238:239], v226 offset:29184\n"
    "v_mfma_f32_16x16x32_bf16 v[164:167], v[4:7], v[100:103], 0\n"
    "ds_read_b64_tr_b16 v[240:241], v227 offset:28672\n"
    "ds_read_b64_tr_b16 v[242:243], v227 offset:29184\n"
    "v_mfma_f32_16x16x32_bf16 v[168:171], v[8:11], v[100:103], 0\n"
    "ds_read_b64_tr_b16 v[244:245], v224 offset:32768\n"
    "ds_read_b64_tr_b16 v[246:247], v224 offset:33280\n"
    "v_mfma_f32_16x16x32_bf16 v[172:175], v[12:15], v[100:103], 0\n"
    "ds_read_b64_tr_b16 v[248:249], v225 offset:32768\n"
    "ds_read_b64_tr_b16 v[250:251], v225 offset:33280\n"
    "v_mfma_f32_16x16x32_bf16 v[160:163], v[16:19], v[104:107], v[160:163]\n"
    "ds_read_b64_tr_b16 v[252:253], v226 offset:32768\n"
    "ds_read_b64_tr_b16 v[254:255], v226 offset:33280\n"
    "v_mfma_f32_16x16x32_bf16 v[164:167], v[20:23], v[104:107], v[164:167]\n"
    "ds_read_b64_tr_b16 v[80:81], v227 offset:32768\n"
    "ds_read_b64_tr_b16 v[82:83], v227 offset:33280\n"
    "v_mfma_f32_16x16x32_bf16 v[168:171], v[24:27], v[104:107], v[168:171]\n"
    "ds_read_b64_tr_b16 v[84:85], v224 offset:36864\n"
    "ds_read_b64_tr_b16 v[86:87], v224 offset:37376\n"
    "v_mfma_f32_16x16x32_bf16 v[172:175], v[28:31], v[104:107], v[172:175]\n"
    "ds_read_b64_tr_b16 v[88:89], v225 offset:36864\n"
    "ds_read_b64_tr_b16 v[90:91], v225 offset:37376\n"
    "v_mfma_f32_16x16x32_bf16 v[160:163], v[32:35], v[108:111], v[160:163]\n"
    "ds_read_b64_tr_b16 v[92:93], v226 offset:36864\n"
    "ds_read_b64_tr_b16 v[94:95], v226 offset:37376\n"
    "v_mfma_f32_16x16x32_bf16 v[164:167], v[36:39], v[108:111], v[164:167]\n"
    "ds_read_b64_tr_b16 v[96:97], v227 offset:36864\n"
    "ds_read_b64_tr_b16 v[98:99], v227 offset:37376\n"
    "v_mfma_f32_16x16x32_bf16 v[168:171], v[40:43], v[108:111], v[168:171]\n"
    "v_mfma_f32_16x16x32_bf16 v[172:175], v[44:47], v[108:111], v[172:175]\n"
    "v_mfma_f32_16x16x32_bf16 v[160:163], v[48:51], v[112:115], v[160:163]\n"
    "v_mfma_f32_16x16x32_bf16 v[164:167], v[52:55], v[112:115], v[164:167]\n"
    "v_mfma_f32_16x16x32_bf16 v[168:171], v[56:59], v[112:115], v[168:171]\n"
    "v_mfma_f32_16x16x32_bf16 v[172:175], v[60:63], v[112:115], v[172:175]\n"
    "v_mfma_f32_16x16x32_bf16 v[160:163], v[64:67], v[116:119], v[160:163]\n"
    "v_mfma_f32_16x16x32_bf16 v[164:167], v[68:71], v[116:119], v[164:167]\n"
    "v_mfma_f32_16x16x32_bf16 v[168:171], v[72:75], v[116:119], v[168:171]\n"
    "v_mfma_f32_16x16x32_bf16 v[172:175], v[76:79], v[116:119], v[172:175]\n"
    "s_setprio 2\n"
    "s_waitcnt lgkmcnt(0)\n"
    "s_cmp_lg_u32 s29, 0\n"
    "s_cbranch_scc1 YJOB%=\n"
    "s_waitcnt vmcnt(3)\n"
    "v_max3_u32 v180, v143, v147, v151\n"
    "v_cmp_eq_u32 vcc, -1, v180\n"
    "s_cbranch_vccnz FIX3ha%=\n"
    "OK3ha%=:\n"
    "s_setprio 0\n"
    "v_mfma_f32_16x16x32_bf16 v[160:163], v[120:123], v[140:143], v[160:163]\n"
    "v_mfma_f32_16x16x32_bf16 v[164:167], v[124:127], v[140:143], v[164:167]\n"
    "v_mfma_f32_16x16x32_bf16 v[168:171], v[128:131], v[140:143], v[168:171]\n"
    "v_mfma_f32_16x16x32_bf16 v[172:175], v[132:135], v[140:143], v[172:175]\n"
    "v_mfma_f32_16x16x32_bf16 v[160:163], v[136:139], v[144:147], v[160:163]\n"
    "v_mfma_f32_16x16x32_bf16 v[164:167], v[184:187], v[144:147], v[164:167]\n"
    "v_mfma_f32_16x16x32_bf16 v[168:171], v[192:195], v[144:147], v[168:171]\n"
    "v_mfma_f32_16x16x32_bf16 v[172:175], v[196:199], v[144:147], v[172:175]\n"
    "v_mfma_f32_16x16x32_bf16 v[160:163], v[200:203], v[148:151], v[160:163]\n"
    "s_waitcnt vmcnt(0)\n"
    "v_mfma_f32_16x16x32_bf16 v[164:167], v[220:223], v[148:151], v[164:167]\n"
    "v_max3_u32 v180, v155, v157, v159\n"
    "v_mfma_f32_16x16x32_bf16 v[168:171], v[236:239], v[148:151], v[168:171]\n"
    "v_cmp_eq_u32 vcc, -1, v180\n"
    "v_mfma_f32_16x16x32_bf16 v[172:175], v[240:243], v[148:151], v[172:175]\n"
    "s_cbranch_vccnz FIX3hb%=\n"
    "OK3hb%=:\n"
    "v_mfma_f32_16x16x32_bf16 v[160:163], v[244:247], v[152:155], v[160:163]\n"
    "v_mfma_f32_16x16x32_bf16 v[164:167], v[248:251], v[152:155], v[164:167]\n"
    "v_mfma_f32_16x16x32_bf16 v[168:171], v[252:255], v[152:155], v[168:171]\n"
    "v_mfma_f32_16x16x32_bf16 v[172:175], v[80:83], v[152:155], v[172:175]\n"
    "v_mfma_f32_16x16x32_bf16 v[160:163], v[84:87], v[156:159], v[160:163]\n"
    "v_mfma_f32_16x16x32_bf16 v[164:167], v[88:91], v[156:159], v[164:167]\n"
    "v_mfma_f32_16x16x32_bf16 v[168:171], v[92:95], v[156:159], v[168:171]\n"
    "v_mfma_f32_16x16x32_bf16 v[172:175], v[96:99], v[156:159], v[172:175]\n"
    "s_setprio 2\n"
    "YB1%=:\n"
    "s_nop 5\n"
    "ds_write_b128 v235, v[168:171]\n"
    "ds_write_b128 v235, v[172:175] offset:1024\n"
    "ds_write_b32 v216, v216\n"
    "s_cmp_lg_u32 s29, 0\n"
    "s_cbranch_scc1 YS%=\n"
    "YB2%=:\n"
    "s_bitcmp1_b32 s25, s83\n"
    "s_cbranch_scc1 YOWN%=\n"
    "YBH%=:\n"
    "s_mov_b32 s3, 16777216\n"
    "EXW%=:\n"
    "ds_read_b32 v4, v217\n"
    "ds_read_b128 v[8:11], v218\n"
    "ds_read_b128 v[12:15], v218 offset:1024\n"
    "s_waitcnt lgkmcnt(0)\n"
    "v_cmp_ne_u32 vcc, 0, v4\n"
    "s_cbranch_vccnz EXD%=\n"
    "s_sub_u32 s3, s3, 1\n"
    "s_cbranch_scc0 EXW%=\n"
    "EXD%=:\n"
    "v_pk_add_f32 v[160:161], v[160:161], v[8:9]\n"
    "v_pk_add_f32 v[162:163], v[162:163], v[10:11]\n"
    "v_pk_add_f32 v[164:165], v[164:165], v[12:13]\n"
    "v_pk_add_f32 v[166:167], v[166:167], v[14:15]\n"
    "v_pk_fma_f32 v[160:161], v[160:161], v[176:177], v[208:209] op_sel_hi:[1,0,1]\n"
    "v_pk_fma_f32 v[162:163], v[162:163], v[176:177], v[210:211] op_sel_hi:[1,0,1]\n"
    "v_cvt_pk_bf16_f32 v160, v160, v161\n"
    "v_cvt_pk_bf16_f32 v161, v162, v163\n"
    "ds_write_b64 v228, v[160:161]\n"
    "v_pk_fma_f32 v[164:165], v[164:165], v[176:177], v[212:213] op_sel_hi:[1,0,1]\n"
    "v_pk_fma_f32 v[166:167], v[166:167], v[176:177], v[214:215] op_sel_hi:[1,0,1]\n"
    "v_cvt_pk_bf16_f32 v164, v164, v165\n"
    "v_cvt_pk_bf16_f32 v165, v166, v167\n"
    "ds_write_b64 v229, v[164:165]\n"
    "ds_read_b128 v[4:7], v232\n"
    "s_waitcnt lgkmcnt(0)\n"
    "buffer_store_dwordx4 v[4:7], v233, s[56:59], 0 offen nt\n"
    "s_endpgm\n"
    "FULL%=:\n"
    "s_add_u32 s8, s76, 0\n"
    "s_add_u32 s9, s76, 7680\n"
    "s_add_u32 s10, s76, 61440\n"
    "s_add_u32 s11, s76, 15360\n"
    "s_add_u32 s12, s76, 23040\n"
    "s_add_u32 s13, s76, 76800\n"
    "s_add_u32 s14, s76, 30720\n"
    "s_add_u32 s15, s76, 38400\n"
    "s_add_u32 s16, s76, 92160\n"
    "s_add_u32 s17, s76, 46080\n"
    "s_add_u32 s18, s76, 53760\n"
    "s_add_u32 s19, s76, 107520\n"
    "s_mov_b32 s3, 16777216\n"
    "v_mov_b32 v144, 156736\n"
    "RXF%=:\n"
    "ds_read_b128 v[140:143], v144\n"
    "s_waitcnt lgkmcnt(0)\n"
    "v_cmp_ne_u32 vcc, 0, v140\n"
    "s_cbranch_vccnz RXFD%=\n"
    "s_sleep 1\n"
    "s_sub_u32 s3, s3, 1\n"
    "s_cbranch_scc0 RXF%=\n"
    "RXFD%=:\n"
    "v_readfirstlane_b32 s34, v140\n"
    "v_readfirstlane_b32 s5, v141\n"
    "v_readfirstlane_b32 s25, v142\n"
    "s_add_u32 s2, s34, 65\n"
    "U0%=:\n"
    "s_memrealtime s[0:1]\n"
    "s_waitcnt lgkmcnt(0)\n"
    "s_sub_u32 s0, s0, s2\n"
    "s_cmp_lt_i32 s0, 0\n"
    "s_cbranch_scc1 U0%=\n"
    "buffer_load_dwordx4 v[80:83], v204, s[48:51], s8 offen sc1\n"
    "buffer_load_dwordx4 v[84:87], v204, s[48:51], s8 offen offset:3840 sc1\n"
    "buffer_load_dwordx4 v[88:91], v204, s[48:51], s9 offen sc1\n"
    "buffer_load_dwordx4 v[92:95], v204, s[48:51], s9 offen offset:3840 sc1\n"
    "buffer_load_dwordx2 v[96:97], v205, s[48:51], s8 offen sc1\n"
    "buffer_load_dwordx2 v[98:99], v205, s[48:51], s10 offen sc1\n"
    "buffer_load_dwordx4 v[120:123], v204, s[48:51], s11 offen sc1\n"
    "buffer_load_dwordx4 v[124:127], v204, s[48:51], s11 offen offset:3840 sc1\n"
    "buffer_load_dwordx4 v[128:131], v204, s[48:51], s12 offen sc1\n"
    "buffer_load_dwordx4 v[132:135], v204, s[48:51], s12 offen offset:3840 sc1\n"
    "buffer_load_dwordx2 v[136:137], v205, s[48:51], s11 offen sc1\n"
    "buffer_load_dwordx2 v[138:139], v205, s[48:51], s13 offen sc1\n"
    "s_waitcnt vmcnt(6)\n"
    "v_max3_u32 v180, v83, v87, v91\n"
    "v_max3_u32 v180, v180, v95, v97\n"
    "v_max3_u32 v180, v180, v99, v99\n"
    "v_cmp_eq_u32 vcc, -1, v180\n"
    "s_cbranch_vccnz FIX0f%=\n"
    "OK0f%=:\n"
    "buffer_load_dwordx4 v[100:103], v204, s[48:51], s14 offen sc1\n"
    "buffer_load_dwordx4 v[104:107], v204, s[48:51], s14 offen offset:3840 sc1\n"
    "buffer_load_dwordx4 v[108:111], v204, s[48:51], s15 offen sc1\n"
    "buffer_load_dwordx4 v[112:115], v204, s[48:51], s15 offen offset:3840 sc1\n"
    "buffer_load_dwordx2 v[116:117], v205, s[48:51], s14 offen sc1\n"
    "buffer_load_dwordx2 v[118:119], v205, s[48:51], s16 offen sc1\n"
    "s_setprio 0\n"
    "v_mfma_f32_16x16x32_bf16 v[160:163], v[0:3], v[80:83], 0\n"
    "ds_read_b64_tr_b16 v[0:1], v224 offset:20480\n"
    "ds_read_b64_tr_b16 v[2:3], v224 offset:20992\n"
    "v_mfma_f32_16x16x32_bf16 v[164:167], v[4:7], v[80:83], 0\n"
    "ds_read_b64_tr_b16 v[4:5], v225 offset:20480\n"
    "ds_read_b64_tr_b16 v[6:7], v225 offset:20992\n"
    "v_mfma_f32_16x16x32_bf16 v[168:171], v[8:11], v[80:83], 0\n"
    "ds_read_b64_tr_b16 v[8:9], v226 offset:20480\n"
    "ds_read_b64_tr_b16 v[10:11], v226 offset:20992\n"
    "v_mfma_f32_16x16x32_bf16 v[172:175], v[12:15], v[80:83], 0\n"
    "ds_read_b64_tr_b16 v[12:13], v227 offset:20480\n"
    "ds_read_b64_tr_b16 v[14:15], v227 offset:20992\n"
    "v_mfma_f32_16x16x32_bf16 v[160:163], v[16:19], v[84:87], v[160:163]\n"
    "ds_read_b64_tr_b16 v[16:17], v224 offset:24576\n"
    "ds_read_b64_tr_b16 v[18:19], v224 offset:25088\n"
    "v_mfma_f32_16x16x32_bf16 v[164:167], v[20:23], v[84:87], v[164:167]\n"
    "ds_read_b64_tr_b16 v[20:21], v225 offset:24576\n"
    "ds_read_b64_tr_b16 v[22:23], v225 offset:25088\n"
    "v_mfma_f32_16x16x32_bf16 v[168:171], v[24:27], v[84:87], v[168:171]\n"
    "ds_read_b64_tr_b16 v[24:25], v226 offset:24576\n"
    "ds_read_b64_tr_b16 v[26:27], v226 offset:25088\n"
    "v_mfma_f32_16x16x32_bf16 v[172:175], v[28:31], v[84:87], v[172:175]\n"
    "ds_read_b64_tr_b16 v[28:29], v227 offset:24576\n"
    "ds_read_b64_tr_b16 v[30:31], v227 offset:25088\n"
    "v_mfma_f32_16x16x32_bf16 v[160:163], v[32:35], v[88:91], v[160:163]\n"
    "ds_read_b64_tr_b16 v[32:33], v224 offset:28672\n"
    "ds_read_b64_tr_b16 v[34:35], v224 offset:29184\n"
    "v_mfma_f32_16x16x32_bf16 v[164:167], v[36:39], v[88:91], v[164:167]\n"
    "ds_read_b64_tr_b16 v[36:37], v225 offset:28672\n"
    "ds_read_b64_tr_b16 v[38:39], v225 offset:29184\n"
    "v_mfma_f32_16x16x32_bf16 v[168:171], v[40:43], v[88:91], v[168:171]\n"
    "ds_read_b64_tr_b16 v[40:41], v226 offset:28672\n"
    "ds_read_b64_tr_b16 v[42:43], v226 offset:29184\n"
    "v_mfma_f32_16x16x32_bf16 v[172:175], v[44:47], v[88:91], v[172:175]\n"
    "ds_read_b64_tr_b16 v[44:45], v227 offset:28672\n"
    "ds_read_b64_tr_b16 v[46:47], v227 offset:29184\n"
    "v_mfma_f32_16x16x32_bf16 v[160:163], v[48:51], v[92:95], v[160:163]\n"
    "ds_read_b64_tr_b16 v[48:49], v224 offset:32768\n"
    "ds_read_b64_tr_b16 v[50:51], v224 offset:33280\n"
    "v_mfma_f32_16x16x32_bf16 v[164:167], v[52:55], v[92:95], v[164:167]\n"
    "ds_read_b64_tr_b16 v[52:53], v225 offset:32768\n"
    "ds_read_b64_tr_b16 v[54:55], v225 offset:33280\n"
    "v_mfma_f32_16x16x32_bf16 v[168:171], v[56:59], v[92:95], v[168:171]\n"
    "ds_read_b64_tr_b16 v[56:57], v226 offset:32768\n"
    "ds_read_b64_tr_b16 v[58:59], v226 offset:33280\n"
    "v_mfma_f32_16x16x32_bf16 v[172:175], v[60:63], v[92:95], v[172:175]\n"
    "ds_read_b64_tr_b16 v[60:61], v227 offset:32768\n"
    "ds_read_b64_tr_b16 v[62:63], v227 offset:33280\n"
    "v_mfma_f32_16x16x32_bf16 v[160:163], v[64:67], v[96:99], v[160:163]\n"
    "ds_read_b64_tr_b16 v[64:65], v224 offset:36864\n"
    "ds_read_b64_tr_b16 v[66:67], v224 offset:37376\n"
    "v_mfma_f32_16x16x32_bf16 v[164:167], v[68:71], v[96:99], v[164:167]\n"
    "ds_read_b64_tr_b16 v[68:69], v225 offset:36864\n"
    "ds_read_b64_tr_b16 v[70:71], v225 offset:37376\n"
    "v_mfma_f32_16x16x32_bf16 v[168:171], v[72:75], v[96:99], v[168:171]\n"
    "ds_read_b64_tr_b16 v[72:73], v226 offset:36864\n"
    "ds_read_b64_tr_b16 v[74:75], v226 offset:37376\n"
    "v_mfma_f32_16x16x32_bf16 v[172:175], v[76:79], v[96:99], v[172:175]\n"
    "ds_read_b64_tr_b16 v[76:77], v227 offset:36864\n"
    "ds_read_b64_tr_b16 v[78:79], v227 offset:37376\n"
    "s_setprio 2\n"
    "s_waitcnt vmcnt(6)\n"
    "v_max3_u32 v180, v123, v127, v131\n"
    "v_max3_u32 v180, v180, v135, v137\n"
    "v_max3_u32 v180, v180, v139, v139\n"
    "v_cmp_eq_u32 vcc, -1, v180\n"
    "s_cbranch_vccnz FIX1f%=\n"
    "OK1f%=:\n"
    "s_waitcnt lgkmcnt(14)\n"
    "s_setprio 0\n"
    "v_mfma_f32_16x16x32_bf16 v[160:163], v[0:3], v[120:123], v[160:163]\n"
    "ds_read_b64_tr_b16 v[0:1], v224 offset:40960\n"
    "ds_read_b64_tr_b16 v[2:3], v224 offset:41472\n"
    "v_mfma_f32_16x16x32_bf16 v[164:167], v[4:7], v[120:123], v[164:167]\n"
    "ds_read_b64_tr_b16 v[4:5], v225 offset:40960\n"
    "ds_read_b64_tr_b16 v[6:7], v225 offset:41472\n"
    "v_mfma_f32_16x16x32_bf16 v[168:171], v[8:11], v[120:123], v[168:171]\n"
    "ds_read_b64_tr_b16 v[8:9], v226 offset:40960\n"
    "ds_read_b64_tr_b16 v[10:11], v226 offset:41472\n"
    "v_mfma_f32_16x16x32_bf16 v[172:175], v[12:15], v[120:123], v[172:175]\n"
    "ds_read_b64_tr_b16 v[12:13], v227 offset:40960\n"
    "ds_read_b64_tr_b16 v[14:15], v227 offset:41472\n"
    "v_mfma_f32_16x16x32_bf16 v[160:163], v[16:19], v[124:127], v[160:163]\n"
    "ds_read_b64_tr_b16 v[16:17], v224 offset:45056\n"
    "ds_read_b64_tr_b16 v[18:19], v224 offset:45568\n"
    "v_mfma_f32_16x16x32_bf16 v[164:167], v[20:23], v[124:127], v[164:167]\n"
    "ds_read_b64_tr_b16 v[20:21], v225 offset:45056\n"
    "ds_read_b64_tr_b16 v[22:23], v225 offset:45568\n"
    "v_mfma_f32_16x16x32_bf16 v[168:171], v[24:27], v[124:127], v[168:171]\n"
    "ds_read_b64_tr_b16 v[24:25], v226 offset:45056\n"
    "ds_read_b64_tr_b16 v[26:27], v226 offset:45568\n"
    "v_mfma_f32_16x16x32_bf16 v[172:175], v[28:31], v[124:127], v[172:175]\n"
    "ds_read_b64_tr_b16 v[28:29], v227 offset:45056\n"
    "ds_read_b64_tr_b16 v[30:31], v227 offset:45568\n"
    "v_mfma_f32_16x16x32_bf16 v[160:163], v[32:35], v[128:131], v[160:163]\n"
    "ds_read_b64_tr_b16 v[32:33], v224 offset:49152\n"
    "ds_read_b64_tr_b16 v[34:35], v224 offset:49664\n"
    "v_mfma_f32_16x16x32_bf16 v[164:167], v[36:39], v[128:131], v[164:167]\n"
    "ds_read_b64_tr_b16 v[36:37], v225 offset:49152\n"
    "ds_read_b64_tr_b16 v[38:39], v225 offset:49664\n"
    "v_mfma_f32_16x16x32_bf16 v[168:171], v[40:43], v[128:131], v[168:171]\n"
    "ds_read_b64_tr_b16 v[40:41], v226 offset:49152\n"
    "ds_read_b64_tr_b16 v[42:43], v226 offset:49664\n"
    "v_mfma_f32_16x16x32_bf16 v[172:175], v[44:47], v[128:131], v[172:175]\n"
    "ds_read_b64_tr_b16 v[44:45], v227 offset:49152\n"
    "ds_read_b64_tr_b16 v[46:47], v227 offset:49664\n"
    "v_mfma_f32_16x16x32_bf16 v[160:163], v[48:51], v[132:135], v[160:163]\n"
    "ds_read_b64_tr_b16 v[48:49], v224 offset:53248\n"
    "ds_read_b64_tr_b16 v[50:51], v224 offset:53760\n"
    "s_waitcnt lgkmcnt(14)\n"
    "v_mfma_f32_16x16x32_bf16 v[164:167], v[52:55], v[132:135], v[164:167]\n"
    "ds_read_b64_tr_b16 v[52:53], v225 offset:53248\n"
    "ds_read_b64_tr_b16 v[54:55], v225 offset:53760\n"
    "v_mfma_f32_16x16x32_bf16 v[168:171], v[56:59], v[132:135], v[168:171]\n"
    "ds_read_b64_tr_b16 v[56:57], v226 offset:53248\n"
    "ds_read_b64_tr_b16 v[58:59], v226 offset:53760\n"
    "v_mfma_f32_16x16x32_bf16 v[172:175], v[60:63], v[132:135], v[172:175]\n"
    "ds_read_b64_tr_b16 v[60:61], v227 offset:53248\n"
    "ds_read_b64_tr_b16 v[62:63], v227 offset:53760\n"
    "v_mfma_f32_16x16x32_bf16 v[160:163], v[64:67], v[136:139], v[160:163]\n"
    "ds_read_b64_tr_b16 v[64:65], v224 offset:57344\n"
    "ds_read_b64_tr_b16 v[66:67], v224 offset:57856\n"
    "v_mfma_f32_16x16x32_bf16 v[164:167], v[68:71], v[136:139], v[164:167]\n"
    "ds_read_b64_tr_b16 v[68:69], v225 offset:57344\n"
    "ds_read_b64_tr_b16 v[70:71], v225 offset:57856\n"
    "v_mfma_f32_16x16x32_bf16 v[168:171], v[72:75], v[136:139], v[168:171]\n"
    "ds_read_b64_tr_b16 v[72:73], v226 offset:57344\n"
    "ds_read_b64_tr_b16 v[74:75], v226 offset:57856\n"
    "v_mfma_f32_16x16x32_bf16 v[172:175], v[76:79], v[136:139], v[172:175]\n"
    "ds_read_b64_tr_b16 v[76:77], v227 offset:57344\n"
    "ds_read_b64_tr_b16 v[78:79], v227 offset:57856\n"
    "s_setprio 2\n"
    "v_mov_b32 v80, 156704\n"
    "ds_read_b32 v81, v80\n"
    "v_add_u32 v224, 40960, v224\n"
    "v_add_u32 v225, 40960, v225\n"
    "v_add_u32 v226, 40960, v226\n"
    "v_add_u32 v227, 40960, v227\n"
    "s_mov_b32 s3, 16777216\n"
    "v_mov_b32 v181, 156700\n"
    "FA%=:\n"
    "ds_read_b32 v180, v181\n"
    "s_waitcnt lgkmcnt(0)\n"
    "v_cmp_le_u32 vcc, 256, v180\n"
    "s_cbranch_vccnz FAD%=\n"
    "s_sleep 1\n"
    "s_sub_u32 s3, s3, 1\n"
    "s_cbranch_scc0 FA%=\n"
    "FAD%=:\n"
    "v_readfirstlane_b32 s6, v81\n"
    "s_nop 0\n"
    "s_cmp_eq_u32 s6, 0\n"
    "s_cbranch_scc1 G3N%=\n"
    "buffer_load_dwordx4 v[140:143], v204, s[48:51], s17 offen sc1\n"
    "buffer_load_dwordx4 v[144:147], v204, s[48:51], s17 offen offset:3840 sc1\n"
    "buffer_load_dwordx4 v[148:151], v204, s[48:51], s18 offen sc1\n"
    "buffer_load_dwordx4 v[152:155], v204, s[48:51], s18 offen offset:3840 sc1\n"
    "buffer_load_dwordx2 v[156:157], v205, s[48:51], s17 offen sc1\n"
    "buffer_load_dwordx2 v[158:159], v205, s[48:51], s19 offen sc1\n"
    "s_waitcnt vmcnt(6)\n"
    "s_branch G3M%=\n"
    "G3N%=:\n"
    "s_waitcnt vmcnt(0)\n"
    "G3M%=:\n"
    "v_max3_u32 v180, v103, v107, v111\n"
    "v_max3_u32 v180, v180, v115, v117\n"
    "v_max3_u32 v180, v180, v119, v119\n"
    "v_cmp_eq_u32 vcc, -1, v180\n"
    "s_cbranch_vccnz FIX2f%=\n"
    "OK2f%=:\n"
    "s_setprio 0\n"
    "v_mfma_f32_16x16x32_bf16 v[160:163], v[0:3], v[100:103], v[160:163]\n"
    "ds_read_b64_tr_b16 v[0:1], v224 offset:20480\n"
    "ds_read_b64_tr_b16 v[2:3], v224 offset:20992\n"
    "v_mfma_f32_16x16x32_bf16 v[164:167], v[4:7], v[100:103], v[164:167]\n"
    "ds_read_b64_tr_b16 v[4:5], v225 offset:20480\n"
    "ds_read_b64_tr_b16 v[6:7], v225 offset:20992\n"
    "v_mfma_f32_16x16x32_bf16 v[168:171], v[8:11], v[100:103], v[168:171]\n"
    "ds_read_b64_tr_b16 v[8:9], v226 offset:20480\n"
    "ds_read_b64_tr_b16 v[10:11], v226 offset:20992\n"
    "v_mfma_f32_16x16x32_bf16 v[172:175], v[12:15], v[100:103], v[172:175]\n"
    "ds_read_b64_tr_b16 v[12:13], v227 offset:20480\n"
    "ds_read_b64_tr_b16 v[14:15], v227 offset:20992\n"
    "v_mfma_f32_16x16x32_bf16 v[160:163], v[16:19], v[104:107], v[160:163]\n"
    "ds_read_b64_tr_b16 v[16:17], v224 offset:24576\n"
    "ds_read_b64_tr_b16 v[18:19], v224 offset:25088\n"
    "v_mfma_f32_16x16x32_bf16 v[164:167], v[20:23], v[104:107], v[164:167]\n"
    "ds_read_b64_tr_b16 v[20:21], v225 offset:24576\n"
    "ds_read_b64_tr_b16 v[22:23], v225 offset:25088\n"
    "v_mfma_f32_16x16x32_bf16 v[168:171], v[24:27], v[104:107], v[168:171]\n"
    "ds_read_b64_tr_b16 v[24:25], v226 offset:24576\n"
    "ds_read_b64_tr_b16 v[26:27], v226 offset:25088\n"
    "v_mfma_f32_16x16x32_bf16 v[172:175], v[28:31], v[104:107], v[172:175]\n"
    "ds_read_b64_tr_b16 v[28:29], v227 offset:24576\n"
    "ds_read_b64_tr_b16 v[30:31], v227 offset:25088\n"
    "v_mfma_f32_16x16x32_bf16 v[160:163], v[32:35], v[108:111], v[160:163]\n"
    "ds_read_b64_tr_b16 v[32:33], v224 offset:28672\n"
    "ds_read_b64_tr_b16 v[34:35], v224 offset:29184\n"
    "v_mfma_f32_16x16x32_bf16 v[164:167], v[36:39], v[108:111], v[164:167]\n"
    "ds_read_b64_tr_b16 v[36:37], v225 offset:28672\n"
    "ds_read_b64_tr_b16 v[38:39], v225 offset:29184\n"
    "v_mfma_f32_16x16x32_bf16 v[168:171], v[40:43], v[108:111], v[168:171]\n"
    "ds_read_b64_tr_b16 v[40:41], v226 offset:28672\n"
    "ds_read_b64_tr_b16 v[42:43], v226 offset:29184\n"
    "v_mfma_f32_16x16x32_bf16 v[172:175], v[44:47], v[108:111], v[172:175]\n"
    "ds_read_b64_tr_b16 v[44:45], v227 offset:28672\n"
    "ds_read_b64_tr_b16 v[46:47], v227 offset:29184\n"
    "v_mfma_f32_16x16x32_bf16 v[160:163], v[48:51], v[112:115], v[160:163]\n"
    "ds_read_b64_tr_b16 v[48:49], v224 offset:32768\n"
    "ds_read_b64_tr_b16 v[50:51], v224 offset:33280\n"
    "v_mfma_f32_16x16x32_bf16 v[164:167], v[52:55], v[112:115], v[164:167]\n"
    "ds_read_b64_tr_b16 v[52:53], v225 offset:32768\n"
    "ds_read_b64_tr_b16 v[54:55], v225 offset:33280\n"
    "v_mfma_f32_16x16x32_bf16 v[168:171], v[56:59], v[112:115], v[168:171]\n"
    "ds_read_b64_tr_b16 v[56:57], v226 offset:32768\n"
    "ds_read_b64_tr_b16 v[58:59], v226 offset:33280\n"
    "v_mfma_f32_16x16x32_bf16 v[172:175], v[60:63], v[112:115], v[172:175]\n"
    "ds_read_b64_tr_b16 v[60:61], v227 offset:32768\n"
    "ds_read_b64_tr_b16 v[62:63], v227 offset:33280\n"
    "v_mfma_f32_16x16x32_bf16 v[160:163], v[64:67], v[116:119], v[160:163]\n"
    "ds_read_b64_tr_b16 v[64:65], v224 offset:36864\n"
    "ds_read_b64_tr_b16 v[66:67], v224 offset:37376\n"
    "v_mfma_f32_16x16x32_bf16 v[164:167], v[68:71], v[116:119], v[164:167]\n"
    "ds_read_b64_tr_b16 v[68:69], v225 offset:36864\n"
    "ds_read_b64_tr_b16 v[70:71], v225 offset:37376\n"
    "v_mfma_f32_16x16x32_bf16 v[168:171], v[72:75], v[116:119], v[168:171]\n"
    "ds_read_b64_tr_b16 v[72:73], v226 offset:36864\n"
    "ds_read_b64_tr_b16 v[74:75], v226 offset:37376\n"
    "v_mfma_f32_16x16x32_bf16 v[172:175], v[76:79], v[116:119], v[172:175]\n"
    "ds_read_b64_tr_b16 v[76:77], v227 offset:36864\n"
    "ds_read_b64_tr_b16 v[78:79], v227 offset:37376\n"
    "s_setprio 2\n"
    "s_cmp_lg_u32 s6, 0\n"
    "s_cbranch_scc1 G3D%=\n"
    "s_mov_b32 s3, 16777216\n"
    "v_mov_b32 v181, 156704\n"
    "GOL%=:\n"
    "ds_read_b32 v180, v181\n"
    "s_waitcnt lgkmcnt(0)\n"
    "v_cmp_ne_u32 vcc, 0, v180\n"
    "s_cbranch_vccnz GOLD%=\n"
    "s_sleep 1\n"
    "s_sub_u32 s3, s3, 1\n"
    "s_cbranch_scc0 GOL%=\n"
    "GOLD%=:\n"
    "buffer_load_dwordx4 v[140:143], v204, s[48:51], s17 offen sc1\n"
    "buffer_load_dwordx4 v[144:147], v204, s[48:51], s17 offen offset:3840 sc1\n"
    "buffer_load_dwordx4 v[148:151], v204, s[48:51], s18 offen sc1\n"
    "buffer_load_dwordx4 v[152:155], v204, s[48:51], s18 offen offset:3840 sc1\n"
    "buffer_load_dwordx2 v[156:157], v205, s[48:51], s17 offen sc1\n"
    "buffer_load_dwordx2 v[158:159], v205, s[48:51], s19 offen sc1\n"
    "G3D%=:\n"
    "ds_read_b32 v176, v234\n"
    "ds_read_b64 v[104:105], v228\n"
    "ds_read_b64 v[106:107], v229\n"
    "ds_read_b64 v[108:109], v230\n"
    "ds_read_b64 v[110:111], v231\n"
    "s_waitcnt lgkmcnt(4)\n"
    "v_sub_f32 v178, 1.0, v176\n"
    "s_waitcnt lgkmcnt(3)\n"
    "v_lshlrev_b32 v208, 16, v104\n"
    "v_and_b32 v209, 0xffff0000, v104\n"
    "v_pk_mul_f32 v[208:209], v[208:209], v[178:179] op_sel_hi:[1,0]\n"
    "v_lshlrev_b32 v210, 16, v105\n"
    "v_and_b32 v211, 0xffff0000, v105\n"
    "v_pk_mul_f32 v[210:211], v[210:211], v[178:179] op_sel_hi:[1,0]\n"
    "s_waitcnt lgkmcnt(2)\n"
    "v_lshlrev_b32 v212, 16, v106\n"
    "v_and_b32 v213, 0xffff0000, v106\n"
    "v_pk_mul_f32 v[212:213], v[212:213], v[178:179] op_sel_hi:[1,0]\n"
    "v_lshlrev_b32 v214, 16, v107\n"
    "v_and_b32 v215, 0xffff0000, v107\n"
    "v_pk_mul_f32 v[214:215], v[214:215], v[178:179] op_sel_hi:[1,0]\n"
    "s_waitcnt lgkmcnt(1)\n"
    "v_lshlrev_b32 v216, 16, v108\n"
    "v_and_b32 v217, 0xffff0000, v108\n"
    "v_pk_mul_f32 v[216:217], v[216:217], v[178:179] op_sel_hi:[1,0]\n"
    "v_lshlrev_b32 v218, 16, v109\n"
    "v_and_b32 v219, 0xffff0000, v109\n"
    "v_pk_mul_f32 v[218:219], v[218:219], v[178:179] op_sel_hi:[1,0]\n"
    "s_waitcnt lgkmcnt(0)\n"
    "v_lshlrev_b32 v220, 16, v110\n"
    "v_and_b32 v221, 0xffff0000, v110\n"
    "v_pk_mul_f32 v[220:221], v[220:221], v[178:179] op_sel_hi:[1,0]\n"
    "v_lshlrev_b32 v222, 16, v111\n"
    "v_and_b32 v223, 0xffff0000, v111\n"
    "v_pk_mul_f32 v[222:223], v[222:223], v[178:179] op_sel_hi:[1,0]\n"
    "s_waitcnt vmcnt(3)\n"
    "v_max3_u32 v180, v143, v147, v151\n"
    "v_cmp_eq_u32 vcc, -1, v180\n"
    "s_cbranch_vccnz FIX3fa%=\n"
    "OK3fa%=:\n"
    "s_setprio 0\n"
    "v_mfma_f32_16x16x32_bf16 v[160:163], v[0:3], v[140:143], v[160:163]\n"
    "v_mfma_f32_16x16x32_bf16 v[164:167], v[4:7], v[140:143], v[164:167]\n"
    "v_mfma_f32_16x16x32_bf16 v[168:171], v[8:11], v[140:143], v[168:171]\n"
    "v_mfma_f32_16x16x32_bf16 v[172:175], v[12:15], v[140:143], v[172:175]\n"
    "v_mfma_f32_16x16x32_bf16 v[160:163], v[16:19], v[144:147], v[160:163]\n"
    "v_mfma_f32_16x16x32_bf16 v[164:167], v[20:23], v[144:147], v[164:167]\n"
    "v_mfma_f32_16x16x32_bf16 v[168:171], v[24:27], v[144:147], v[168:171]\n"
    "v_mfma_f32_16x16x32_bf16 v[172:175], v[28:31], v[144:147], v[172:175]\n"
    "v_mfma_f32_16x16x32_bf16 v[160:163], v[32:35], v[148:151], v[160:163]\n"
    "s_waitcnt vmcnt(0)\n"
    "v_mfma_f32_16x16x32_bf16 v[164:167], v[36:39], v[148:151], v[164:167]\n"
    "v_max3_u32 v180, v155, v157, v159\n"
    "v_mfma_f32_16x16x32_bf16 v[168:171], v[40:43], v[148:151], v[168:171]\n"
    "v_cmp_eq_u32 vcc, -1, v180\n"
    "v_mfma_f32_16x16x32_bf16 v[172:175], v[44:47], v[148:151], v[172:175]\n"
    "s_cbranch_vccnz FIX3fb%=\n"
    "OK3fb%=:\n"
    "v_mfma_f32_16x16x32_bf16 v[160:163], v[48:51], v[152:155], v[160:163]\n"
    "v_mfma_f32_16x16x32_bf16 v[164:167], v[52:55], v[152:155], v[164:167]\n"
    "v_mfma_f32_16x16x32_bf16 v[168:171], v[56:59], v[152:155], v[168:171]\n"
    "v_mfma_f32_16x16x32_bf16 v[172:175], v[60:63], v[152:155], v[172:175]\n"
    "v_mfma_f32_16x16x32_bf16 v[160:163], v[64:67], v[156:159], v[160:163]\n"
    "v_mfma_f32_16x16x32_bf16 v[164:167], v[68:71], v[156:159], v[164:167]\n"
    "v_mfma_f32_16x16x32_bf16 v[168:171], v[72:75], v[156:159], v[168:171]\n"
    "v_mfma_f32_16x16x32_bf16 v[172:175], v[76:79], v[156:159], v[172:175]\n"
    "s_setprio 2\n"
    "s_bitcmp1_b32 s25, s83\n"
    "s_cbranch_scc1 YOWN%=\n"
    "YBF%=:\n"
    "s_nop 2\n"
    "v_pk_fma_f32 v[160:161], v[160:161], v[176:177], v[208:209] op_sel_hi:[1,0,1]\n"
    "v_pk_fma_f32 v[162:163], v[162:163], v[176:177], v[210:211] op_sel_hi:[1,0,1]\n"
    "v_cvt_pk_bf16_f32 v160, v160, v161\n"
    "v_cvt_pk_bf16_f32 v161, v162, v163\n"
    "ds_write_b64 v228, v[160:161]\n"
    "v_pk_fma_f32 v[164:165], v[164:165], v[176:177], v[212:213] op_sel_hi:[1,0,1]\n"
    "v_pk_fma_f32 v[166:167], v[166:167], v[176:177], v[214:215] op_sel_hi:[1,0,1]\n"
    "v_cvt_pk_bf16_f32 v164, v164, v165\n"
    "v_cvt_pk_bf16_f32 v165, v166, v167\n"
    "ds_write_b64 v229, v[164:165]\n"
    "v_pk_fma_f32 v[168:169], v[168:169], v[176:177], v[216:217] op_sel_hi:[1,0,1]\n"
    "v_pk_fma_f32 v[170:171], v[170:171], v[176:177], v[218:219] op_sel_hi:[1,0,1]\n"
    "v_cvt_pk_bf16_f32 v168, v168, v169\n"
    "v_cvt_pk_bf16_f32 v169, v170, v171\n"
    "ds_write_b64 v230, v[168:169]\n"
    "v_pk_fma_f32 v[172:173], v[172:173], v[176:177], v[220:221] op_sel_hi:[1,0,1]\n"
    "v_pk_fma_f32 v[174:175], v[174:175], v[176:177], v[222:223] op_sel_hi:[1,0,1]\n"
    "v_cvt_pk_bf16_f32 v172, v172, v173\n"
    "v_cvt_pk_bf16_f32 v173, v174, v175\n"
    "ds_write_b64 v231, v[172:173]\n"
    "ds_read_b128 v[0:3], v232\n"
    "ds_read_b128 v[4:7], v235\n"
    "s_movk_i32 s0, 20480\n"
    "s_waitcnt lgkmcnt(1)\n"
    "buffer_store_dwordx4 v[0:3], v233, s[56:59], 0 offen nt\n"
    "s_waitcnt lgkmcnt(0)\n"
    "buffer_store_dwordx4 v[4:7], v233, s[56:59], s0 offen nt\n"
    "s_endpgm\n"
    "FIX2h%=:\n"
    "s_mov_b32 s3, 262144\n"
    "s_nop 7\n"
    "FL2h%=:\n"
    "buffer_load_dwordx4 v[100:103], v204, s[48:51], s14 offen sc1\n"
    "buffer_load_dwordx4 v[104:107], v204, s[48:51], s14 offen offset:3840 sc1\n"
    "buffer_load_dwordx4 v[108:111], v204, s[48:51], s15 offen sc1\n"
    "buffer_load_dwordx4 v[112:115], v204, s[48:51], s15 offen offset:3840 sc1\n"
    "buffer_load_dwordx2 v[116:117], v205, s[48:51], s14 offen sc1\n"
    "buffer_load_dwordx2 v[118:119], v205, s[48:51], s16 offen sc1\n"
    "s_waitcnt vmcnt(0)\n"
    "v_max3_u32 v180, v103, v107, v111\n"
    "v_max3_u32 v180, v180, v115, v117\n"
    "v_max3_u32 v180, v180, v119, v119\n"
    "v_cmp_eq_u32 vcc, -1, v180\n"
    "s_cbranch_vccz OK2h%=\n"
    "s_sub_u32 s3, s3, 1\n"
    "s_cbranch_scc0 FL2h%=\n"
    "s_branch OK2h%=\n"
    "FIX3ha%=:\n"
    "s_mov_b32 s3, 262144\n"
    "s_nop 7\n"
    "FL3ha%=:\n"
    "buffer_load_dwordx4 v[140:143], v204, s[48:51], s17 offen sc1\n"
    "buffer_load_dwordx4 v[144:147], v204, s[48:51], s17 offen offset:3840 sc1\n"
    "buffer_load_dwordx4 v[148:151], v204, s[48:51], s18 offen sc1\n"
    "buffer_load_dwordx4 v[152:155], v204, s[48:51], s18 offen offset:3840 sc1\n"
    "buffer_load_dwordx2 v[156:157], v205, s[48:51], s17 offen sc1\n"
    "buffer_load_dwordx2 v[158:159], v205, s[48:51], s19 offen sc1\n"
    "s_waitcnt vmcnt(0)\n"
    "v_max3_u32 v180, v143, v147, v151\n"
    "v_max3_u32 v180, v180, v155, v157\n"
    "v_max3_u32 v180, v180, v159, v159\n"
    "v_cmp_eq_u32 vcc, -1, v180\n"
    "s_cbranch_vccz OK3ha%=\n"
    "s_sub_u32 s3, s3, 1\n"
    "s_cbranch_scc0 FL3ha%=\n"
    "s_branch OK3ha%=\n"
    "FIX3hb%=:\n"
    "s_mov_b32 s3, 262144\n"
    "s_nop 7\n"
    "FL3hb%=:\n"
    "buffer_load_dwordx4 v[140:143], v204, s[48:51], s17 offen sc1\n"
    "buffer_load_dwordx4 v[144:147], v204, s[48:51], s17 offen offset:3840 sc1\n"
    "buffer_load_dwordx4 v[148:151], v204, s[48:51], s18 offen sc1\n"
    "buffer_load_dwordx4 v[152:155], v204, s[48:51], s18 offen offset:3840 sc1\n"
    "buffer_load_dwordx2 v[156:157], v205, s[48:51], s17 offen sc1\n"
    "buffer_load_dwordx2 v[158:159], v205, s[48:51], s19 offen sc1\n"
    "s_waitcnt vmcnt(0)\n"
    "v_max3_u32 v180, v143, v147, v151\n"
    "v_max3_u32 v180, v180, v155, v157\n"
    "v_max3_u32 v180, v180, v159, v159\n"
    "v_cmp_eq_u32 vcc, -1, v180\n"
    "s_cbranch_vccz OK3hb%=\n"
    "s_sub_u32 s3, s3, 1\n"
    "s_cbranch_scc0 FL3hb%=\n"
    "s_branch OK3hb%=\n"
    "FIX0f%=:\n"
    "s_mov_b32 s3, 262144\n"
    "s_nop 7\n"
    "FL0f%=:\n"
    "buffer_load_dwordx4 v[80:83], v204, s[48:51], s8 offen sc1\n"
    "buffer_load_dwordx4 v[84:87], v204, s[48:51], s8 offen offset:3840 sc1\n"
    "buffer_load_dwordx4 v[88:91], v204, s[48:51], s9 offen sc1\n"
    "buffer_load_dwordx4 v[92:95], v204, s[48:51], s9 offen offset:3840 sc1\n"
    "buffer_load_dwordx2 v[96:97], v205, s[48:51], s8 offen sc1\n"
    "buffer_load_dwordx2 v[98:99], v205, s[48:51], s10 offen sc1\n"
    "s_waitcnt vmcnt(0)\n"
    "v_max3_u32 v180, v83, v87, v91\n"
    "v_max3_u32 v180, v180, v95, v97\n"
    "v_max3_u32 v180, v180, v99, v99\n"
    "v_cmp_eq_u32 vcc, -1, v180\n"
    "s_cbranch_vccz OK0f%=\n"
    "s_sub_u32 s3, s3, 1\n"
    "s_cbranch_scc0 FL0f%=\n"
    "s_branch OK0f%=\n"
    "FIX1f%=:\n"
    "s_mov_b32 s3, 262144\n"
    "s_nop 7\n"
    "FL1f%=:\n"
    "buffer_load_dwordx4 v[120:123], v204, s[48:51], s11 offen sc1\n"
    "buffer_load_dwordx4 v[124:127], v204, s[48:51], s11 offen offset:3840 sc1\n"
    "buffer_load_dwordx4 v[128:131], v204, s[48:51], s12 offen sc1\n"
    "buffer_load_dwordx4 v[132:135], v204, s[48:51], s12 offen offset:3840 sc1\n"
    "buffer_load_dwordx2 v[136:137], v205, s[48:51], s11 offen sc1\n"
    "buffer_load_dwordx2 v[138:139], v205, s[48:51], s13 offen sc1\n"
    "s_waitcnt vmcnt(0)\n"
    "v_max3_u32 v180, v123, v127, v131\n"
    "v_max3_u32 v180, v180, v135, v137\n"
    "v_max3_u32 v180, v180, v139, v139\n"
    "v_cmp_eq_u32 vcc, -1, v180\n"
    "s_cbranch_vccz OK1f%=\n"
    "s_sub_u32 s3, s3, 1\n"
    "s_cbranch_scc0 FL1f%=\n"
    "s_branch OK1f%=\n"
    "FIX2f%=:\n"
    "s_mov_b32 s3, 262144\n"
    "s_nop 7\n"
    "FL2f%=:\n"
    "buffer_load_dwordx4 v[100:103], v204, s[48:51], s14 offen sc1\n"
    "buffer_load_dwordx4 v[104:107], v204, s[48:51], s14 offen offset:3840 sc1\n"
    "buffer_load_dwordx4 v[108:111], v204, s[48:51], s15 offen sc1\n"
    "buffer_load_dwordx4 v[112:115], v204, s[48:51], s15 offen offset:3840 sc1\n"
    "buffer_load_dwordx2 v[116:117], v205, s[48:51], s14 offen sc1\n"
    "buffer_load_dwordx2 v[118:119], v205, s[48:51], s16 offen sc1\n"
    "s_waitcnt vmcnt(0)\n"
    "v_max3_u32 v180, v103, v107, v111\n"
    "v_max3_u32 v180, v180, v115, v117\n"
    "v_max3_u32 v180, v180, v119, v119\n"
    "v_cmp_eq_u32 vcc, -1, v180\n"
    "s_cbranch_vccz OK2f%=\n"
    "s_sub_u32 s3, s3, 1\n"
    "s_cbranch_scc0 FL2f%=\n"
    "s_branch OK2f%=\n"
    "FIX3fa%=:\n"
    "s_mov_b32 s3, 262144\n"
    "s_nop 7\n"
    "FL3fa%=:\n"
    "buffer_load_dwordx4 v[140:143], v204, s[48:51], s17 offen sc1\n"
    "buffer_load_dwordx4 v[144:147], v204, s[48:51], s17 offen offset:3840 sc1\n"
    "buffer_load_dwordx4 v[148:151], v204, s[48:51], s18 offen sc1\n"
    "buffer_load_dwordx4 v[152:155], v204, s[48:51], s18 offen offset:3840 sc1\n"
    "buffer_load_dwordx2 v[156:157], v205, s[48:51], s17 offen sc1\n"
    "buffer_load_dwordx2 v[158:159], v205, s[48:51], s19 offen sc1\n"
    "s_waitcnt vmcnt(0)\n"
    "v_max3_u32 v180, v143, v147, v151\n"
    "v_max3_u32 v180, v180, v155, v157\n"
    "v_max3_u32 v180, v180, v159, v159\n"
    "v_cmp_eq_u32 vcc, -1, v180\n"
    "s_cbranch_vccz OK3fa%=\n"
    "s_sub_u32 s3, s3, 1\n"
    "s_cbranch_scc0 FL3fa%=\n"
    "s_branch OK3fa%=\n"
    "FIX3fb%=:\n"
    "s_mov_b32 s3, 262144\n"
    "s_nop 7\n"
    "FL3fb%=:\n"
    "buffer_load_dwordx4 v[140:143], v204, s[48:51], s17 offen sc1\n"
    "buffer_load_dwordx4 v[144:147], v204, s[48:51], s17 offen offset:3840 sc1\n"
    "buffer_load_dwordx4 v[148:151], v204, s[48:51], s18 offen sc1\n"
    "buffer_load_dwordx4 v[152:155], v204, s[48:51], s18 offen offset:3840 sc1\n"
    "buffer_load_dwordx2 v[156:157], v205, s[48:51], s17 offen sc1\n"
    "buffer_load_dwordx2 v[158:159], v205, s[48:51], s19 offen sc1\n"
    "s_waitcnt vmcnt(0)\n"
    "v_max3_u32 v180, v143, v147, v151\n"
    "v_max3_u32 v180, v180, v155, v157\n"
    "v_max3_u32 v180, v180, v159, v159\n"
    "v_cmp_eq_u32 vcc, -1, v180\n"
    "s_cbranch_vccz OK3fb%=\n"
    "s_sub_u32 s3, s3, 1\n"
    "s_cbranch_scc0 FL3fb%=\n"
    "s_branch OK3fb%=\n"
    "YJOB%=:\n"
    "v_mbcnt_lo_u32_b32 v114, -1, 0\n"
    "v_mbcnt_hi_u32_b32 v114, -1, v114\n"
    "v_lshlrev_b32 v114, 4, v114\n"
    "v_and_b32 v117, 496, v114\n"
    "s_and_b32 s28, s22, 65535\n"
    "s_lshr_b32 s24, s22, 16\n"
    "s_cmp_lt_u32 s28, 96\n"
    "s_cselect_b32 s27, 0, 1\n"
    "s_cselect_b32 s28, s28, s24\n"
    "s_cselect_b32 s24, s24, -1\n"
    "s_lshr_b32 s7, s78, 6\n"
    "s_add_u32 s7, s7, s27\n"
    "s_mul_i32 s7, s7, 13824\n"
    "buffer_load_dwordx4 v[0:3], v114, s[64:67], s7 offen sc1\n"
    "buffer_load_dwordx4 v[4:7], v114, s[64:67], s7 offen offset:1024 sc1\n"
    "buffer_load_dwordx4 v[8:11], v114, s[64:67], s7 offen offset:2048 sc1\n"
    "buffer_load_dwordx4 v[12:15], v114, s[64:67], s7 offen offset:3072 sc1\n"
    "s_add_u32 s7, s7, 4096\n"
    "buffer_load_dwordx4 v[16:19], v114, s[64:67], s7 offen sc1\n"
    "buffer_load_dwordx4 v[20:23], v114, s[64:67], s7 offen offset:1024 sc1\n"
    "buffer_load_dwordx4 v[24:27], v114, s[64:67], s7 offen offset:2048 sc1\n"
    "buffer_load_dwordx4 v[28:31], v114, s[64:67], s7 offen offset:3072 sc1\n"
    "s_add_u32 s7, s7, 4096\n"
    "buffer_load_dwordx4 v[32:35], v114, s[64:67], s7 offen sc1\n"
    "buffer_load_dwordx4 v[36:39], v114, s[64:67], s7 offen offset:1024 sc1\n"
    "buffer_load_dwordx4 v[40:43], v114, s[64:67], s7 offen offset:2048 sc1\n"
    "buffer_load_dwordx4 v[44:47], v114, s[64:67], s7 offen offset:3072 sc1\n"
    "s_add_u32 s7, s7, 4096\n"
    "buffer_load_dwordx4 v[48:51], v114, s[64:67], s7 offen sc1\n"
    "buffer_load_dwordx4 v[52:55], v117, s[64:67], s7 offen offset:1024 sc1\n"
    "v_and_b32 v115, 96, v114\n"
    "v_bfe_u32 v116, v114, 7, 2\n"
    "v_lshl_add_u32 v115, v116, 3, v115\n"
    "v_bfe_u32 v116, v114, 4, 1\n"
    "v_lshl_add_u32 v115, v116, 2, v115\n"
    "v_cmp_eq_u32 vcc, v114, v117\n"
    "s_nop 1\n"
    "v_cndmask_b32 v112, 0, 1.0, vcc\n"
    "s_lshl_b32 s7, s28, 3\n"
    "s_and_b32 s7, s7, 112\n"
    "v_xor_b32 v113, s7, v115\n"
    "s_lshl_b32 s7, s28, 7\n"
    "s_add_u32 s7, s7, 133120\n"
    "v_add_u32 v113, s7, v113\n"
    "s_lshl_b32 s7, s27, 3\n"
    "s_add_u32 s7, s7, 156560\n"
    "v_mov_b32 v116, s7\n"
    "ds_read_b32 v116, v116\n"
    "s_mov_b32 s35, 262144\n"
    "s_waitcnt vmcnt(17)\n"
    "v_max3_u32 v180, v143, v147, v151\n"
    "v_cmp_eq_u32 vcc, -1, v180\n"
    "s_cbranch_vccnz FIX3ya%=\n"
    "OK3ya%=:\n"
    "s_setprio 0\n"
    "v_mfma_f32_16x16x32_bf16 v[160:163], v[120:123], v[140:143], v[160:163]\n"
    "v_mfma_f32_16x16x32_bf16 v[164:167], v[124:127], v[140:143], v[164:167]\n"
    "v_mfma_f32_16x16x32_bf16 v[168:171], v[128:131], v[140:143], v[168:171]\n"
    "v_mfma_f32_16x16x32_bf16 v[172:175], v[132:135], v[140:143], v[172:175]\n"
    "v_mfma_f32_16x16x32_bf16 v[160:163], v[136:139], v[144:147], v[160:163]\n"
    "v_mfma_f32_16x16x32_bf16 v[164:167], v[184:187], v[144:147], v[164:167]\n"
    "v_mfma_f32_16x16x32_bf16 v[168:171], v[192:195], v[144:147], v[168:171]\n"
    "v_mfma_f32_16x16x32_bf16 v[172:175], v[196:199], v[144:147], v[172:175]\n"
    "v_mfma_f32_16x16x32_bf16 v[160:163], v[200:203], v[148:151], v[160:163]\n"
    "s_waitcnt vmcnt(14)\n"
    "v_mfma_f32_16x16x32_bf16 v[164:167], v[220:223], v[148:151], v[164:167]\n"
    "v_max3_u32 v180, v155, v157, v159\n"
    "v_mfma_f32_16x16x32_bf16 v[168:171], v[236:239], v[148:151], v[168:171]\n"
    "v_cmp_eq_u32 vcc, -1, v180\n"
    "v_mfma_f32_16x16x32_bf16 v[172:175], v[240:243], v[148:151], v[172:175]\n"
    "s_cbranch_vccnz FIX3yb%=\n"
    "OK3yb%=:\n"
    "v_mfma_f32_16x16x32_bf16 v[160:163], v[244:247], v[152:155], v[160:163]\n"
    "v_mfma_f32_16x16x32_bf16 v[164:167], v[248:251], v[152:155], v[164:167]\n"
    "v_mfma_f32_16x16x32_bf16 v[168:171], v[252:255], v[152:155], v[168:171]\n"
    "v_mfma_f32_16x16x32_bf16 v[172:175], v[80:83], v[152:155], v[172:175]\n"
    "v_mfma_f32_16x16x32_bf16 v[160:163], v[84:87], v[156:159], v[160:163]\n"
    "v_mfma_f32_16x16x32_bf16 v[164:167], v[88:91], v[156:159], v[164:167]\n"
    "v_mfma_f32_16x16x32_bf16 v[168:171], v[92:95], v[156:159], v[168:171]\n"
    "v_mfma_f32_16x16x32_bf16 v[172:175], v[96:99], v[156:159], v[172:175]\n"
    "s_setprio 2\n"
    "s_branch YB1%=\n"
    "FIX3ya%=:\n"
    "s_mov_b32 s3, 262144\n"
    "s_nop 7\n"
    "FL3ya%=:\n"
    "buffer_load_dwordx4 v[140:143], v204, s[48:51], s17 offen sc1\n"
    "buffer_load_dwordx4 v[144:147], v204, s[48:51], s17 offen offset:3840 sc1\n"
    "buffer_load_dwordx4 v[148:151], v204, s[48:51], s18 offen sc1\n"
    "buffer_load_dwordx4 v[152:155], v204, s[48:51], s18 offen offset:3840 sc1\n"
    "buffer_load_dwordx2 v[156:157], v205, s[48:51], s17 offen sc1\n"
    "buffer_load_dwordx2 v[158:159], v205, s[48:51], s19 offen sc1\n"
    "s_waitcnt vmcnt(0)\n"
    "v_max3_u32 v180, v143, v147, v151\n"
    "v_max3_u32 v180, v180, v155, v157\n"
    "v_max3_u32 v180, v180, v159, v159\n"
    "v_cmp_eq_u32 vcc, -1, v180\n"
    "s_cbranch_vccz OK3ya%=\n"
    "s_sub_u32 s3, s3, 1\n"
    "s_cbranch_scc0 FL3ya%=\n"
    "s_branch OK3ya%=\n"
    "FIX3yb%=:\n"
    "s_mov_b32 s3, 262144\n"
    "s_nop 7\n"
    "FL3yb%=:\n"
    "buffer_load_dwordx4 v[140:143], v204, s[48:51], s17 offen sc1\n"
    "buffer_load_dwordx4 v[144:147], v204, s[48:51], s17 offen offset:3840 sc1\n"
    "buffer_load_dwordx4 v[148:151], v204, s[48:51], s18 offen sc1\n"
    "buffer_load_dwordx4 v[152:155], v204, s[48:51], s18 offen offset:3840 sc1\n"
    "buffer_load_dwordx2 v[156:157], v205, s[48:51], s17 offen sc1\n"
    "buffer_load_dwordx2 v[158:159], v205, s[48:51], s19 offen sc1\n"
    "s_waitcnt vmcnt(0)\n"
    "v_max3_u32 v180, v143, v147, v151\n"
    "v_max3_u32 v180, v180, v155, v157\n"
    "v_max3_u32 v180, v180, v159, v159\n"
    "v_cmp_eq_u32 vcc, -1, v180\n"
    "s_cbranch_vccz OK3yb%=\n"
    "s_sub_u32 s3, s3, 1\n"
    "s_cbranch_scc0 FL3yb%=\n"
    "s_branch OK3yb%=\n"
    "YS%=:\n"
    "s_waitcnt vmcnt(0)\n"
    "ds_read_b32 v107, v113\n"
    "v_pk_mul_f32 v[104:105], v[52:53], v[112:113] op_sel_hi:[1,0]\n"
    "v_xor_b32 v106, s68, v54\n"
    "v_pk_add_f32 v[104:105], v[104:105], v[0:1]\n"
    "v_bitop3_b32 v106, v106, v2, s68 bitop3:0xf6\n"
    "v_pk_add_f32 v[104:105], v[104:105], v[4:5]\n"
    "v_bitop3_b32 v106, v106, v6, s68 bitop3:0xf6\n"
    "v_pk_add_f32 v[104:105], v[104:105], v[8:9]\n"
    "v_bitop3_b32 v106, v106, v10, s68 bitop3:0xf6\n"
    "v_pk_add_f32 v[104:105], v[104:105], v[12:13]\n"
    "v_bitop3_b32 v106, v106, v14, s68 bitop3:0xf6\n"
    "v_pk_add_f32 v[104:105], v[104:105], v[16:17]\n"
    "v_bitop3_b32 v106, v106, v18, s68 bitop3:0xf6\n"
    "v_pk_add_f32 v[104:105], v[104:105], v[20:21]\n"
    "v_bitop3_b32 v106, v106, v22, s68 bitop3:0xf6\n"
    "v_pk_add_f32 v[104:105], v[104:105], v[24:25]\n"
    "v_bitop3_b32 v106, v106, v26, s68 bitop3:0xf6\n"
    "v_pk_add_f32 v[104:105], v[104:105], v[28:29]\n"
    "v_bitop3_b32 v106, v106, v30, s68 bitop3:0xf6\n"
    "v_pk_add_f32 v[104:105], v[104:105], v[32:33]\n"
    "v_bitop3_b32 v106, v106, v34, s68 bitop3:0xf6\n"
    "v_pk_add_f32 v[104:105], v[104:105], v[36:37]\n"
    "v_bitop3_b32 v106, v106, v38, s68 bitop3:0xf6\n"
    "v_pk_add_f32 v[104:105], v[104:105], v[40:41]\n"
    "v_bitop3_b32 v106, v106, v42, s68 bitop3:0xf6\n"
    "v_pk_add_f32 v[104:105], v[104:105], v[44:45]\n"
    "v_bitop3_b32 v106, v106, v46, s68 bitop3:0xf6\n"
    "v_pk_add_f32 v[104:105], v[104:105], v[48:49]\n"
    "v_bitop3_b32 v106, v106, v50, s68 bitop3:0xf6\n"
    "s_waitcnt lgkmcnt(0)\n"
    "v_cmp_ne_u32 vcc, 0, v106\n"
    "s_cbranch_vccz YK%=\n"
    "s_sleep 4\n"
    "s_sub_u32 s35, s35, 1\n"
    "s_cbranch_scc0 YL%=\n"
    "YK%=:\n"
    "v_mov_b32 v108, v104\n"
    "v_mov_b32 v109, v105\n"
    "s_nop 0\n"
    "v_permlane32_swap_b32 v104, v108\n"
    "v_permlane32_swap_b32 v105, v109\n"
    "v_pk_add_f32 v[104:105], v[104:105], v[108:109]\n"
    "v_cvt_pk_bf16_f32 v104, v104, v105\n"
    "v_and_b32 v105, 0xffff0000, v104\n"
    "v_lshlrev_b32 v104, 16, v104\n"
    "v_pk_mul_f32 v[104:105], v[104:105], v[116:117] op_sel_hi:[1,0]\n"
    "v_cvt_pk_bf16_f32 v104, v104, v105\n"
    "v_and_b32 v105, 0xffff0000, v104\n"
    "v_lshlrev_b32 v104, 16, v104\n"
    "v_lshlrev_b32 v106, 16, v107\n"
    "v_and_b32 v107, 0xffff0000, v107\n"
    "v_pk_add_f32 v[104:105], v[104:105], v[106:107]\n"
    "v_cvt_pk_bf16_f32 v104, v104, v105\n"
    "ds_write_b32 v113, v104\n"
    "s_cmp_ge_u32 s24, 96\n"
    "s_cbranch_scc1 YD%=\n"
    "s_mov_b32 s27, 1\n"
    "s_mov_b32 s28, s24\n"
    "s_mov_b32 s24, -1\n"
    "s_lshl_b32 s7, s28, 3\n"
    "s_and_b32 s7, s7, 112\n"
    "v_xor_b32 v113, s7, v115\n"
    "s_lshl_b32 s7, s28, 7\n"
    "s_add_u32 s7, s7, 133120\n"
    "v_add_u32 v113, s7, v113\n"
    "s_lshl_b32 s7, s27, 3\n"
    "s_add_u32 s7, s7, 156560\n"
    "v_mov_b32 v116, s7\n"
    "ds_read_b32 v116, v116\n"
    "YL%=:\n"
    "s_lshr_b32 s7, s78, 6\n"
    "s_add_u32 s7, s7, s27\n"
    "s_mul_i32 s7, s7, 13824\n"
    "buffer_load_dwordx4 v[0:3], v114, s[64:67], s7 offen sc1\n"
    "buffer_load_dwordx4 v[4:7], v114, s[64:67], s7 offen offset:1024 sc1\n"
    "buffer_load_dwordx4 v[8:11], v114, s[64:67], s7 offen offset:2048 sc1\n"
    "buffer_load_dwordx4 v[12:15], v114, s[64:67], s7 offen offset:3072 sc1\n"
    "s_add_u32 s7, s7, 4096\n"
    "buffer_load_dwordx4 v[16:19], v114, s[64:67], s7 offen sc1\n"
    "buffer_load_dwordx4 v[20:23], v114, s[64:67], s7 offen offset:1024 sc1\n"
    "buffer_load_dwordx4 v[24:27], v114, s[64:67], s7 offen offset:2048 sc1\n"
    "buffer_load_dwordx4 v[28:31], v114, s[64:67], s7 offen offset:3072 sc1\n"
    "s_add_u32 s7, s7, 4096\n"
    "buffer_load_dwordx4 v[32:35], v114, s[64:67], s7 offen sc1\n"
    "buffer_load_dwordx4 v[36:39], v114, s[64:67], s7 offen offset:1024 sc1\n"
    "buffer_load_dwordx4 v[40:43], v114, s[64:67], s7 offen offset:2048 sc1\n"
    "buffer_load_dwordx4 v[44:47], v114, s[64:67], s7 offen offset:3072 sc1\n"
    "s_add_u32 s7, s7, 4096\n"
    "buffer_load_dwordx4 v[48:51], v114, s[64:67], s7 offen sc1\n"
    "buffer_load_dwordx4 v[52:55], v117, s[64:67], s7 offen offset:1024 sc1\n"
    "s_branch YS%=\n"
    "YD%=:\n"
    "v_mov_b32 v104, 156696\n"
    "ds_write_b32 v104, v104\n"
    "s_branch YB2%=\n"
    "YOWN%=:\n"
    "s_mov_b32 s3, 16777216\n"
    "v_mov_b32 v181, 156696\n"
    "YWL%=:\n"
    "ds_read_b32 v180, v181\n"
    "s_waitcnt lgkmcnt(0)\n"
    "v_cmp_ne_u32 vcc, 0, v180\n"
    "s_cbranch_vccnz YWLD%=\n"
    "s_sleep 1\n"
    "s_sub_u32 s3, s3, 1\n"
    "s_cbranch_scc0 YWL%=\n"
    "YWLD%=:\n"
    "s_cmp_ge_u32 s69, 4\n"
    "s_cbranch_scc1 YC2%=\n"
    "ds_read_b32 v176, v234\n"
    "ds_read_b64 v[104:105], v228\n"
    "ds_read_b64 v[106:107], v229\n"
    "ds_read_b64 v[108:109], v230\n"
    "ds_read_b64 v[110:111], v231\n"
    "s_waitcnt lgkmcnt(4)\n"
    "v_sub_f32 v178, 1.0, v176\n"
    "s_waitcnt lgkmcnt(3)\n"
    "v_lshlrev_b32 v208, 16, v104\n"
    "v_and_b32 v209, 0xffff0000, v104\n"
    "v_pk_mul_f32 v[208:209], v[208:209], v[178:179] op_sel_hi:[1,0]\n"
    "v_lshlrev_b32 v210, 16, v105\n"
    "v_and_b32 v211, 0xffff0000, v105\n"
    "v_pk_mul_f32 v[210:211], v[210:211], v[178:179] op_sel_hi:[1,0]\n"
    "s_waitcnt lgkmcnt(2)\n"
    "v_lshlrev_b32 v212, 16, v106\n"
    "v_and_b32 v213, 0xffff0000, v106\n"
    "v_pk_mul_f32 v[212:213], v[212:213], v[178:179] op_sel_hi:[1,0]\n"
    "v_lshlrev_b32 v214, 16, v107\n"
    "v_and_b32 v215, 0xffff0000, v107\n"
    "v_pk_mul_f32 v[214:215], v[214:215], v[178:179] op_sel_hi:[1,0]\n"
    "s_waitcnt lgkmcnt(1)\n"
    "v_lshlrev_b32 v216, 16, v108\n"
    "v_and_b32 v217, 0xffff0000, v108\n"
    "v_pk_mul_f32 v[216:217], v[216:217], v[178:179] op_sel_hi:[1,0]\n"
    "v_lshlrev_b32 v218, 16, v109\n"
    "v_and_b32 v219, 0xffff0000, v109\n"
    "v_pk_mul_f32 v[218:219], v[218:219], v[178:179] op_sel_hi:[1,0]\n"
    "s_waitcnt lgkmcnt(0)\n"
    "v_lshlrev_b32 v220, 16, v110\n"
    "v_and_b32 v221, 0xffff0000, v110\n"
    "v_pk_mul_f32 v[220:221], v[220:221], v[178:179] op_sel_hi:[1,0]\n"
    "v_lshlrev_b32 v222, 16, v111\n"
    "v_and_b32 v223, 0xffff0000, v111\n"
    "v_pk_mul_f32 v[222:223], v[222:223], v[178:179] op_sel_hi:[1,0]\n"
    "s_branch YBF%=\n"
    "YC2%=:\n"
    "ds_read_b32 v176, v234\n"
    "ds_read_b64 v[104:105], v228\n"
    "ds_read_b64 v[106:107], v229\n"
    "s_waitcnt lgkmcnt(2)\n"
    "v_sub_f32 v178, 1.0, v176\n"
    "s_waitcnt lgkmcnt(1)\n"
    "v_lshlrev_b32 v208, 16, v104\n"
    "v_and_b32 v209, 0xffff0000, v104\n"
    "v_pk_mul_f32 v[208:209], v[208:209], v[178:179] op_sel_hi:[1,0]\n"
    "v_lshlrev_b32 v210, 16, v105\n"
    "v_and_b32 v211, 0xffff0000, v105\n"
    "v_pk_mul_f32 v[210:211], v[210:211], v[178:179] op_sel_hi:[1,0]\n"
    "s_waitcnt lgkmcnt(0)\n"
    "v_lshlrev_b32 v212, 16, v106\n"
    "v_and_b32 v213, 0xffff0000, v106\n"
    "v_pk_mul_f32 v[212:213], v[212:213], v[178:179] op_sel_hi:[1,0]\n"
    "v_lshlrev_b32 v214, 16, v107\n"
    "v_and_b32 v215, 0xffff0000, v107\n"
    "v_pk_mul_f32 v[214:215], v[214:215], v[178:179] op_sel_hi:[1,0]\n"
    "s_branch YBH%=\n"
    ::"{v[192:207]}"(p0), "{v[208:223]}"(p1), "{v[224:239]}"(p2), "{v[240:255]}"(p3), "{v[176:191]}"(r0), "{s[68:83]}"(uc), "{s84}"(C_H + gb * 128), "{s85}"(b), "{s[90:91]}"(kp0)
    : "memory", "s99");
  __builtin_unreachable();
}

extern "C" void kernel_launch(void* const* d_in, const int* in_sizes, int n_in, void* d_out, int out_size, void* d_ws,
                              size_t ws_size, hipStream_t stream) {
  kf<<<256, 512, 0, stream>>>((const u16*)d_in[0], (const u16*)d_in[1], (const u16*)d_in[2], (const u16*)d_in[4],
                              (const u16*)d_in[8], (const u16*)d_in[5], (const u16*)d_in[6], (const u16*)d_in[7],
                              (char*)d_ws, (u16*)d_out);
}
```
